```python
import torch

def _softmax(x, axis=-1):
    return torch.softmax(x, dim=axis)
def _gelu(x):
    return torch.nn.functional.gelu(x, approximate="tanh")
def _silu(x):
    return torch.nn.functional.silu(x)
def _sigmoid(x):
    return torch.sigmoid(x)
def _layernorm(x, axis=-1, eps=1e-5):
    m = x.mean(dim=axis, keepdim=True)
    v = x.var(dim=axis, keepdim=True, unbiased=False)
    return (x - m) / torch.sqrt(v + eps)
def _scatter_add(tgt, idx, src, axis=0):
    return tgt.clone().index_add_(axis, idx, src)
def _segment_sum(src, seg, n):
    out = torch.zeros((n,) + src.shape[1:], dtype=src.dtype)
    return out.index_add_(0, seg, src)
def _pad(x, pw):
    flat = tuple(v for pair in reversed(pw) for v in pair)
    return torch.nn.functional.pad(x, flat)

kernel_name = 'sequential_preLN__sliding_block_cached__mixer_token_mlp__decode'

def setup_inputs():

    _w = (lambda a: (lambda t: t.to(torch.bfloat16) if t.is_floating_point() else t)(torch.as_tensor(a)))
    def _A_setup():
        B, Sq, D = 4, 1, 512
        Hq, Hkv, Sk, Dh = 32, 8, 8192, 128
        assert Hq % Hkv == 0, (Hq, Hkv)
        return {
            "x": _w((lambda *s: torch.randn(*s, dtype=torch.bfloat16) * 0.05)(B, Sq, D)),
            "k_cache": _w((lambda *s: torch.randn(*s, dtype=torch.bfloat16) * 3.2)(B, Hkv, Sk, Dh)),
            "v_cache": _w((lambda *s: torch.randn(*s, dtype=torch.bfloat16) * 0.8)(B, Hkv, Sk, Dh)),
            "wq": _w((lambda *s: torch.randn(*s, dtype=torch.bfloat16) * 3.2)(D, Hq * Dh) * (D ** -0.5)),
            "wo": _w((lambda *s: torch.randn(*s, dtype=torch.bfloat16) * 0.8)(Hq * Dh, D) * ((Hq * Dh) ** -0.5)),
        }
    _A_in = _A_setup()

    def _B_setup():
        B, S, D, F = 4, 1, 512, 1792
        return {
            "x": _w((lambda *s: torch.randn(*s, dtype=torch.bfloat16) * 0.05)(B, S, D)),
            "w1": _w((lambda *s: torch.randn(*s, dtype=torch.bfloat16) * 1.6)(S, F) * (S ** -0.5)),
            "b1": _w((lambda *s: torch.randn(*s, dtype=torch.bfloat16) * 1.6)(F) * 0.02),
            "w2": _w((lambda *s: torch.randn(*s, dtype=torch.bfloat16) * 1.6)(F, S) * (F ** -0.5)),
            "b2": _w((lambda *s: torch.randn(*s, dtype=torch.bfloat16) * 0.8)(S) * 0.02),
        }
    _B_in = _B_setup()

    return {
        "A_x": _A_in["x"],
        "A_k_cache": _A_in["k_cache"],
        "A_v_cache": _A_in["v_cache"],
        "A_wq": _A_in["wq"],
        "A_wo": _A_in["wo"],
        "B_x": _B_in["x"],
        "B_w1": _B_in["w1"],
        "B_b1": _B_in["b1"],
        "B_w2": _B_in["w2"],
        "B_b2": _B_in["b2"],
    }

def reference(A_x, A_k_cache, A_v_cache, A_wq, A_wo, B_x, B_w1, B_b1, B_w2, B_b2):
    def _A_ref(x, k_cache, v_cache, wq, wo):
        B, Sq, D = x.shape
        Hq, Dh = 32, 128
        Hkv, Sk = k_cache.shape[1], k_cache.shape[2]
        W = min(4096, max(256, Sk // 4))
        rep = Hq // Hkv
        q = (lambda x, *d: x.permute(*d))((x @ wq).reshape(B, Sq, Hq, Dh), 0, 2, 1, 3)
        k = (lambda x, n, axis: torch.repeat_interleave(x, n, dim=axis))(k_cache[:, :, Sk - W:, :], rep, axis=1)
        v = (lambda x, n, axis: torch.repeat_interleave(x, n, dim=axis))(v_cache[:, :, Sk - W:, :], rep, axis=1)
        scores = torch.einsum("bhqd,bhkd->bhqk", q, k) * (Dh ** -0.5)
        p = _softmax(scores, axis=-1)
        o = torch.einsum("bhqk,bhkd->bhqd", p, v)
        return (lambda x, *d: x.permute(*d))(o, 0, 2, 1, 3).reshape(B, Sq, Hq * Dh) @ wo

    def _B_ref(x, w1, b1, w2, b2):
        B, S, D = x.shape
        h = _layernorm(x, axis=-1)
        u = torch.einsum("bsd,sf->bdf", h, w1) + b1
        u = _gelu(u)
        y = torch.einsum("bdf,fs->bds", u, w2) + b2
        return (lambda x, *d: x.permute(*d))(y, 0, 2, 1)

    x = A_x
    h = x + _A_ref(_layernorm(x, axis=-1), A_k_cache, A_v_cache, A_wq, A_wo)
    h2 = h + _B_ref(_layernorm(h, axis=-1), B_w1, B_b1, B_w2, B_b2)
    return h2
```

```cpp
#include <hip/hip_runtime.h>
#include <hip/hip_bf16.h>
#include <stdint.h>

typedef unsigned short u16;
__device__ __forceinline__ float bf2f(u16 b) { return __builtin_bit_cast(float, (unsigned)b << 16); }
__device__ __forceinline__ float rbf(float f) {
    __bf16 h = (__bf16)f; return (float)h;
}
__device__ __forceinline__ u16 f2bf(float f) { __bf16 h = (__bf16)f; return __builtin_bit_cast(u16, h); }

constexpr int B = 4, D = 512, HQ = 32, HKV = 8, SK = 8192, DH = 128, W = 2048, F = 1792;
constexpr int NSPLIT = 8;

__device__ float block_sum(float v, float* red) {
    for (int o = 32; o > 0; o >>= 1) v += __shfl_xor(v, o);
    __syncthreads();
    if ((threadIdx.x & 63) == 0) red[threadIdx.x >> 6] = v;
    __syncthreads();
    float s = 0; for (int i = 0; i < (int)(blockDim.x >> 6); ++i) s += red[i];
    return s;
}

__device__ float ln512(float x, float* red) {
    float mean = block_sum(x, red) * (1.f / D);
    float d = x - mean;
    float var = block_sum(d * d, red) * (1.f / D);
    float mb = rbf(mean), vb = rbf(var);
    float den = rbf(sqrtf(rbf(vb + 1e-5f)));
    return rbf(rbf(x - mb) / den);
}

__global__ void k_q(const u16* x, const u16* wq, float* q) {
    __shared__ float xn[B][D];
    __shared__ float red[16];
    __shared__ float part[8][B][64];
    int t = threadIdx.x;
    for (int b = 0; b < B; ++b) { float v = ln512(bf2f(x[b * D + t]), red); xn[b][t] = v; }
    __syncthreads();
    int col = blockIdx.x * 64 + (t & 63), w = t >> 6;
    float acc[B] = {0, 0, 0, 0};
    for (int k = w * 64; k < w * 64 + 64; ++k) {
        float wv = bf2f(wq[(size_t)k * (HQ * DH) + col]);
        for (int b = 0; b < B; ++b) acc[b] += xn[b][k] * wv;
    }
    for (int b = 0; b < B; ++b) part[w][b][t & 63] = acc[b];
    __syncthreads();
    if (t < 256) { int b = t >> 6, c = t & 63; float s = 0; for (int i = 0; i < 8; ++i) s += part[i][b][c];
        q[b * HQ * DH + blockIdx.x * 64 + c] = rbf(s); }
}

__global__ void k_attn(const float* q, const u16* kc, const u16* vc, float* po, float* pml) {
    __shared__ float qs[4][DH];
    __shared__ float p[4][256];
    __shared__ float red[16];
    int s = blockIdx.x, g = blockIdx.y, b = blockIdx.z, t = threadIdx.x;
    for (int i = t; i < 4 * DH; i += 256) qs[i / DH][i % DH] = q[b * HQ * DH + g * 512 + i];
    __syncthreads();
    size_t base = ((size_t)(b * HKV + g) * SK + (SK - W) + s * 256) * DH;
    const u16* kr = kc + base + (size_t)t * DH;
    float sc[4] = {0, 0, 0, 0};
    for (int d = 0; d < DH; ++d) { float kv = bf2f(kr[d]); for (int h = 0; h < 4; ++h) sc[h] += qs[h][d] * kv; }
    float m[4], l[4];
    for (int h = 0; h < 4; ++h) {
        float v = rbf(rbf(sc[h]) * 0.08838834764831845f);
        float mx = v; for (int o = 32; o > 0; o >>= 1) mx = fmaxf(mx, __shfl_xor(mx, o));
        __syncthreads(); if ((t & 63) == 0) red[t >> 6] = mx; __syncthreads();
        mx = fmaxf(fmaxf(red[0], red[1]), fmaxf(red[2], red[3]));
        float e = __expf(v - mx);
        p[h][t] = e; m[h] = mx; l[h] = block_sum(e, red);
    }
    __syncthreads();
    int d = t & 127, hh = t >> 7;
    float a0 = 0, a1 = 0;
    for (int k = 0; k < 256; ++k) { float vv = bf2f(vc[base + (size_t)k * DH + d]); a0 += p[hh * 2][k] * vv; a1 += p[hh * 2 + 1][k] * vv; }
    size_t slot = ((size_t)(b * HKV + g) * NSPLIT + s);
    po[slot * 512 + (hh * 2) * DH + d] = a0; po[slot * 512 + (hh * 2 + 1) * DH + d] = a1;
    if (t < 4) { pml[slot * 8 + t * 2] = m[t]; pml[slot * 8 + t * 2 + 1] = l[t]; }
}

__global__ void k_wo(const float* po, const float* pml, const u16* wo, float* pa) {
    __shared__ float o[512];
    __shared__ float part[8][64];
    int cs = blockIdx.x, g = blockIdx.y, b = blockIdx.z, t = threadIdx.x;
    int h = t >> 7;
    size_t slot0 = (size_t)(b * HKV + g) * NSPLIT;
    float M = -INFINITY; for (int s = 0; s < NSPLIT; ++s) M = fmaxf(M, pml[(slot0 + s) * 8 + h * 2]);
    float L = 0, a = 0;
    for (int s = 0; s < NSPLIT; ++s) { float w = __expf(pml[(slot0 + s) * 8 + h * 2] - M); L += w * pml[(slot0 + s) * 8 + h * 2 + 1]; a += w * po[(slot0 + s) * 512 + t]; }
    o[t] = rbf(a / L);
    __syncthreads();
    int c = t & 63, w = t >> 6; float acc = 0;
    for (int k = w * 64; k < w * 64 + 64; ++k) acc += o[k] * bf2f(wo[(size_t)(g * 512 + k) * D + cs * 64 + c]);
    part[w][c] = acc; __syncthreads();
    if (t < 64) { float s = 0; for (int i = 0; i < 8; ++i) s += part[i][t]; pa[(b * HKV + g) * D + cs * 64 + t] = s; }
}

__global__ void k_fin(const u16* x, const float* pa, const u16* w1, const u16* b1, const u16* w2, const u16* b2, u16* out) {
    __shared__ float red[16];
    __shared__ float hs[D], ts[D];
    int j = blockIdx.x, b = blockIdx.y, t = threadIdx.x;
    float a = 0; for (int g = 0; g < HKV; ++g) a += pa[(b * HKV + g) * D + t];
    float h = rbf(bf2f(x[b * D + t]) + rbf(a));
    float n1 = ln512(h, red); float n2 = ln512(n1, red);
    hs[t] = h; ts[t] = n2; __syncthreads();
    int w = t >> 6, lane = t & 63; int d = j * 8 + w; float tv = ts[d]; float acc = 0;
    for (int f = lane; f < F; f += 64) {
        float u = rbf(rbf(tv * bf2f(w1[f])) + bf2f(b1[f]));
        float z = 0.7978845608028654f * (u + 0.044715f * u * u * u);
        float ge = rbf(0.5f * u * (1.f + tanhf(z)));
        acc += ge * bf2f(w2[f]);
    }
    for (int o = 32; o > 0; o >>= 1) acc += __shfl_xor(acc, o);
    if (lane == 0) { float y = rbf(rbf(acc) + bf2f(b2[0])); out[b * D + d] = f2bf(hs[d] + y); }
}

extern "C" void kernel_launch(void* const* d_in, const int* in_sizes, int n_in, void* d_out, int out_size, void* d_ws, size_t ws_size, hipStream_t stream) {
    const u16* x = (const u16*)d_in[0]; const u16* kc = (const u16*)d_in[1]; const u16* vc = (const u16*)d_in[2];
    const u16* wq = (const u16*)d_in[3]; const u16* wo = (const u16*)d_in[4];
    const u16* w1 = (const u16*)d_in[6]; const u16* b1 = (const u16*)d_in[7]; const u16* w2 = (const u16*)d_in[8]; const u16* b2 = (const u16*)d_in[9];
    float* ws = (float*)d_ws;
    float* q = ws; float* po = ws + 16384; float* pml = po + 256 * 512; float* pa = pml + 256 * 8;
    k_q<<<64, 512, 0, stream>>>(x, wq, q);
    k_attn<<<dim3(NSPLIT, HKV, B), 256, 0, stream>>>(q, kc, vc, po, pml);
    k_wo<<<dim3(8, HKV, B), 512, 0, stream>>>(po, pml, wo, pa);
    k_fin<<<dim3(64, B), 512, 0, stream>>>(x, pa, w1, b1, w2, b2, (u16*)d_out);
}
```

```cpp
#include <hip/hip_runtime.h>
#include <stdint.h>

typedef unsigned u32;
typedef unsigned short u16;
typedef u32 v4u __attribute__((ext_vector_type(4)));
typedef u32 v2u __attribute__((ext_vector_type(2)));
typedef float v4f __attribute__((ext_vector_type(4)));
typedef float v2f __attribute__((ext_vector_type(2)));
typedef __bf16 v8bf __attribute__((ext_vector_type(8)));
typedef __bf16 v2bf __attribute__((ext_vector_type(2)));
typedef short v4s __attribute__((ext_vector_type(4)));
#define LAS __attribute__((address_space(3)))

#define STAMP(i) do { } while (0)

#define AUX_O 2

constexpr int AUX_NT = 2, AUX_SC1 = 16;
constexpr u32 WS_EPOCH = 0;
constexpr u32 WS_Q = 4096;
constexpr u32 WS_O = WS_Q + 32 * 2048;
constexpr u32 WS_ML = WS_O + 256 * 2048;
constexpr u32 WS_A = WS_ML + 256 * 64;
constexpr u32 WS_END = WS_A + 256 * 256;
constexpr int SPIN_MAX = 1 << 18;
constexpr u32 CODE_BYTES = 9216;
constexpr int OOB = 0x40000000;

__device__ __forceinline__ float u2f(u32 v) { return __builtin_bit_cast(float, v); }
__device__ __forceinline__ float bflo(u32 v) { return __builtin_bit_cast(float, v << 16); }
__device__ __forceinline__ float bfhi(u32 v) { return __builtin_bit_cast(float, v & 0xffff0000u); }
__device__ __forceinline__ u32 pkbf(float a, float b) { v2f f = {a, b}; return __builtin_bit_cast(u32, __builtin_convertvector(f, v2bf)); }
__device__ __forceinline__ float rbf(float a) { return bflo(pkbf(a, a)); }
template <int CTRL> __device__ __forceinline__ float dppf(float v) {
    return __builtin_bit_cast(float, __builtin_amdgcn_update_dpp(0, __builtin_bit_cast(int, v), CTRL, 0xf, 0xf, true));
}
template <int CTRL> __device__ __forceinline__ u32 dppu(u32 v) { return (u32)__builtin_amdgcn_update_dpp(0, (int)v, CTRL, 0xf, 0xf, true); }
constexpr int DPP_X1 = 0xB1, DPP_X2 = 0x4E, DPP_HMIR = 0x141, DPP_ROR4 = 0x124, DPP_ROR8 = 0x128, DPP_SHL4 = 0x104, DPP_SHL8 = 0x108;
__device__ __forceinline__ v2f swap32(float a, float b) {
    auto r = __builtin_amdgcn_permlane32_swap(__builtin_bit_cast(u32, a), __builtin_bit_cast(u32, b), false, false);
    v2f o = {__builtin_bit_cast(float, (u32)r[0]), __builtin_bit_cast(float, (u32)r[1])}; return o;
}
__device__ __forceinline__ v2f swap16(float a, float b) {
    auto r = __builtin_amdgcn_permlane16_swap(__builtin_bit_cast(u32, a), __builtin_bit_cast(u32, b), false, false);
    v2f o = {__builtin_bit_cast(float, (u32)r[0]), __builtin_bit_cast(float, (u32)r[1])}; return o;
}
__device__ __forceinline__ float sum_x16_x32(float v) { v2f a = swap16(v, v); v = a.x + a.y; a = swap32(v, v); return a.x + a.y; }
__device__ __forceinline__ float max_x16_x32(float v) { v2f a = swap16(v, v); v = fmaxf(a.x, a.y); a = swap32(v, v); return fmaxf(a.x, a.y); }
__device__ __forceinline__ float wave_sum(float v) {
    v += dppf<DPP_X1>(v); v += dppf<DPP_X2>(v); v += dppf<DPP_HMIR>(v); v += dppf<DPP_ROR8>(v);
    return sum_x16_x32(v);
}
__device__ __forceinline__ float treduce8(const float (&y)[8], bool bit3) {
    float z[4], u[2];
#pragma unroll
    for (int e = 0; e < 4; ++e) { v2f a = swap32(y[e], y[e + 4]); z[e] = a.x + a.y; }
#pragma unroll
    for (int e = 0; e < 2; ++e) { v2f a = swap16(z[e], z[e + 2]); u[e] = a.x + a.y; }
    float keep = bit3 ? u[1] : u[0], send = bit3 ? u[0] : u[1];
    return keep + dppf<DPP_ROR8>(send);
}
__device__ __forceinline__ v4f mfma16(v4u a, v4u b, v4f c) {
    return __builtin_amdgcn_mfma_f32_16x16x32_bf16(__builtin_bit_cast(v8bf, a), __builtin_bit_cast(v8bf, b), c, 0, 0, 0);
}
__device__ __forceinline__ void lds_barrier() { asm volatile("s_waitcnt lgkmcnt(0)\n\ts_barrier" ::: "memory"); }
#define SB() __builtin_amdgcn_sched_barrier(0)
__device__ __forceinline__ __amdgpu_buffer_rsrc_t mkrsrc(const void* p, u32 bytes) { return __builtin_amdgcn_make_buffer_rsrc((void*)p, 0, bytes, 0x00020000); }
__device__ __forceinline__ u32 perm_lo(u32 hi, u32 lo) { return __builtin_amdgcn_perm(hi, lo, 0x05040100u); }
__device__ __forceinline__ u32 perm_hi(u32 hi, u32 lo) { return __builtin_amdgcn_perm(hi, lo, 0x07060302u); }

struct __attribute__((aligned(16))) Smem {
    float opart[8 * 512];
    u32 mw[3][1024];
    u32 xrow[256];
    float qpart[8 * 64];
    u32 qimg[256];
    float stats[4][8][2];
    float mx[4][8];
    float ls[4][8];
    float apart[8 * 64];
    float hbuf[512];
    float pad[16 * 1024];
};

__global__ void __launch_bounds__(512, 2)
fused_decode_kernel(const u16* __restrict__ x, const u16* __restrict__ kc, const u16* __restrict__ vc,
                    const u16* __restrict__ wq, const u16* __restrict__ wo, const u16* __restrict__ w1,
                    const u16* __restrict__ b1, const u16* __restrict__ w2, const u16* __restrict__ b2,
                    u16* __restrict__ out, unsigned char* ws) {
    __shared__ Smem sm;
    STAMP(0);
    const int bid = blockIdx.x, tid = threadIdx.x, l = tid & 63;
    const int w = __builtin_amdgcn_readfirstlane(tid >> 6);
    const int s = bid & 7, r = bid >> 3, b = r >> 3, g = r & 7;
    const int n16 = l & 15, kg = l >> 4;

    const __amdgpu_buffer_rsrc_t r_ws = mkrsrc(ws, WS_END);
    const size_t kvbase = ((size_t)(b * 8 + g) * 8192 + 6144 + s * 256) * 128;
    const __amdgpu_buffer_rsrc_t r_k = mkrsrc(kc + kvbase, 65536);
    const __amdgpu_buffer_rsrc_t r_v = mkrsrc(vc + kvbase, 65536);
    const __amdgpu_buffer_rsrc_t r_x = mkrsrc(x, 4096);
    const __amdgpu_buffer_rsrc_t r_wq = mkrsrc(wq, 4u << 20);
    const __amdgpu_buffer_rsrc_t r_wo = mkrsrc(wo, 4u << 20);

    v4u xf[2], wqr[2], kr[8], vr[8], wr[8];
#pragma unroll
    for (int t = 0; t < 2; ++t) xf[t] = __builtin_amdgcn_raw_buffer_load_b128(r_x, (l & 3) * 1024 + kg * 16 + t * 64, w * 128, 0);
#pragma unroll
    for (int t = 0; t < 2; ++t) wqr[t] = __builtin_amdgcn_raw_buffer_load_b128(r_wq, (l >> 1) * 8192 + (l & 1) * 16, (w * 64 + t * 32) * 8192 + s * 1024 + r * 32, AUX_NT);
    const u32 tag = __builtin_amdgcn_raw_buffer_load_b32(r_ws, 0, WS_EPOCH, AUX_SC1) + 1u;
    u32 pre;
    {
        unsigned long long pc;
        asm volatile("s_getpc_b64 %0" : "=s"(pc));
        pre = __builtin_amdgcn_raw_buffer_load_b32(mkrsrc((const void*)(pc & ~127ull), CODE_BYTES), n16 * 128 + w * 2048, 0, 0);
    }
    const int koff = (n16 >> 2) * 256 + (4 * kg + (n16 & 3)) * 16;
#pragma unroll
    for (int i = 0; i < 3; ++i) kr[i] = __builtin_amdgcn_raw_buffer_load_b128(r_k, koff, (w * 8 + i) * 1024, AUX_NT);
    SB();

    float xv[16];
#pragma unroll
    for (int t = 0; t < 2; ++t)
#pragma unroll
        for (int q = 0; q < 4; ++q) { xv[t * 8 + 2 * q] = bflo(xf[t][q]); xv[t * 8 + 2 * q + 1] = bfhi(xf[t][q]); }
    {
        float sum = 0.f;
#pragma unroll
        for (int i = 0; i < 16; ++i) sum += xv[i];
        sum = sum_x16_x32(sum);
        const float mw = sum * (1.f / 64.f);
        float m2 = 0.f;
#pragma unroll
        for (int i = 0; i < 16; ++i) { float d = xv[i] - mw; m2 += d * d; }
        m2 = sum_x16_x32(m2);
        if (l < 4) { v2f st = {sum, m2}; *(v2f*)&sm.stats[l][w][0] = st; }
    }
    STAMP(1);
    lds_barrier();
    STAMP(12);
    v4u xa[2];
    {
        v4f st[4];
#pragma unroll
        for (int i = 0; i < 4; ++i) st[i] = *(const v4f*)&sm.stats[l & 3][2 * i][0];
        float sum = 0.f;
#pragma unroll
        for (int i = 0; i < 4; ++i) sum += st[i].x + st[i].z;
        const float mean = sum * (1.f / 512.f);
        float m2 = 0.f;
#pragma unroll
        for (int i = 0; i < 4; ++i) {
            float d0 = st[i].x * (1.f / 64.f) - mean, d1 = st[i].z * (1.f / 64.f) - mean;
            m2 += st[i].y + st[i].w + 64.f * (d0 * d0 + d1 * d1);
        }
        const float mb = rbf(mean), vb = rbf(m2 * (1.f / 512.f));
        const float rden = 1.0f / rbf(__builtin_sqrtf(rbf(vb + 1e-5f)));
#pragma unroll
        for (int t = 0; t < 2; ++t)
#pragma unroll
            for (int q = 0; q < 4; ++q) {
                u32 d = pkbf(xv[t * 8 + 2 * q] - mb, xv[t * 8 + 2 * q + 1] - mb);
                xa[t][q] = pkbf(bflo(d) * rden, bfhi(d) * rden);
            }
    }
    {
        unsigned char* img = (unsigned char*)sm.opart + w * 2048;
        STAMP(13);
        STAMP(14);
        *(v4u*)(img + l * 16) = wqr[0];
        *(v4u*)(img + 1024 + l * 16) = wqr[1];
        v4f acc = {0.f, 0.f, 0.f, 0.f};
#pragma unroll
        for (int t = 0; t < 2; ++t) {
            LAS v4s* p = (LAS v4s*)(img + t * 1024 + kg * 256 + n16 * 8);
            v2u lo = __builtin_bit_cast(v2u, __builtin_amdgcn_ds_read_tr16_b64_v4i16(p));
            v2u hi = __builtin_bit_cast(v2u, __builtin_amdgcn_ds_read_tr16_b64_v4i16(p + 16));
            v4u bf = {lo.x, lo.y, hi.x, hi.y};
            acc = mfma16(xa[t], bf, acc);
        }
        if (l < 16) *(v4f*)&sm.qpart[w * 64 + l * 4] = acc;
    }
    lds_barrier();
    STAMP(15);
    if (w == 0) {
        float v = 0.f;
#pragma unroll
        for (int i = 0; i < 8; ++i) v += sm.qpart[i * 64 + l];
        u32 p = pkbf(v, dppf<DPP_SHL4>(v));
        u32 p2 = dppu<DPP_SHL8>(p);
        if ((l & 12) == 0) {
            v4u gq = {p, tag, p2, tag};
            __builtin_amdgcn_raw_buffer_store_b128(gq, r_ws, (l & 3) * (8 * 2048) + kg * 16, WS_Q + s * 2048 + r * 64, AUX_SC1);
        }
    }
    STAMP(2);
    SB();

#pragma unroll
    for (int i = 3; i < 8; ++i) kr[i] = __builtin_amdgcn_raw_buffer_load_b128(r_k, koff, (w * 8 + i) * 1024, AUX_NT);
    v4u sa, sb2;
    {
        const __amdgpu_buffer_rsrc_t ra = mkrsrc(w < 4 ? w1 : b1, 3584);
        const __amdgpu_buffer_rsrc_t rb = mkrsrc(w < 4 ? w2 : x + b * 512, w < 4 ? 3584 : 1024);
        sa = __builtin_amdgcn_raw_buffer_load_b128(ra, l * 16 + (w & 3) * 1024, 0, 0);
        sb2 = __builtin_amdgcn_raw_buffer_load_b128(rb, l * 16 + (w & 3) * 1024, 0, 0);
    }
    const float b2v = bflo(b2[0]);
    const int qoff = WS_Q + (b * 8 + g) * 2048 + w * 256;
    v4u pv = __builtin_amdgcn_raw_buffer_load_b128(r_ws, n16 * 16, qoff, AUX_SC1);
#pragma unroll
    for (int i = 0; i < 3; ++i) vr[i] = __builtin_amdgcn_raw_buffer_load_b128(r_v, l * 16, (w * 8 + i) * 1024, AUX_NT);
    SB();
    *(v4u*)&sm.mw[w < 4 ? 0 : 1][(w & 3) * 256 + l * 4] = sa;
    if (w < 4) *(v4u*)&sm.mw[2][w * 256 + l * 4] = sb2;
    else if (w == 4) *(v4u*)&sm.xrow[l * 4] = sb2;
    if (!__all((pv.y == tag) & (pv.w == tag))) {
#pragma unroll 1
        for (int spin = 0; spin < SPIN_MAX; ++spin) {
            asm volatile("" ::: "memory");
            pv = __builtin_amdgcn_raw_buffer_load_b128(r_ws, n16 * 16, qoff, AUX_SC1);
            if (__all((pv.y == tag) & (pv.w == tag))) break;
        }
    }
    if (l < 16) { v2u qq = {pv.x, pv.z}; *(v2u*)&sm.qimg[w * 32 + l * 2] = qq; }
    STAMP(3);
    lds_barrier();

    float sc[8];
    {
        const v4u qf = *(const v4u*)&sm.qimg[(n16 & 3) * 64 + (4 * kg + (n16 >> 2)) * 4];
        const bool j0 = (l & 4) != 0, j1 = (l & 8) != 0;
        float m = -__builtin_inff();
#pragma unroll
        for (int i = 0; i < 8; ++i) {
            v4f c = {0.f, 0.f, 0.f, 0.f};
            c = mfma16(kr[i], qf, c);
            float dl = j0 ? c.y : c.x, dh = j0 ? c.w : c.z;
            float d = j1 ? dh : dl;
            d += dppf<DPP_ROR4>(d);
            d += dppf<DPP_ROR8>(d);
            d = rbf(rbf(d) * 0.08838834764831845f);
            sc[i] = d;
            m = fmaxf(m, d);
        }
        m = max_x16_x32(m);
        if (l < 4) sm.mx[l][w] = m;
        asm volatile("" :: "v"(pre));
    }
    SB();
#pragma unroll
    for (int i = 3; i < 5; ++i) vr[i] = __builtin_amdgcn_raw_buffer_load_b128(r_v, l * 16, (w * 8 + i) * 1024, AUX_NT);
    SB();
    lds_barrier();
    float M;
    v4u pa[4];
    {
        const v4f m0 = *(const v4f*)&sm.mx[l & 3][0], m1 = *(const v4f*)&sm.mx[l & 3][4];
        M = fmaxf(fmaxf(fmaxf(m0.x, m0.y), fmaxf(m0.z, m0.w)), fmaxf(fmaxf(m1.x, m1.y), fmaxf(m1.z, m1.w)));
        float lsum = 0.f;
#pragma unroll
        for (int i = 0; i < 8; ++i) { sc[i] = __builtin_amdgcn_exp2f((sc[i] - M) * 1.4426950408889634f); lsum += sc[i]; }
        lsum = sum_x16_x32(lsum);
        if (l < 4) sm.ls[l][w] = lsum;
        v4u pp = {pkbf(sc[0], sc[1]), pkbf(sc[2], sc[3]), pkbf(sc[4], sc[5]), pkbf(sc[6], sc[7])};
        const v4u z = {0u, 0u, 0u, 0u};
#pragma unroll
        for (int a = 0; a < 4; ++a) pa[a] = (n16 >> 2) == a ? pp : z;
    }
    STAMP(4);
    SB();
#pragma unroll
    for (int i = 5; i < 8; ++i) vr[i] = __builtin_amdgcn_raw_buffer_load_b128(r_v, l * 16, (w * 8 + i) * 1024, AUX_NT);
    const int hh = w >> 1, ch = w & 1;
    {
        const int wvo = (32 * kg + (n16 >> 3)) * 1024 + (n16 & 7) * 16;
        const int wso = (g * 512 + hh * 128 + 4 * ch) * 1024 + s * 128;
#pragma unroll
        for (int j = 0; j < 8; ++j) wr[j] = __builtin_amdgcn_raw_buffer_load_b128(r_wo, wvo, wso + (8 * (j & 3) + 2 * (j >> 2)) * 1024, AUX_O);
    }
    SB();
    STAMP(5);

    {
        v4u vf[8];
#pragma unroll
        for (int p = 0; p < 4; ++p)
#pragma unroll
            for (int q = 0; q < 4; ++q) {
                vf[2 * q][p] = perm_lo(vr[2 * p + 1][q], vr[2 * p][q]);
                vf[2 * q + 1][p] = perm_hi(vr[2 * p + 1][q], vr[2 * p][q]);
            }
        v4f a0 = {0.f, 0.f, 0.f, 0.f}, a1 = {0.f, 0.f, 0.f, 0.f};
#pragma unroll
        for (int a = 0; a < 4; ++a) { a0 = mfma16(pa[a], vf[a], a0); a1 = mfma16(pa[a], vf[4 + a], a1); }
        *(v4f*)&sm.opart[w * 512 + l * 4] = a0;
        *(v4f*)&sm.opart[w * 512 + 256 + l * 4] = a1;
    }
    STAMP(6);
    lds_barrier();
    {
        float v = 0.f;
#pragma unroll
        for (int i = 0; i < 8; ++i) v += sm.opart[i * 512 + tid];
        u32 p = pkbf(v, dppf<DPP_SHL4>(v));
        u32 p2 = dppu<DPP_SHL8>(p);
        v4u go = {p, tag, p2, tag};
        __builtin_amdgcn_raw_buffer_store_b128(go, r_ws, (l & 12) == 0 ? (l & 3) * 512 + kg * 16 : OOB, WS_O + bid * 2048 + (w >> 2) * 256 + (w & 3) * 64, AUX_SC1);
        const v4f l0 = *(const v4f*)&sm.ls[l & 3][0], l1 = *(const v4f*)&sm.ls[l & 3][4];
        const float L = ((l0.x + l0.y) + (l0.z + l0.w)) + ((l1.x + l1.y) + (l1.z + l1.w));
        v4u gm = {__builtin_bit_cast(u32, M), tag, __builtin_bit_cast(u32, L), tag};
        __builtin_amdgcn_raw_buffer_store_b128(gm, r_ws, tid < 4 ? l * 16 : OOB, WS_ML + bid * 64, AUX_SC1);
    }
    STAMP(7);
    SB();

    v2f cw[14], cb[14], cg1[14], cg0[14];
    {
        u32 a[14], bb[14], c[14];
#pragma unroll
        for (int i = 0; i < 3; ++i) {
            v4u t0 = *(const v4u*)&sm.mw[0][i * 256 + l * 4], t1 = *(const v4u*)&sm.mw[1][i * 256 + l * 4], t2 = *(const v4u*)&sm.mw[2][i * 256 + l * 4];
#pragma unroll
            for (int q = 0; q < 4; ++q) { a[i * 4 + q] = t0[q]; bb[i * 4 + q] = t1[q]; c[i * 4 + q] = t2[q]; }
        }
        v2u t0 = *(const v2u*)&sm.mw[0][768 + l * 2], t1 = *(const v2u*)&sm.mw[1][768 + l * 2], t2 = *(const v2u*)&sm.mw[2][768 + l * 2];
        a[12] = t0.x; a[13] = t0.y; bb[12] = t1.x; bb[13] = t1.y; c[12] = t2.x; c[13] = t2.y;
#pragma unroll
        for (int i = 0; i < 14; ++i) {
            v2f wv = {bflo(a[i]), bfhi(a[i])}, bv = {bflo(bb[i]), bfhi(bb[i])}, cv = {bflo(c[i]), bfhi(c[i])};
            cw[i] = wv; cb[i] = bv; cg1[i] = wv * cv; cg0[i] = bv * cv;
        }
    }
    v4u wf[8];
#pragma unroll
    for (int p = 0; p < 4; ++p)
#pragma unroll
        for (int q = 0; q < 4; ++q) {
            wf[2 * q][p] = perm_lo(wr[2 * p + 1][q], wr[2 * p][q]);
            wf[2 * q + 1][p] = perm_hi(wr[2 * p + 1][q], wr[2 * p][q]);
        }
    const int lp = (l & 32) + 4 * (l & 7) + ((l >> 3) & 3);
    const int fcol = 64 * w + 8 * (lp & 7) + (lp >> 3);
    const float xres = bflo((u32)((const u16*)sm.xrow)[fcol]);
#pragma unroll
    for (int e = 0; e < 8; ++e) asm volatile("" : "+v"(wf[e]));
#pragma unroll
    for (int i = 0; i < 14; ++i) asm volatile("" : "+v"(cw[i]), "+v"(cb[i]), "+v"(cg1[i]), "+v"(cg0[i]));
    SB();

    {
        const int ovo = (n16 & 7) * 2048 + ((n16 >> 3) * 4 + kg) * 16;
        const int oso = WS_O + r * 8 * 2048 + w * 256;
        const int mvo = (4 * (kg & 1) + (l & 3)) * 64;
        const int mso = WS_ML + r * 8 * 64 + hh * 16;
        v4u g0, g1, gm;
#pragma unroll 1
        for (int spin = 0; spin < SPIN_MAX; ++spin) {
            asm volatile("" ::: "memory");
            gm = __builtin_amdgcn_raw_buffer_load_b128(r_ws, mvo, mso, AUX_SC1);
            g0 = __builtin_amdgcn_raw_buffer_load_b128(r_ws, ovo, oso, AUX_SC1);
            g1 = __builtin_amdgcn_raw_buffer_load_b128(r_ws, ovo + 128, oso, AUX_SC1);
            bool ok = (g0.y == tag) & (g0.w == tag) & (g1.y == tag) & (g1.w == tag) & (gm.y == tag) & (gm.w == tag);
            if (__all(ok)) break;
        }
        STAMP(8);
        const v4u av = {g0.x, g0.z, g1.x, g1.z};
        v4f c[8];
#pragma unroll
        for (int e = 0; e < 8; ++e) { v4f z = {0.f, 0.f, 0.f, 0.f}; c[e] = mfma16(av, wf[e], z); }
        const float ms = u2f(gm.x), lv = u2f(gm.z);
        float mm = fmaxf(ms, dppf<DPP_X1>(ms)); mm = fmaxf(mm, dppf<DPP_X2>(mm));
        { v2f a = swap16(mm, mm); mm = fmaxf(a.x, a.y); }
        const float wg = __builtin_amdgcn_exp2f((ms - mm) * 1.4426950408889634f);
        float L = wg * lv; L += dppf<DPP_X1>(L); L += dppf<DPP_X2>(L);
        { v2f a = swap16(L, L); L = a.x + a.y; }
        float W = wg * __builtin_amdgcn_rcpf(L);
        W = ((l >> 5) == ((l >> 3) & 1)) ? W : 0.f;
        const float W0 = dppf<0x00>(W), W1 = dppf<0x55>(W), W2 = dppf<0xAA>(W), W3 = dppf<0xFF>(W);
        float y[8];
#pragma unroll
        for (int e = 0; e < 8; ++e) y[e] = (W0 * c[e].x + W1 * c[e].y) + (W2 * c[e].z + W3 * c[e].w);
        sm.apart[w * 64 + l] = treduce8(y, (l & 8) != 0);
    }
    lds_barrier();
    if (w == 0) {
        float v = 0.f;
#pragma unroll
        for (int i = 0; i < 8; ++i) v += sm.apart[i * 64 + l];
        u32 p = pkbf(v, dppf<DPP_X1>(v));
        u32 p2 = dppu<DPP_X2>(p);
        if ((l & 3) == 0) {
            v4u ga = {p, tag, p2, tag};
            __builtin_amdgcn_raw_buffer_store_b128(ga, r_ws, l * 4, WS_A + bid * 256, AUX_SC1);
        }
    }
    STAMP(9);

    {
        const int avo = (l >> 3) * (8 * 256) + (l & 7) * 16;
        const int aso = WS_A + (b * 64 + w) * 256;
        v4u g0, g1;
#pragma unroll 1
        for (int spin = 0; spin < SPIN_MAX; ++spin) {
            asm volatile("" ::: "memory");
            g0 = __builtin_amdgcn_raw_buffer_load_b128(r_ws, avo, aso, AUX_SC1);
            g1 = __builtin_amdgcn_raw_buffer_load_b128(r_ws, avo + 128, aso, AUX_SC1);
            bool ok = (g0.y == tag) & (g0.w == tag) & (g1.y == tag) & (g1.w == tag);
            if (__all(ok)) break;
        }
        float y[8] = {bflo(g0.x), bfhi(g0.x), bflo(g0.z), bfhi(g0.z), bflo(g1.x), bfhi(g1.x), bflo(g1.z), bfhi(g1.z)};
        const float at = treduce8(y, (l & 8) != 0);
        sm.hbuf[fcol] = rbf(xres + rbf(at));
    }
    STAMP(10);
    lds_barrier();
    {
        const int jj = g * 8 + s;
        const v4f h0 = *(const v4f*)&sm.hbuf[l * 8], h1 = *(const v4f*)&sm.hbuf[l * 8 + 4];
        const float hown = sm.hbuf[jj * 8 + w];
        float hv[8] = {h0.x, h0.y, h0.z, h0.w, h1.x, h1.y, h1.z, h1.w};
        float sum = ((hv[0] + hv[1]) + (hv[2] + hv[3])) + ((hv[4] + hv[5]) + (hv[6] + hv[7]));
        const float mean = wave_sum(sum) * (1.f / 512.f);
        float m2 = 0.f;
#pragma unroll
        for (int i = 0; i < 8; ++i) { float d = hv[i] - mean; m2 += d * d; }
        const float var = wave_sum(m2) * (1.f / 512.f);
        const float mb = rbf(mean);
        const float rden = __builtin_amdgcn_rcpf(rbf(__builtin_amdgcn_sqrtf(rbf(rbf(var) + 1e-5f))));
        float s1 = 0.f, s2 = 0.f;
#pragma unroll
        for (int i = 0; i < 4; ++i) {
            u32 d = pkbf(hv[2 * i] - mb, hv[2 * i + 1] - mb);
            u32 e = pkbf(bflo(d) * rden, bfhi(d) * rden);
            float e0 = bflo(e), e1 = bfhi(e);
            s1 += e0 + e1; s2 += e0 * e0 + e1 * e1;
        }
        const float mean2 = wave_sum(s1) * (1.f / 512.f);
        const float var2 = fmaxf(wave_sum(s2) * (1.f / 512.f) - mean2 * mean2, 0.f);
        const float rden2 = __builtin_amdgcn_rcpf(rbf(__builtin_amdgcn_sqrtf(rbf(rbf(var2) + 1e-5f))));
        const float n1 = rbf(rbf(hown - mb) * rden);
        const float tt = rbf(rbf(n1 - rbf(mean2)) * rden2);
        const v2f t2 = {tt, tt};
        const float K0 = -2.0f * 1.4426950408889634f * 0.7978845608028654f, K1 = K0 * 0.044715f;
        const v2f k0 = {K0, K0}, k1 = {K1, K1}, one = {1.f, 1.f};
        v2f acc = {0.f, 0.f};
#pragma unroll
        for (int i = 0; i < 14; ++i) {
            v2f u = __builtin_elementwise_fma(t2, cw[i], cb[i]);
            v2f a = u * __builtin_elementwise_fma(u * u, k1, k0);
            v2f e = {__builtin_amdgcn_exp2f(a.x), __builtin_amdgcn_exp2f(a.y)};
            e += one;
            v2f rc = {__builtin_amdgcn_rcpf(e.x), __builtin_amdgcn_rcpf(e.y)};
            acc = __builtin_elementwise_fma(__builtin_elementwise_fma(t2, cg1[i], cg0[i]), rc, acc);
        }
        const float yv = rbf(rbf(wave_sum(acc.x + acc.y)) + b2v);
        if (l == 0) out[b * 512 + jj * 8 + w] = (u16)pkbf(hown + yv, 0.f);
        if (bid == 0 && tid == 0) __builtin_amdgcn_raw_buffer_store_b32(tag, r_ws, 0, WS_EPOCH, AUX_SC1);
    }
    STAMP(11);
}

extern "C" void kernel_launch(void* const* d_in, const int* in_sizes, int n_in, void* d_out, int out_size, void* d_ws, size_t ws_size, hipStream_t stream) {
    fused_decode_kernel<<<256, 512, 0, stream>>>((const u16*)d_in[0], (const u16*)d_in[1], (const u16*)d_in[2], (const u16*)d_in[3], (const u16*)d_in[4],
                                                 (const u16*)d_in[6], (const u16*)d_in[7], (const u16*)d_in[8], (const u16*)d_in[9],
                                                 (u16*)d_out, (unsigned char*)d_ws);
}
```

```cpp
#define BENCH_STAMPS
#define PROBE 0
#include <hip/hip_runtime.h>
#include <stdint.h>

typedef unsigned u32;
typedef unsigned short u16;
typedef u32 v4u __attribute__((ext_vector_type(4)));
typedef u32 v2u __attribute__((ext_vector_type(2)));
typedef float v4f __attribute__((ext_vector_type(4)));
typedef float v2f __attribute__((ext_vector_type(2)));
typedef __bf16 v8bf __attribute__((ext_vector_type(8)));
typedef __bf16 v2bf __attribute__((ext_vector_type(2)));
typedef short v4s __attribute__((ext_vector_type(4)));
#define LAS __attribute__((address_space(3)))

#define NSTAMP 16
#define STAMP_OFF (128u << 20)
#define STAMP(i) do { __builtin_amdgcn_sched_barrier(0); tstamp[i] = __builtin_amdgcn_s_memrealtime(); __builtin_amdgcn_sched_barrier(0); } while (0)

#define AUX_O 2

constexpr int AUX_NT = 2, AUX_SC1 = 16;
constexpr u32 WS_EPOCH = 0;
constexpr u32 WS_Q = 4096;
constexpr u32 WS_O = WS_Q + 32 * 2048;
constexpr u32 WS_ML = WS_O + 256 * 2048;
constexpr u32 WS_A = WS_ML + 256 * 64;
constexpr u32 WS_END = WS_A + 256 * 256;
constexpr int SPIN_MAX = 1 << 18;
constexpr u32 CODE_BYTES = 9216;
constexpr int OOB = 0x40000000;

__device__ __forceinline__ float u2f(u32 v) { return __builtin_bit_cast(float, v); }
__device__ __forceinline__ float bflo(u32 v) { return __builtin_bit_cast(float, v << 16); }
__device__ __forceinline__ float bfhi(u32 v) { return __builtin_bit_cast(float, v & 0xffff0000u); }
__device__ __forceinline__ u32 pkbf(float a, float b) { v2f f = {a, b}; return __builtin_bit_cast(u32, __builtin_convertvector(f, v2bf)); }
__device__ __forceinline__ float rbf(float a) { return bflo(pkbf(a, a)); }
template <int CTRL> __device__ __forceinline__ float dppf(float v) {
    return __builtin_bit_cast(float, __builtin_amdgcn_update_dpp(0, __builtin_bit_cast(int, v), CTRL, 0xf, 0xf, true));
}
template <int CTRL> __device__ __forceinline__ u32 dppu(u32 v) { return (u32)__builtin_amdgcn_update_dpp(0, (int)v, CTRL, 0xf, 0xf, true); }
constexpr int DPP_X1 = 0xB1, DPP_X2 = 0x4E, DPP_HMIR = 0x141, DPP_ROR4 = 0x124, DPP_ROR8 = 0x128, DPP_SHL4 = 0x104, DPP_SHL8 = 0x108;
__device__ __forceinline__ v2f swap32(float a, float b) {
    auto r = __builtin_amdgcn_permlane32_swap(__builtin_bit_cast(u32, a), __builtin_bit_cast(u32, b), false, false);
    v2f o = {__builtin_bit_cast(float, (u32)r[0]), __builtin_bit_cast(float, (u32)r[1])}; return o;
}
__device__ __forceinline__ v2f swap16(float a, float b) {
    auto r = __builtin_amdgcn_permlane16_swap(__builtin_bit_cast(u32, a), __builtin_bit_cast(u32, b), false, false);
    v2f o = {__builtin_bit_cast(float, (u32)r[0]), __builtin_bit_cast(float, (u32)r[1])}; return o;
}
__device__ __forceinline__ float sum_x16_x32(float v) { v2f a = swap16(v, v); v = a.x + a.y; a = swap32(v, v); return a.x + a.y; }
__device__ __forceinline__ float max_x16_x32(float v) { v2f a = swap16(v, v); v = fmaxf(a.x, a.y); a = swap32(v, v); return fmaxf(a.x, a.y); }
__device__ __forceinline__ float wave_sum(float v) {
    v += dppf<DPP_X1>(v); v += dppf<DPP_X2>(v); v += dppf<DPP_HMIR>(v); v += dppf<DPP_ROR8>(v);
    return sum_x16_x32(v);
}
__device__ __forceinline__ float treduce8(const float (&y)[8], bool bit3) {
    float z[4], u[2];
#pragma unroll
    for (int e = 0; e < 4; ++e) { v2f a = swap32(y[e], y[e + 4]); z[e] = a.x + a.y; }
#pragma unroll
    for (int e = 0; e < 2; ++e) { v2f a = swap16(z[e], z[e + 2]); u[e] = a.x + a.y; }
    float keep = bit3 ? u[1] : u[0], send = bit3 ? u[0] : u[1];
    return keep + dppf<DPP_ROR8>(send);
}
__device__ __forceinline__ v4f mfma16(v4u a, v4u b, v4f c) {
    return __builtin_amdgcn_mfma_f32_16x16x32_bf16(__builtin_bit_cast(v8bf, a), __builtin_bit_cast(v8bf, b), c, 0, 0, 0);
}
__device__ __forceinline__ void lds_barrier() { asm volatile("s_waitcnt lgkmcnt(0)\n\ts_barrier" ::: "memory"); }
#define SB() __builtin_amdgcn_sched_barrier(0)
__device__ __forceinline__ __amdgpu_buffer_rsrc_t mkrsrc(const void* p, u32 bytes) { return __builtin_amdgcn_make_buffer_rsrc((void*)p, 0, bytes, 0x00020000); }
__device__ __forceinline__ u32 perm_lo(u32 hi, u32 lo) { return __builtin_amdgcn_perm(hi, lo, 0x05040100u); }
__device__ __forceinline__ u32 perm_hi(u32 hi, u32 lo) { return __builtin_amdgcn_perm(hi, lo, 0x07060302u); }

struct __attribute__((aligned(16))) Smem {
    float opart[8 * 512];
    u32 mw[3][1024];
    u32 xrow[256];
    float qpart[8 * 64];
    u32 qimg[256];
    float stats[4][8][2];
    float mx[4][8];
    float ls[4][8];
    float apart[8 * 64];
    float hbuf[512];
    float pad[16 * 1024];
};

__global__ void __launch_bounds__(512, 2)
fused_decode_kernel(const u16* __restrict__ x, const u16* __restrict__ kc, const u16* __restrict__ vc,
                    const u16* __restrict__ wq, const u16* __restrict__ wo, const u16* __restrict__ w1,
                    const u16* __restrict__ b1, const u16* __restrict__ w2, const u16* __restrict__ b2,
                    u16* __restrict__ out, unsigned char* ws) {
    __shared__ Smem sm;
    unsigned long long tstamp[NSTAMP];
    STAMP(0);
    const int bid = blockIdx.x, tid = threadIdx.x, l = tid & 63;
    const int w = __builtin_amdgcn_readfirstlane(tid >> 6);
    const int s = bid & 7, r = bid >> 3, b = r >> 3, g = r & 7;
    const int n16 = l & 15, kg = l >> 4;

    const __amdgpu_buffer_rsrc_t r_ws = mkrsrc(ws, WS_END);
    const size_t kvbase = ((size_t)(b * 8 + g) * 8192 + 6144 + s * 256) * 128;
    const __amdgpu_buffer_rsrc_t r_k = mkrsrc(kc + kvbase, 65536);
    const __amdgpu_buffer_rsrc_t r_v = mkrsrc(vc + kvbase, 65536);
    const __amdgpu_buffer_rsrc_t r_x = mkrsrc(x, 4096);
    const __amdgpu_buffer_rsrc_t r_wq = mkrsrc(wq, 4u << 20);
    const __amdgpu_buffer_rsrc_t r_wo = mkrsrc(wo, 4u << 20);

    v4u xf[2], wqr[2], kr[8], vr[8], wr[8];
#pragma unroll
    for (int t = 0; t < 2; ++t) xf[t] = __builtin_amdgcn_raw_buffer_load_b128(r_x, (l & 3) * 1024 + kg * 16 + t * 64, w * 128, 0);
#pragma unroll
    for (int t = 0; t < 2; ++t) wqr[t] = __builtin_amdgcn_raw_buffer_load_b128(r_wq, (l >> 1) * 8192 + (l & 1) * 16, (w * 64 + t * 32) * 8192 + s * 1024 + r * 32, AUX_NT);
    const u32 tag = __builtin_amdgcn_raw_buffer_load_b32(r_ws, 0, WS_EPOCH, AUX_SC1) + 1u;
    u32 pre;
    {
        unsigned long long pc;
        asm volatile("s_getpc_b64 %0" : "=s"(pc));
        pre = __builtin_amdgcn_raw_buffer_load_b32(mkrsrc((const void*)(pc & ~127ull), CODE_BYTES), n16 * 128 + w * 2048, 0, 0);
    }
    const int koff = (n16 >> 2) * 256 + (4 * kg + (n16 & 3)) * 16;
#pragma unroll
    for (int i = 0; i < 3; ++i) kr[i] = __builtin_amdgcn_raw_buffer_load_b128(r_k, koff, (w * 8 + i) * 1024, AUX_NT);
    SB();

    float xv[16];
#pragma unroll
    for (int t = 0; t < 2; ++t)
#pragma unroll
        for (int q = 0; q < 4; ++q) { xv[t * 8 + 2 * q] = bflo(xf[t][q]); xv[t * 8 + 2 * q + 1] = bfhi(xf[t][q]); }
    {
        float sum = 0.f;
#pragma unroll
        for (int i = 0; i < 16; ++i) sum += xv[i];
        sum = sum_x16_x32(sum);
        const float mw = sum * (1.f / 64.f);
        float m2 = 0.f;
#pragma unroll
        for (int i = 0; i < 16; ++i) { float d = xv[i] - mw; m2 += d * d; }
        m2 = sum_x16_x32(m2);
        if (l < 4) { v2f st = {sum, m2}; *(v2f*)&sm.stats[l][w][0] = st; }
    }
    STAMP(1);
    lds_barrier();
    STAMP(12);
    v4u xa[2];
    {
        v4f st[4];
#pragma unroll
        for (int i = 0; i < 4; ++i) st[i] = *(const v4f*)&sm.stats[l & 3][2 * i][0];
        float sum = 0.f;
#pragma unroll
        for (int i = 0; i < 4; ++i) sum += st[i].x + st[i].z;
        const float mean = sum * (1.f / 512.f);
        float m2 = 0.f;
#pragma unroll
        for (int i = 0; i < 4; ++i) {
            float d0 = st[i].x * (1.f / 64.f) - mean, d1 = st[i].z * (1.f / 64.f) - mean;
            m2 += st[i].y + st[i].w + 64.f * (d0 * d0 + d1 * d1);
        }
        const float mb = rbf(mean), vb = rbf(m2 * (1.f / 512.f));
        const float rden = 1.0f / rbf(__builtin_sqrtf(rbf(vb + 1e-5f)));
#pragma unroll
        for (int t = 0; t < 2; ++t)
#pragma unroll
            for (int q = 0; q < 4; ++q) {
                u32 d = pkbf(xv[t * 8 + 2 * q] - mb, xv[t * 8 + 2 * q + 1] - mb);
                xa[t][q] = pkbf(bflo(d) * rden, bfhi(d) * rden);
            }
    }
    {
        unsigned char* img = (unsigned char*)sm.opart + w * 2048;
        STAMP(13);
        asm volatile("" : "+v"(wqr[0]), "+v"(wqr[1]));
        STAMP(14);
        *(v4u*)(img + l * 16) = wqr[0];
        *(v4u*)(img + 1024 + l * 16) = wqr[1];
        v4f acc = {0.f, 0.f, 0.f, 0.f};
#pragma unroll
        for (int t = 0; t < 2; ++t) {
            LAS v4s* p = (LAS v4s*)(img + t * 1024 + kg * 256 + n16 * 8);
            v2u lo = __builtin_bit_cast(v2u, __builtin_amdgcn_ds_read_tr16_b64_v4i16(p));
            v2u hi = __builtin_bit_cast(v2u, __builtin_amdgcn_ds_read_tr16_b64_v4i16(p + 16));
            v4u bf = {lo.x, lo.y, hi.x, hi.y};
            acc = mfma16(xa[t], bf, acc);
        }
        if (l < 16) *(v4f*)&sm.qpart[w * 64 + l * 4] = acc;
    }
    lds_barrier();
    STAMP(15);
    if (w == 0) {
        float v = 0.f;
#pragma unroll
        for (int i = 0; i < 8; ++i) v += sm.qpart[i * 64 + l];
        u32 p = pkbf(v, dppf<DPP_SHL4>(v));
        u32 p2 = dppu<DPP_SHL8>(p);
        if ((l & 12) == 0) {
            v4u gq = {p, tag, p2, tag};
            __builtin_amdgcn_raw_buffer_store_b128(gq, r_ws, (l & 3) * (8 * 2048) + kg * 16, WS_Q + s * 2048 + r * 64, AUX_SC1);
        }
    }
    STAMP(2);
    SB();

#pragma unroll
    for (int i = 3; i < 8; ++i) kr[i] = __builtin_amdgcn_raw_buffer_load_b128(r_k, koff, (w * 8 + i) * 1024, AUX_NT);
    v4u sa, sb2;
    {
        const __amdgpu_buffer_rsrc_t ra = mkrsrc(w < 4 ? w1 : b1, 3584);
        const __amdgpu_buffer_rsrc_t rb = mkrsrc(w < 4 ? w2 : x + b * 512, w < 4 ? 3584 : 1024);
        sa = __builtin_amdgcn_raw_buffer_load_b128(ra, l * 16 + (w & 3) * 1024, 0, 0);
        sb2 = __builtin_amdgcn_raw_buffer_load_b128(rb, l * 16 + (w & 3) * 1024, 0, 0);
    }
    const float b2v = bflo(b2[0]);
    const int qoff = WS_Q + (b * 8 + g) * 2048 + w * 256;
    v4u pv = __builtin_amdgcn_raw_buffer_load_b128(r_ws, n16 * 16, qoff, AUX_SC1);
#pragma unroll
    for (int i = 0; i < 3; ++i) vr[i] = __builtin_amdgcn_raw_buffer_load_b128(r_v, l * 16, (w * 8 + i) * 1024, AUX_NT);
    SB();
    *(v4u*)&sm.mw[w < 4 ? 0 : 1][(w & 3) * 256 + l * 4] = sa;
    if (w < 4) *(v4u*)&sm.mw[2][w * 256 + l * 4] = sb2;
    else if (w == 4) *(v4u*)&sm.xrow[l * 4] = sb2;
    if (!__all((pv.y == tag) & (pv.w == tag))) {
#pragma unroll 1
        for (int spin = 0; spin < SPIN_MAX; ++spin) {
            asm volatile("" ::: "memory");
            pv = __builtin_amdgcn_raw_buffer_load_b128(r_ws, n16 * 16, qoff, AUX_SC1);
            if (__all((pv.y == tag) & (pv.w == tag))) break;
        }
    }
    if (l < 16) { v2u qq = {pv.x, pv.z}; *(v2u*)&sm.qimg[w * 32 + l * 2] = qq; }
    STAMP(3);
    lds_barrier();

    float sc[8];
    {
        const v4u qf = *(const v4u*)&sm.qimg[(n16 & 3) * 64 + (4 * kg + (n16 >> 2)) * 4];
        const bool j0 = (l & 4) != 0, j1 = (l & 8) != 0;
        float m = -__builtin_inff();
#pragma unroll
        for (int i = 0; i < 8; ++i) {
            v4f c = {0.f, 0.f, 0.f, 0.f};
            c = mfma16(kr[i], qf, c);
            float dl = j0 ? c.y : c.x, dh = j0 ? c.w : c.z;
            float d = j1 ? dh : dl;
            d += dppf<DPP_ROR4>(d);
            d += dppf<DPP_ROR8>(d);
            d = rbf(rbf(d) * 0.08838834764831845f);
            sc[i] = d;
            m = fmaxf(m, d);
        }
        m = max_x16_x32(m);
        if (l < 4) sm.mx[l][w] = m;
        asm volatile("" :: "v"(pre));
    }
    SB();
#pragma unroll
    for (int i = 3; i < 5; ++i) vr[i] = __builtin_amdgcn_raw_buffer_load_b128(r_v, l * 16, (w * 8 + i) * 1024, AUX_NT);
    SB();
    lds_barrier();
    float M;
    v4u pa[4];
    {
        const v4f m0 = *(const v4f*)&sm.mx[l & 3][0], m1 = *(const v4f*)&sm.mx[l & 3][4];
        M = fmaxf(fmaxf(fmaxf(m0.x, m0.y), fmaxf(m0.z, m0.w)), fmaxf(fmaxf(m1.x, m1.y), fmaxf(m1.z, m1.w)));
        float lsum = 0.f;
#pragma unroll
        for (int i = 0; i < 8; ++i) { sc[i] = __builtin_amdgcn_exp2f((sc[i] - M) * 1.4426950408889634f); lsum += sc[i]; }
        lsum = sum_x16_x32(lsum);
        if (l < 4) sm.ls[l][w] = lsum;
        v4u pp = {pkbf(sc[0], sc[1]), pkbf(sc[2], sc[3]), pkbf(sc[4], sc[5]), pkbf(sc[6], sc[7])};
        const v4u z = {0u, 0u, 0u, 0u};
#pragma unroll
        for (int a = 0; a < 4; ++a) pa[a] = (n16 >> 2) == a ? pp : z;
    }
    STAMP(4);
    SB();
#pragma unroll
    for (int i = 5; i < 8; ++i) vr[i] = __builtin_amdgcn_raw_buffer_load_b128(r_v, l * 16, (w * 8 + i) * 1024, AUX_NT);
    const int hh = w >> 1, ch = w & 1;
    {
        const int wvo = (32 * kg + (n16 >> 3)) * 1024 + (n16 & 7) * 16;
        const int wso = (g * 512 + hh * 128 + 4 * ch) * 1024 + s * 128;
#pragma unroll
        for (int j = 0; j < 8; ++j) wr[j] = __builtin_amdgcn_raw_buffer_load_b128(r_wo, wvo, wso + (8 * (j & 3) + 2 * (j >> 2)) * 1024, AUX_O);
    }
    SB();
    STAMP(5);

    {
        v4u vf[8];
#pragma unroll
        for (int p = 0; p < 4; ++p)
#pragma unroll
            for (int q = 0; q < 4; ++q) {
                vf[2 * q][p] = perm_lo(vr[2 * p + 1][q], vr[2 * p][q]);
                vf[2 * q + 1][p] = perm_hi(vr[2 * p + 1][q], vr[2 * p][q]);
            }
        v4f a0 = {0.f, 0.f, 0.f, 0.f}, a1 = {0.f, 0.f, 0.f, 0.f};
#pragma unroll
        for (int a = 0; a < 4; ++a) { a0 = mfma16(pa[a], vf[a], a0); a1 = mfma16(pa[a], vf[4 + a], a1); }
        *(v4f*)&sm.opart[w * 512 + l * 4] = a0;
        *(v4f*)&sm.opart[w * 512 + 256 + l * 4] = a1;
    }
    STAMP(6);
    lds_barrier();
    {
        float v = 0.f;
#pragma unroll
        for (int i = 0; i < 8; ++i) v += sm.opart[i * 512 + tid];
        u32 p = pkbf(v, dppf<DPP_SHL4>(v));
        u32 p2 = dppu<DPP_SHL8>(p);
        v4u go = {p, tag, p2, tag};
        __builtin_amdgcn_raw_buffer_store_b128(go, r_ws, (l & 12) == 0 ? (l & 3) * 512 + kg * 16 : OOB, WS_O + bid * 2048 + (w >> 2) * 256 + (w & 3) * 64, AUX_SC1);
        const v4f l0 = *(const v4f*)&sm.ls[l & 3][0], l1 = *(const v4f*)&sm.ls[l & 3][4];
        const float L = ((l0.x + l0.y) + (l0.z + l0.w)) + ((l1.x + l1.y) + (l1.z + l1.w));
        v4u gm = {__builtin_bit_cast(u32, M), tag, __builtin_bit_cast(u32, L), tag};
        __builtin_amdgcn_raw_buffer_store_b128(gm, r_ws, tid < 4 ? l * 16 : OOB, WS_ML + bid * 64, AUX_SC1);
    }
    STAMP(7);
    SB();

    v2f cw[14], cb[14], cg1[14], cg0[14];
    {
        u32 a[14], bb[14], c[14];
#pragma unroll
        for (int i = 0; i < 3; ++i) {
            v4u t0 = *(const v4u*)&sm.mw[0][i * 256 + l * 4], t1 = *(const v4u*)&sm.mw[1][i * 256 + l * 4], t2 = *(const v4u*)&sm.mw[2][i * 256 + l * 4];
#pragma unroll
            for (int q = 0; q < 4; ++q) { a[i * 4 + q] = t0[q]; bb[i * 4 + q] = t1[q]; c[i * 4 + q] = t2[q]; }
        }
        v2u t0 = *(const v2u*)&sm.mw[0][768 + l * 2], t1 = *(const v2u*)&sm.mw[1][768 + l * 2], t2 = *(const v2u*)&sm.mw[2][768 + l * 2];
        a[12] = t0.x; a[13] = t0.y; bb[12] = t1.x; bb[13] = t1.y; c[12] = t2.x; c[13] = t2.y;
#pragma unroll
        for (int i = 0; i < 14; ++i) {
            v2f wv = {bflo(a[i]), bfhi(a[i])}, bv = {bflo(bb[i]), bfhi(bb[i])}, cv = {bflo(c[i]), bfhi(c[i])};
            cw[i] = wv; cb[i] = bv; cg1[i] = wv * cv; cg0[i] = bv * cv;
        }
    }
    v4u wf[8];
#pragma unroll
    for (int p = 0; p < 4; ++p)
#pragma unroll
        for (int q = 0; q < 4; ++q) {
            wf[2 * q][p] = perm_lo(wr[2 * p + 1][q], wr[2 * p][q]);
            wf[2 * q + 1][p] = perm_hi(wr[2 * p + 1][q], wr[2 * p][q]);
        }
    const int lp = (l & 32) + 4 * (l & 7) + ((l >> 3) & 3);
    const int fcol = 64 * w + 8 * (lp & 7) + (lp >> 3);
    const float xres = bflo((u32)((const u16*)sm.xrow)[fcol]);
#pragma unroll
    for (int e = 0; e < 8; ++e) asm volatile("" : "+v"(wf[e]));
#pragma unroll
    for (int i = 0; i < 14; ++i) asm volatile("" : "+v"(cw[i]), "+v"(cb[i]), "+v"(cg1[i]), "+v"(cg0[i]));
    SB();

    {
        const int ovo = (n16 & 7) * 2048 + ((n16 >> 3) * 4 + kg) * 16;
        const int oso = WS_O + r * 8 * 2048 + w * 256;
        const int mvo = (4 * (kg & 1) + (l & 3)) * 64;
        const int mso = WS_ML + r * 8 * 64 + hh * 16;
        v4u g0, g1, gm;
#pragma unroll 1
        for (int spin = 0; spin < SPIN_MAX; ++spin) {
            asm volatile("" ::: "memory");
            gm = __builtin_amdgcn_raw_buffer_load_b128(r_ws, mvo, mso, AUX_SC1);
            g0 = __builtin_amdgcn_raw_buffer_load_b128(r_ws, ovo, oso, AUX_SC1);
            g1 = __builtin_amdgcn_raw_buffer_load_b128(r_ws, ovo + 128, oso, AUX_SC1);
            bool ok = (g0.y == tag) & (g0.w == tag) & (g1.y == tag) & (g1.w == tag) & (gm.y == tag) & (gm.w == tag);
            if (__all(ok)) break;
        }
        STAMP(8);
        const v4u av = {g0.x, g0.z, g1.x, g1.z};
        v4f c[8];
#pragma unroll
        for (int e = 0; e < 8; ++e) { v4f z = {0.f, 0.f, 0.f, 0.f}; c[e] = mfma16(av, wf[e], z); }
        const float ms = u2f(gm.x), lv = u2f(gm.z);
        float mm = fmaxf(ms, dppf<DPP_X1>(ms)); mm = fmaxf(mm, dppf<DPP_X2>(mm));
        { v2f a = swap16(mm, mm); mm = fmaxf(a.x, a.y); }
        const float wg = __builtin_amdgcn_exp2f((ms - mm) * 1.4426950408889634f);
        float L = wg * lv; L += dppf<DPP_X1>(L); L += dppf<DPP_X2>(L);
        { v2f a = swap16(L, L); L = a.x + a.y; }
        float W = wg * __builtin_amdgcn_rcpf(L);
        W = ((l >> 5) == ((l >> 3) & 1)) ? W : 0.f;
        const float W0 = dppf<0x00>(W), W1 = dppf<0x55>(W), W2 = dppf<0xAA>(W), W3 = dppf<0xFF>(W);
        float y[8];
#pragma unroll
        for (int e = 0; e < 8; ++e) y[e] = (W0 * c[e].x + W1 * c[e].y) + (W2 * c[e].z + W3 * c[e].w);
        sm.apart[w * 64 + l] = treduce8(y, (l & 8) != 0);
    }
    lds_barrier();
    if (w == 0) {
        float v = 0.f;
#pragma unroll
        for (int i = 0; i < 8; ++i) v += sm.apart[i * 64 + l];
        u32 p = pkbf(v, dppf<DPP_X1>(v));
        u32 p2 = dppu<DPP_X2>(p);
        if ((l & 3) == 0) {
            v4u ga = {p, tag, p2, tag};
            __builtin_amdgcn_raw_buffer_store_b128(ga, r_ws, l * 4, WS_A + bid * 256, AUX_SC1);
        }
    }
    STAMP(9);

    {
        const int avo = (l >> 3) * (8 * 256) + (l & 7) * 16;
        const int aso = WS_A + (b * 64 + w) * 256;
        v4u g0, g1;
#pragma unroll 1
        for (int spin = 0; spin < SPIN_MAX; ++spin) {
            asm volatile("" ::: "memory");
            g0 = __builtin_amdgcn_raw_buffer_load_b128(r_ws, avo, aso, AUX_SC1);
            g1 = __builtin_amdgcn_raw_buffer_load_b128(r_ws, avo + 128, aso, AUX_SC1);
            bool ok = (g0.y == tag) & (g0.w == tag) & (g1.y == tag) & (g1.w == tag);
            if (__all(ok)) break;
        }
        float y[8] = {bflo(g0.x), bfhi(g0.x), bflo(g0.z), bfhi(g0.z), bflo(g1.x), bfhi(g1.x), bflo(g1.z), bfhi(g1.z)};
        const float at = treduce8(y, (l & 8) != 0);
        sm.hbuf[fcol] = rbf(xres + rbf(at));
    }
    STAMP(10);
    lds_barrier();
    {
        const int jj = g * 8 + s;
        const v4f h0 = *(const v4f*)&sm.hbuf[l * 8], h1 = *(const v4f*)&sm.hbuf[l * 8 + 4];
        const float hown = sm.hbuf[jj * 8 + w];
        float hv[8] = {h0.x, h0.y, h0.z, h0.w, h1.x, h1.y, h1.z, h1.w};
        float sum = ((hv[0] + hv[1]) + (hv[2] + hv[3])) + ((hv[4] + hv[5]) + (hv[6] + hv[7]));
        const float mean = wave_sum(sum) * (1.f / 512.f);
        float m2 = 0.f;
#pragma unroll
        for (int i = 0; i < 8; ++i) { float d = hv[i] - mean; m2 += d * d; }
        const float var = wave_sum(m2) * (1.f / 512.f);
        const float mb = rbf(mean);
        const float rden = __builtin_amdgcn_rcpf(rbf(__builtin_amdgcn_sqrtf(rbf(rbf(var) + 1e-5f))));
        float s1 = 0.f, s2 = 0.f;
#pragma unroll
        for (int i = 0; i < 4; ++i) {
            u32 d = pkbf(hv[2 * i] - mb, hv[2 * i + 1] - mb);
            u32 e = pkbf(bflo(d) * rden, bfhi(d) * rden);
            float e0 = bflo(e), e1 = bfhi(e);
            s1 += e0 + e1; s2 += e0 * e0 + e1 * e1;
        }
        const float mean2 = wave_sum(s1) * (1.f / 512.f);
        const float var2 = fmaxf(wave_sum(s2) * (1.f / 512.f) - mean2 * mean2, 0.f);
        const float rden2 = __builtin_amdgcn_rcpf(rbf(__builtin_amdgcn_sqrtf(rbf(rbf(var2) + 1e-5f))));
        const float n1 = rbf(rbf(hown - mb) * rden);
        const float tt = rbf(rbf(n1 - rbf(mean2)) * rden2);
        const v2f t2 = {tt, tt};
        const float K0 = -2.0f * 1.4426950408889634f * 0.7978845608028654f, K1 = K0 * 0.044715f;
        const v2f k0 = {K0, K0}, k1 = {K1, K1}, one = {1.f, 1.f};
        v2f acc = {0.f, 0.f};
#pragma unroll
        for (int i = 0; i < 14; ++i) {
            v2f u = __builtin_elementwise_fma(t2, cw[i], cb[i]);
            v2f a = u * __builtin_elementwise_fma(u * u, k1, k0);
            v2f e = {__builtin_amdgcn_exp2f(a.x), __builtin_amdgcn_exp2f(a.y)};
            e += one;
            v2f rc = {__builtin_amdgcn_rcpf(e.x), __builtin_amdgcn_rcpf(e.y)};
            acc = __builtin_elementwise_fma(__builtin_elementwise_fma(t2, cg1[i], cg0[i]), rc, acc);
        }
        const float yv = rbf(rbf(wave_sum(acc.x + acc.y)) + b2v);
        if (l == 0) out[b * 512 + jj * 8 + w] = (u16)pkbf(hown + yv, 0.f);
        if (bid == 0 && tid == 0) __builtin_amdgcn_raw_buffer_store_b32(tag, r_ws, 0, WS_EPOCH, AUX_SC1);
    }
    STAMP(11);
    if (w == 0) { const unsigned long long tend = tstamp[0] + 3000 + 4 * (tstamp[PROBE] - tstamp[0]);
        for (int i = 0; i < 100000 && __builtin_amdgcn_s_memrealtime() < tend; ++i) __builtin_amdgcn_s_sleep(1); }
}

extern "C" void kernel_launch(void* const* d_in, const int* in_sizes, int n_in, void* d_out, int out_size, void* d_ws, size_t ws_size, hipStream_t stream) {
    fused_decode_kernel<<<256, 512, 0, stream>>>((const u16*)d_in[0], (const u16*)d_in[1], (const u16*)d_in[2], (const u16*)d_in[3], (const u16*)d_in[4],
                                                 (const u16*)d_in[6], (const u16*)d_in[7], (const u16*)d_in[8], (const u16*)d_in[9],
                                                 (u16*)d_out, (unsigned char*)d_ws);
}
```

```cpp
#include <hip/hip_runtime.h>
#include <stdint.h>

typedef unsigned u32;
typedef unsigned short u16;
typedef u32 v4u __attribute__((ext_vector_type(4)));
typedef u32 v2u __attribute__((ext_vector_type(2)));
typedef float v4f __attribute__((ext_vector_type(4)));
typedef float v2f __attribute__((ext_vector_type(2)));
typedef __bf16 v8bf __attribute__((ext_vector_type(8)));
typedef __bf16 v2bf __attribute__((ext_vector_type(2)));
typedef short v4s __attribute__((ext_vector_type(4)));
#define LAS __attribute__((address_space(3)))

#define STAMP(i) do { } while (0)

#define AUX_O 2
#ifndef NV0
#define NV0 3
#endif
#define MAPG 0
#define QRX 0
#define TLBW 1
#define QDELAY 20
#define KDELAY 0
#define WIN 3
#ifndef NVY
#define NVY 6
#endif
#ifndef NV1
#define NV1 8
#endif
#define HPRIO 3

constexpr int AUX_NT = 2, AUX_SC1 = 16;
constexpr u32 WS_EPOCH = 0;
#define QSPLIT 1
constexpr u32 WS_Q = 4096;
constexpr u32 WS_O = WS_Q + 32 * 16384;
constexpr u32 WS_ML = WS_O + 256 * 2048;
constexpr u32 WS_A = WS_ML + 256 * 64;
constexpr u32 WS_END = WS_A + 256 * 256;
constexpr int SPIN_MAX = 1 << 18;
constexpr u32 CODE_BYTES = 12032;
constexpr int OOB = 0x40000000;
constexpr float LOG2E = 1.4426950408889634f;

__device__ __forceinline__ float u2f(u32 v) { return __builtin_bit_cast(float, v); }
__device__ __forceinline__ u32 f2u(float v) { return __builtin_bit_cast(u32, v); }
__device__ __forceinline__ float bflo(u32 v) { return u2f(v << 16); }
__device__ __forceinline__ float bfhi(u32 v) { return u2f(v & 0xffff0000u); }
__device__ __forceinline__ v2f bf2(u32 v) { v2f r = {bflo(v), bfhi(v)}; return r; }
__device__ __forceinline__ u32 pk2(v2f f) { return __builtin_bit_cast(u32, __builtin_convertvector(f, v2bf)); }
__device__ __forceinline__ u32 pkbf(float a, float b) { v2f f = {a, b}; return pk2(f); }
__device__ __forceinline__ float rbf(float a) { return bflo(pkbf(a, a)); }
template <int CTRL> __device__ __forceinline__ float dppf(float v) { return u2f((u32)__builtin_amdgcn_update_dpp(0, (int)f2u(v), CTRL, 0xf, 0xf, true)); }
template <int CTRL> __device__ __forceinline__ u32 dppu(u32 v) { return (u32)__builtin_amdgcn_update_dpp(0, (int)v, CTRL, 0xf, 0xf, true); }
constexpr int DPP_X1 = 0xB1, DPP_X2 = 0x4E, DPP_HMIR = 0x141, DPP_ROR4 = 0x124, DPP_ROR8 = 0x128, DPP_SHL4 = 0x104, DPP_SHL8 = 0x108;
__device__ __forceinline__ float fmx(float a, float b) { return __builtin_fmaxf(a, b); }
__device__ __forceinline__ v2f swap32(float a, float b) {
    auto r = __builtin_amdgcn_permlane32_swap(f2u(a), f2u(b), false, false);
    v2f o = {u2f(r[0]), u2f(r[1])}; return o;
}
__device__ __forceinline__ v2f swap16(float a, float b) {
    auto r = __builtin_amdgcn_permlane16_swap(f2u(a), f2u(b), false, false);
    v2f o = {u2f(r[0]), u2f(r[1])}; return o;
}
__device__ __forceinline__ float sum_x16_x32(float v) { v2f a = swap16(v, v); v = a.x + a.y; a = swap32(v, v); return a.x + a.y; }
__device__ __forceinline__ float max_x16_x32(float v) { v2f a = swap16(v, v); v = fmx(a.x, a.y); a = swap32(v, v); return fmx(a.x, a.y); }
__device__ __forceinline__ float row_sum(float v) { v += dppf<DPP_X1>(v); v += dppf<DPP_X2>(v); v += dppf<DPP_HMIR>(v); v += dppf<DPP_ROR8>(v); return v; }
__device__ __forceinline__ float oct_sum(float v) { v += dppf<DPP_X1>(v); v += dppf<DPP_X2>(v); v += dppf<DPP_HMIR>(v); return v; }
__device__ __forceinline__ float oct_max(float v) { v = fmx(v, dppf<DPP_X1>(v)); v = fmx(v, dppf<DPP_X2>(v)); v = fmx(v, dppf<DPP_HMIR>(v)); return v; }
__device__ __forceinline__ float wave_sum(float v) { return sum_x16_x32(row_sum(v)); }
__device__ __forceinline__ float treduce8(const float (&y)[8], bool bit3) {
    float z[4], u[2];
#pragma unroll
    for (int e = 0; e < 4; ++e) { v2f a = swap32(y[e], y[e + 4]); z[e] = a.x + a.y; }
#pragma unroll
    for (int e = 0; e < 2; ++e) { v2f a = swap16(z[e], z[e + 2]); u[e] = a.x + a.y; }
    float keep = bit3 ? u[1] : u[0], send = bit3 ? u[0] : u[1];
    return keep + dppf<DPP_ROR8>(send);
}
__device__ __forceinline__ v4f mfma16(v4u a, v4u b, v4f c) {
    return __builtin_amdgcn_mfma_f32_16x16x32_bf16(__builtin_bit_cast(v8bf, a), __builtin_bit_cast(v8bf, b), c, 0, 0, 0);
}
__device__ __forceinline__ void lds_barrier() { asm volatile("s_waitcnt lgkmcnt(0)\n\ts_barrier" ::: "memory"); }
#define SB() __builtin_amdgcn_sched_barrier(0)
#define VMCNT(n) (0x0F70 | ((n) & 15) | (((n) >> 4) << 14))
__device__ __forceinline__ __amdgpu_buffer_rsrc_t mkrsrc(const void* p, u32 bytes) { return __builtin_amdgcn_make_buffer_rsrc((void*)p, 0, bytes, 0x00020000); }
__device__ __forceinline__ u32 perm_lo(u32 hi, u32 lo) { return __builtin_amdgcn_perm(hi, lo, 0x05040100u); }
__device__ __forceinline__ u32 perm_hi(u32 hi, u32 lo) { return __builtin_amdgcn_perm(hi, lo, 0x07060302u); }
__device__ __forceinline__ bool all_lanes(bool p) { return __builtin_amdgcn_ballot_w64(!p) == 0ull; }
__device__ __forceinline__ void junk_load(__amdgpu_buffer_rsrc_t rs, int vo, u32 lds) {
    u32 keep;
    asm volatile("s_mov_b32 %0, m0\n\ts_mov_b32 m0, %3\n\ts_nop 0\n\tbuffer_load_ushort %1, %2, 0 offen lds\n\ts_mov_b32 m0, %0" : "=&s"(keep) : "v"(vo), "s"(rs), "s"(lds) : "memory");
}
#define LD128(r, vo, so, aux) __builtin_amdgcn_raw_buffer_load_b128(r, vo, so, aux)
#define ST128(d, r, vo, so, aux) __builtin_amdgcn_raw_buffer_store_b128(d, r, vo, so, aux)

struct __attribute__((aligned(16))) Smem {
    u32 xraw[4][256];
    u32 tagb[64];
    float opart[8 * 512];
    u32 junk[64];
    v4f coef[14][2][64];
    u32 xn[4][68];
    u32 qimg[256];
    v2f ml[4][8];
    float apart[8 * 64];
    float hbuf[512];
    v2f hst[8];
    float b2v; u32 pad0[3];
    float pad[9 * 1024];
};

__global__ void __launch_bounds__(576)
fused_decode_kernel(const u16* __restrict__ x, const u16* __restrict__ kc, const u16* __restrict__ vc,
                    const u16* __restrict__ wq, const u16* __restrict__ wo, const u16* __restrict__ w1,
                    const u16* __restrict__ b1, const u16* __restrict__ w2, const u16* __restrict__ b2,
                    u16* __restrict__ out, unsigned char* ws) {
    __shared__ Smem sm;
    unsigned long long pc0;
    asm volatile("s_getpc_b64 %0" : "=s"(pc0));
    STAMP(0);
    asm volatile("" :: "s"(x), "s"(wq), "s"(kc), "s"(ws));
    const int pid = blockIdx.x, l = threadIdx.x & 63;
    const int wave = __builtin_amdgcn_readfirstlane(threadIdx.x >> 6);
    const int s = pid & 7, g = (pid >> 3) & 7, b = pid >> 6;
    const int r = b * 8 + g, bid = r * 8 + s;
    const int qc = pid & 7, qj = (pid >> 3) & 7, kq = pid >> 6;
    const int n16 = l & 15, kg = l >> 4;
    const __amdgpu_buffer_rsrc_t r_ws = mkrsrc(ws, WS_END);
    const size_t kvbase = ((size_t)(b * 8 + g) * 8192 + 6144 + s * 256) * 128;
    const __amdgpu_buffer_rsrc_t r_v = mkrsrc(vc + kvbase, 65536);
    const __amdgpu_buffer_rsrc_t r_wo = mkrsrc(wo, 4u << 20);

    const __amdgpu_buffer_rsrc_t r_k = mkrsrc(kc + kvbase, 65536);
    const int w = wave - 1;
    const int koff = (n16 >> 2) * 256 + (4 * kg + (n16 & 3)) * 16;
    v4u kr[8];
    if (wave == 0) {
        __builtin_amdgcn_s_setprio(HPRIO);
        const __amdgpu_buffer_rsrc_t r_x = mkrsrc(x, 4096);
        const __amdgpu_buffer_rsrc_t r_wq = mkrsrc(wq, 4u << 20);
        u32 keep, so = kq * (128 * 8192) + qc * 1024 + qj * 128;
        const int ve = (l >> 3) * 8192 + ((l & 7) ^ ((l >> 3) & 2)) * 16;
#define WQL2 "s_add_u32 m0, m0, 0x400\n\ts_add_u32 %1, %1, 0x10000\n\tbuffer_load_dwordx4 %6, %7, %1 offen nt lds\n\t" \
             "s_add_u32 m0, m0, 0x400\n\ts_add_u32 %1, %1, 0x10000\n\tbuffer_load_dwordx4 %5, %7, %1 offen nt lds\n\t"
        asm volatile("s_mov_b32 %0, m0\n\ts_mov_b32 m0, %2\n\ts_nop 0\n\t"
                     "buffer_load_dwordx4 %3, %4, 0 offen lds\n\t"
                     "buffer_load_dwordx4 %3, %4, 0 offen offset:1024 lds\n\t"
                     "buffer_load_dwordx4 %3, %4, 0 offen offset:2048 lds\n\t"
                     "buffer_load_dwordx4 %3, %4, 0 offen offset:3072 lds\n\t"
                     "s_add_u32 m0, m0, 0x1000\n\ts_nop 0\n\t"
                     "buffer_load_dword off, %8, 0 sc1 lds\n\t"
                     "s_add_u32 m0, m0, 0x100\n\ts_nop 0\n\t"
                     "buffer_load_dwordx4 %5, %7, %1 offen nt lds\n\t"
                     WQL2 WQL2 WQL2 WQL2 WQL2 WQL2 WQL2
                     "s_add_u32 m0, m0, 0x400\n\ts_add_u32 %1, %1, 0x10000\n\tbuffer_load_dwordx4 %6, %7, %1 offen nt lds\n\t"
                     "s_mov_b32 m0, %0"
                     : "=&s"(keep), "+s"(so)
                     : "s"((u32)(size_t)(LAS void*)sm.xraw), "v"(l * 16), "s"(r_x), "v"(ve), "v"(ve ^ 64), "s"(r_wq), "s"(r_ws)
                     : "memory", "scc");
        SB();
        asm volatile("s_barrier" ::: "memory");
    } else {
        {
            const __amdgpu_buffer_rsrc_t r_c = mkrsrc((const void*)(pc0 & ~255ull), CODE_BYTES);
            junk_load(r_c, (w == 0 && l < 12) ? (qj * 12 + l) * 128 : OOB, (u32)(size_t)(LAS void*)sm.junk);
        }
        asm volatile("s_barrier" ::: "memory");
        if (KDELAY) __builtin_amdgcn_s_sleep(KDELAY);
        SB();
#pragma unroll
        for (int i = 0; i < WIN; ++i) kr[i] = LD128(r_k, koff, (i * 8 + w) * 1024, AUX_NT);
    }
    SB();
    int wave2 = wave;
    asm volatile("" : "+s"(wave2));

    if (wave2 == 0) {
        asm volatile("s_waitcnt vmcnt(16)" ::: "memory");
        STAMP(1);
        const u32 tag = sm.tagb[0] + 1u;
        v4u af[4];
        {
            const v4u own = *(const v4u*)&sm.xraw[kg][kq * 64 + n16 * 4];
            v2f xv[16];
#pragma unroll
            for (int u = 0; u < 4; ++u) {
                const v4u t = *(const v4u*)&sm.xraw[kg][u * 64 + n16 * 4];
#pragma unroll
                for (int q = 0; q < 4; ++q) xv[u * 4 + q] = bf2(t[q]);
            }
            v2f s2 = xv[0], s3 = xv[1];
#pragma unroll
            for (int i = 2; i < 16; i += 2) { s2 += xv[i]; s3 += xv[i + 1]; }
            s2 += s3;
            const float mean = row_sum(s2.x + s2.y) * (1.f / 512.f);
            const v2f mean2 = {mean, mean};
            v2f m2 = {0.f, 0.f}, m3 = {0.f, 0.f};
#pragma unroll
            for (int i = 0; i < 16; i += 2) {
                v2f d = xv[i] - mean2, e = xv[i + 1] - mean2;
                m2 = __builtin_elementwise_fma(d, d, m2); m3 = __builtin_elementwise_fma(e, e, m3);
            }
            m2 += m3;
            const float var = row_sum(m2.x + m2.y) * (1.f / 512.f);
            const float mb = rbf(mean);
            const float rden = __builtin_amdgcn_rcpf(rbf(__builtin_amdgcn_sqrtf(rbf(rbf(var) + 1e-5f))));
            const v2f mb2 = {mb, mb}, rd2 = {rden, rden};
            v4u o;
#pragma unroll
            for (int q = 0; q < 4; ++q) o[q] = pk2(bf2(pk2(bf2(own[q]) - mb2)) * rd2);
            *(v4u*)&sm.xn[kg][n16 * 4] = o;
#pragma unroll
            for (int t = 0; t < 4; ++t) af[t] = *(const v4u*)&sm.xn[l & 3][t * 16 + kg * 4];
        }
        STAMP(2);
        {
            asm volatile("s_waitcnt vmcnt(0)" ::: "memory");
            unsigned char* img = (unsigned char*)sm.opart + (kg * 8 + (n16 >> 2)) * 128 + (n16 & 3) * 8;
            const int sw = ((n16 >> 3) & 1) | ((kg & 1) << 1);
            v4f acc[4];
#pragma unroll
            for (int ct = 0; ct < 4; ++ct) {
                acc[ct] = (v4f){0.f, 0.f, 0.f, 0.f};
                LAS v4s* p = (LAS v4s*)(img + (ct ^ sw) * 32);
#pragma unroll
                for (int ks = 0; ks < 4; ++ks) {
                    v2u lo = __builtin_bit_cast(v2u, __builtin_amdgcn_ds_read_tr16_b64_v4i16(p + ks * 512));
                    v2u hi = __builtin_bit_cast(v2u, __builtin_amdgcn_ds_read_tr16_b64_v4i16(p + ks * 512 + 64));
                    v4u bf = {lo.x, lo.y, hi.x, hi.y};
                    acc[ct] = mfma16(af[ks], bf, acc[ct]);
                }
            }
            const bool c0 = (l & 16) != 0, c1 = (l & 32) != 0;
            const int qso = WS_Q + qc * 16384 + kq * 4096 + qj * 512;
            float v[4];
#pragma unroll
            for (int i = 0; i < 4; ++i) {
                const float a0 = acc[0][i], a1 = acc[1][i], a2 = acc[2][i], a3 = acc[3][i];
                const float lo = c0 ? a1 : a0, hi = c0 ? a3 : a2;
                v[i] = c1 ? hi : lo;
            }
            const bool odd = (l & 1) != 0;
#pragma unroll
            for (int i = 0; i < 4; i += 2) {
                const float rcv = dppf<DPP_X1>(odd ? v[i] : v[i + 1]);
                v4u gq = {f2u(odd ? rcv : v[i]), tag, f2u(odd ? v[i + 1] : rcv), tag};
                ST128(gq, r_ws, (l >> 1) * 16 + (l & 1) * (8 * 16384), qso + i * (8 * 16384), AUX_SC1);
            }
        }
        STAMP(3);
        SB();
        {
            const int qoff = WS_Q + (b * 8 + g) * 16384;
            if (QDELAY) __builtin_amdgcn_s_sleep(QDELAY);
            int vo[4] = {l * 16, l * 16 + 1024, l * 16 + 2048, l * 16 + 3072};
#pragma unroll 1
            for (int spin = 0; spin < SPIN_MAX; ++spin) {
                asm volatile("" ::: "memory");
                v4u p[4][4];
#pragma unroll
                for (int u = 0; u < 4; ++u)
#pragma unroll
                    for (int k = 0; k < 4; ++k) p[u][k] = LD128(r_ws, vo[u], qoff + k * 4096, AUX_SC1);
#pragma unroll
                for (int u = 0; u < 4; ++u) {
                    u32 bad = (u32)vo[u] & OOB;
#pragma unroll
                    for (int k = 0; k < 4; ++k) bad |= (p[u][k].y ^ tag) | (p[u][k].w ^ tag);
                    const float q0 = (u2f(p[u][0].x) + u2f(p[u][1].x)) + (u2f(p[u][2].x) + u2f(p[u][3].x));
                    const float q1 = (u2f(p[u][0].z) + u2f(p[u][1].z)) + (u2f(p[u][2].z) + u2f(p[u][3].z));
                    u32* dst = bad == 0 ? &sm.qimg[u * 64 + l] : &sm.junk[l];
                    *dst = pkbf(q0, q1);
                    vo[u] = bad == 0 ? OOB : vo[u];
                }
                if (all_lanes(((vo[0] & vo[1] & vo[2] & vo[3]) & OOB) != 0)) break;
            }
            STAMP(4);
            lds_barrier();
#pragma unroll
            for (int i = 0; i < WIN; ++i) asm volatile("" :: "v"(kr[i]));
            __builtin_amdgcn_s_setprio(0);
        }
        lds_barrier();
        {
            const v2f ml = sm.ml[(l >> 3) & 3][l & 7];
            const float M = oct_max(ml.x);
            const float L = oct_sum(ml.y * __builtin_amdgcn_exp2f((ml.x - M) * LOG2E));
            v4u gm = {f2u(M), tag, f2u(L), tag};
            ST128(gm, r_ws, ((l & 7) == 0 && l < 32) ? l * 2 : OOB, WS_ML + bid * 64, AUX_SC1);
        }
        SB();
        const __amdgpu_buffer_rsrc_t ra = mkrsrc(w1, 3584), rb = mkrsrc(b1, 3584), rc = mkrsrc(w2, 3584);
        v4u ta[3], tb[3], tc[3]; v2u ua, ub, uc;
#pragma unroll
        for (int i = 0; i < 3; ++i) { ta[i] = LD128(ra, l * 16 + i * 1024, 0, 0); tb[i] = LD128(rb, l * 16 + i * 1024, 0, 0); tc[i] = LD128(rc, l * 16 + i * 1024, 0, 0); }
        ua = __builtin_amdgcn_raw_buffer_load_b64(ra, l * 8 + 3072, 0, 0);
        ub = __builtin_amdgcn_raw_buffer_load_b64(rb, l * 8 + 3072, 0, 0);
        uc = __builtin_amdgcn_raw_buffer_load_b64(rc, l * 8 + 3072, 0, 0);
        const u32 b2l = __builtin_amdgcn_raw_buffer_load_b16(mkrsrc(b2, 2), 0, 0, 0);
        SB();
#pragma unroll
        for (int i = 0; i < 14; ++i) {
            const u32 a = i < 12 ? ta[i >> 2][i & 3] : ua[i & 1], c = i < 12 ? tb[i >> 2][i & 3] : ub[i & 1], d = i < 12 ? tc[i >> 2][i & 3] : uc[i & 1];
            const v2f wv = bf2(a), bv = bf2(c), cv = bf2(d);
            const v2f g1 = wv * cv, g0 = bv * cv;
            const v4f o0 = {wv.x, wv.y, bv.x, bv.y}, o1 = {g1.x, g1.y, g0.x, g0.y};
            sm.coef[i][0][l] = o0; sm.coef[i][1][l] = o1;
        }
        sm.b2v = bflo(b2l);
        lds_barrier();
        return;
    }

    v4u vr[8], wr[8];
    {
        typedef const unsigned long long __attribute__((address_space(4)))* kap;
        unsigned long long p = ((kap)__builtin_amdgcn_kernarg_segment_ptr())[w == 7 ? 2 : 4 + w];
        u32 off = w == 7 ? (u32)kvbase * 2u : 0u;
        off += w == 0 ? (u32)(g * 512 * 1024) : 0u;
        off += w == 6 ? WS_A : 0u;
        p += off;
        junk_load(mkrsrc((const void*)p, 2), l * 4, (u32)(size_t)(LAS void*)sm.junk);
    }
#pragma unroll
    for (int i = WIN; i < 8 + NV0; ++i) {
        SB();
        __builtin_amdgcn_s_waitcnt(VMCNT(WIN - 1));
        SB();
        if (i < 8) kr[i] = LD128(r_k, koff, (i * 8 + w) * 1024, AUX_NT);
        else vr[i - 8] = LD128(r_v, l * 16, ((i - 8) * 8 + w) * 1024, AUX_NT);
    }
    SB();
    STAMP(5);
    lds_barrier();
    STAMP(6);
#pragma unroll
    for (int i = NV0; i < NVY; ++i) vr[i] = LD128(r_v, l * 16, (i * 8 + w) * 1024, AUX_NT);
    SB();
    const u32 tag = sm.tagb[0] + 1u;
    float sc[8], mloc;
    {
        const v4u qf = *(const v4u*)&sm.qimg[(n16 & 3) * 64 + (4 * kg + (n16 >> 2)) * 4];
        const bool j0 = (l & 4) != 0, j1 = (l & 8) != 0;
#pragma unroll
        for (int i = 0; i < 8; ++i) {
            v4f c = {0.f, 0.f, 0.f, 0.f};
            c = mfma16(kr[i], qf, c);
            float dl = j0 ? c.y : c.x, dh = j0 ? c.w : c.z;
            float d = j1 ? dh : dl;
            d += dppf<DPP_ROR4>(d);
            d += dppf<DPP_ROR8>(d);
            sc[i] = d;
        }
        const v2f scale = {0.08838834764831845f, 0.08838834764831845f};
#pragma unroll
        for (int i = 0; i < 4; ++i) { v2f t = bf2(pk2(bf2(pkbf(sc[2 * i], sc[2 * i + 1])) * scale)); sc[2 * i] = t.x; sc[2 * i + 1] = t.y; }
        float m = fmx(fmx(fmx(sc[0], sc[1]), fmx(sc[2], sc[3])), fmx(fmx(sc[4], sc[5]), fmx(sc[6], sc[7])));
        mloc = max_x16_x32(m);
        const float mo = -mloc * LOG2E;
        float lsum = 0.f;
#pragma unroll
        for (int i = 0; i < 8; ++i) { sc[i] = __builtin_amdgcn_exp2f(__builtin_fmaf(sc[i], LOG2E, mo)); lsum += sc[i]; }
        lsum = sum_x16_x32(lsum);
        if (l < 4) { v2f t = {mloc, lsum}; sm.ml[l][w] = t; }
    }
    SB();
#pragma unroll
    for (int i = NVY; i < NV1; ++i) vr[i] = LD128(r_v, l * 16, (i * 8 + w) * 1024, AUX_NT);
    SB();
    lds_barrier();
    v4u pa[4];
    {
        const v4f* mp = (const v4f*)&sm.ml[l & 3][0];
        const v4f m0 = mp[0], m1 = mp[1], m2 = mp[2], m3 = mp[3];
        const float M = fmx(fmx(fmx(m0.x, m0.z), fmx(m1.x, m1.z)), fmx(fmx(m2.x, m2.z), fmx(m3.x, m3.z)));
        const float f = __builtin_amdgcn_exp2f((mloc - M) * LOG2E);
        v4u pp = {pkbf(sc[0] * f, sc[1] * f), pkbf(sc[2] * f, sc[3] * f), pkbf(sc[4] * f, sc[5] * f), pkbf(sc[6] * f, sc[7] * f)};
        const v4u z = {0u, 0u, 0u, 0u};
#pragma unroll
        for (int a = 0; a < 4; ++a) pa[a] = (n16 >> 2) == a ? pp : z;
    }
    STAMP(7);
    SB();
#pragma unroll
    for (int i = NV1; i < 8; ++i) vr[i] = LD128(r_v, l * 16, (i * 8 + w) * 1024, AUX_NT);
    const int hh = w >> 1, ch = w & 1;
    {
        const int wvo = (32 * kg + (n16 >> 3)) * 1024 + (n16 & 7) * 16;
        const int wso = (g * 512 + hh * 128 + 4 * ch) * 1024 + s * 128;
#pragma unroll
        for (int j = 0; j < 8; ++j) wr[j] = LD128(r_wo, wvo, wso + (8 * (j & 3) + 2 * (j >> 2)) * 1024, AUX_O);
    }
    const int mvo = (4 * (kg & 1) + (l & 3)) * 64;
    const int mso = WS_ML + r * 8 * 64 + hh * 16;
    v4u gm = LD128(r_ws, mvo, mso, AUX_SC1);
    SB();
    STAMP(8);

    {
        v4u vf[8];
#pragma unroll
        for (int p = 0; p < 4; ++p) {
#pragma unroll
            for (int q = 0; q < 4; ++q) {
                vf[2 * q][p] = perm_lo(vr[2 * p + 1][q], vr[2 * p][q]);
                vf[2 * q + 1][p] = perm_hi(vr[2 * p + 1][q], vr[2 * p][q]);
            }
            SB();
        }
        v4f a0 = {0.f, 0.f, 0.f, 0.f}, a1 = {0.f, 0.f, 0.f, 0.f};
#pragma unroll
        for (int a = 0; a < 4; ++a) { a0 = mfma16(pa[a], vf[a], a0); a1 = mfma16(pa[a], vf[4 + a], a1); }
        *(v4f*)&sm.opart[w * 512 + l * 4] = a0;
        *(v4f*)&sm.opart[w * 512 + 256 + l * 4] = a1;
    }
    STAMP(9);
    lds_barrier();
    {
        float v0 = sm.opart[w * 64 + l] + sm.opart[512 + w * 64 + l], v1 = sm.opart[1024 + w * 64 + l] + sm.opart[1536 + w * 64 + l];
        float v2 = sm.opart[2048 + w * 64 + l] + sm.opart[2560 + w * 64 + l], v3 = sm.opart[3072 + w * 64 + l] + sm.opart[3584 + w * 64 + l];
        const float v = (v0 + v1) + (v2 + v3);
        const u32 p = pkbf(v, dppf<DPP_SHL4>(v));
        const u32 p2 = dppu<DPP_SHL8>(p);
        v4u go = {p, tag, p2, tag};
        ST128(go, r_ws, (l & 12) == 0 ? (l & 3) * 512 + kg * 16 : OOB, WS_O + bid * 2048 + (w >> 2) * 256 + (w & 3) * 64, AUX_SC1);
    }
    STAMP(10);
    SB();

    v4u wf[8];
#pragma unroll
    for (int p = 0; p < 4; ++p)
#pragma unroll
        for (int q = 0; q < 4; ++q) {
            wf[2 * q][p] = perm_lo(wr[2 * p + 1][q], wr[2 * p][q]);
            wf[2 * q + 1][p] = perm_hi(wr[2 * p + 1][q], wr[2 * p][q]);
        }
    if (!all_lanes((gm.y == tag) & (gm.w == tag))) {
#pragma unroll 1
        for (int spin = 0; spin < SPIN_MAX; ++spin) {
            asm volatile("" ::: "memory");
            gm = LD128(r_ws, mvo, mso, AUX_SC1);
            if (all_lanes((gm.y == tag) & (gm.w == tag))) break;
        }
    }
    v2f W01, W23;
    {
        const float ms = u2f(gm.x), lv = u2f(gm.z);
        float mm = fmx(ms, dppf<DPP_X1>(ms)); mm = fmx(mm, dppf<DPP_X2>(mm));
        { v2f a = swap16(mm, mm); mm = fmx(a.x, a.y); }
        const float wg = __builtin_amdgcn_exp2f((ms - mm) * LOG2E);
        float L = wg * lv; L += dppf<DPP_X1>(L); L += dppf<DPP_X2>(L);
        { v2f a = swap16(L, L); L = a.x + a.y; }
        float W = wg * __builtin_amdgcn_rcpf(L);
        W = ((l >> 5) == ((l >> 3) & 1)) ? W : 0.f;
        W01.x = dppf<0x00>(W); W01.y = dppf<0x55>(W); W23.x = dppf<0xAA>(W); W23.y = dppf<0xFF>(W);
    }
    const int lp = (l & 32) + 4 * (l & 7) + ((l >> 3) & 3);
    const int fcol = 64 * w + 8 * (lp & 7) + (lp >> 3);
    float xres = bflo((u32)((const u16*)sm.xraw[b])[fcol]);
#pragma unroll
    for (int e = 0; e < 8; ++e) asm volatile("" : "+v"(wf[e]));
    asm volatile("" : "+v"(W01), "+v"(W23), "+v"(xres));
    SB();

    {
        const int ovo = (n16 & 7) * 2048 + ((n16 >> 3) * 4 + kg) * 16;
        const int oso = WS_O + r * 8 * 2048 + w * 256;
        v4u g0, g1;
#pragma unroll 1
        for (int spin = 0; spin < SPIN_MAX; ++spin) {
            asm volatile("" ::: "memory");
            g0 = LD128(r_ws, ovo, oso, AUX_SC1);
            g1 = LD128(r_ws, ovo + 128, oso, AUX_SC1);
            if (all_lanes((g0.y == tag) & (g0.w == tag) & (g1.y == tag) & (g1.w == tag))) break;
        }
        STAMP(11);
        const v4u av = {g0.x, g0.z, g1.x, g1.z};
        float y[8];
#pragma unroll
        for (int e = 0; e < 8; ++e) {
            v4f z = {0.f, 0.f, 0.f, 0.f};
            const v4f c = mfma16(av, wf[e], z);
            const v2f c01 = {c.x, c.y}, c23 = {c.z, c.w};
            const v2f t = __builtin_elementwise_fma(c23, W23, c01 * W01);
            y[e] = t.x + t.y;
        }
        sm.apart[w * 64 + l] = treduce8(y, (l & 8) != 0);
    }
    lds_barrier();
    if (w == 0) {
        float v0 = sm.apart[l] + sm.apart[64 + l], v1 = sm.apart[128 + l] + sm.apart[192 + l];
        float v2 = sm.apart[256 + l] + sm.apart[320 + l], v3 = sm.apart[384 + l] + sm.apart[448 + l];
        const float v = (v0 + v1) + (v2 + v3);
        const u32 p = pkbf(v, dppf<DPP_X1>(v));
        const u32 p2 = dppu<DPP_X2>(p);
        v4u ga = {p, tag, p2, tag};
        ST128(ga, r_ws, (l & 3) == 0 ? l * 4 : OOB, WS_A + bid * 256, AUX_SC1);
    }
    STAMP(12);
    SB();
    v4f cf0[14], cf1[14];
#pragma unroll
    for (int i = 0; i < 14; ++i) { cf0[i] = sm.coef[i][0][l]; cf1[i] = sm.coef[i][1][l]; }
#pragma unroll
    for (int i = 0; i < 14; ++i) asm volatile("" : "+v"(cf0[i]), "+v"(cf1[i]));
    SB();

    {
        const int avo = (l >> 3) * (8 * 256) + (l & 7) * 16;
        const int aso = WS_A + (b * 64 + w) * 256;
        v4u g0, g1;
#pragma unroll 1
        for (int spin = 0; spin < SPIN_MAX; ++spin) {
            asm volatile("" ::: "memory");
            g0 = LD128(r_ws, avo, aso, AUX_SC1);
            g1 = LD128(r_ws, avo + 128, aso, AUX_SC1);
            if (all_lanes((g0.y == tag) & (g0.w == tag) & (g1.y == tag) & (g1.w == tag))) break;
        }
        STAMP(13);
        float y[8] = {bflo(g0.x), bfhi(g0.x), bflo(g0.z), bfhi(g0.z), bflo(g1.x), bfhi(g1.x), bflo(g1.z), bfhi(g1.z)};
        const float at = treduce8(y, (l & 8) != 0);
        const float h = rbf(xres + rbf(at));
        sm.hbuf[fcol] = h;
        const float h0 = u2f(__builtin_amdgcn_readfirstlane(f2u(h)));
        const float d = h - h0;
        const float sd = wave_sum(d), sdd = wave_sum(d * d);
        if (l == 0) { v2f t = {64.f * h0 + sd, sdd - sd * sd * (1.f / 64.f)}; sm.hst[w] = t; }
    }
    lds_barrier();
    STAMP(14);
    {
        const int jj = g * 8 + s;
        const v2f st = sm.hst[l & 7];
        const float hown = sm.hbuf[jj * 8 + w], b2v = sm.b2v;
        const float mean = oct_sum(st.x) * (1.f / 512.f);
        const float dm = st.x * (1.f / 64.f) - mean;
        const float var = fmx(oct_sum(__builtin_fmaf(64.f * dm, dm, st.y)) * (1.f / 512.f), 0.f);
        const float mb = rbf(mean);
        const float rden = __builtin_amdgcn_rcpf(rbf(__builtin_amdgcn_sqrtf(rbf(rbf(var) + 1e-5f))));
        const float mean2 = (mean - mb) * rden, var2 = var * rden * rden;
        const float rden2 = __builtin_amdgcn_rcpf(rbf(__builtin_amdgcn_sqrtf(rbf(rbf(var2) + 1e-5f))));
        const float n1 = rbf(rbf(hown - mb) * rden);
        const float tt = rbf(rbf(n1 - rbf(mean2)) * rden2);
        const v2f t2 = {tt, tt};
        const float K0 = -2.0f * LOG2E * 0.7978845608028654f, K1 = K0 * 0.044715f;
        const v2f k0 = {K0, K0}, k1 = {K1, K1}, one = {1.f, 1.f};
        v2f acc = {0.f, 0.f};
#pragma unroll
        for (int i = 0; i < 14; ++i) {
            const v2f cw = {cf0[i].x, cf0[i].y}, cb = {cf0[i].z, cf0[i].w}, g1 = {cf1[i].x, cf1[i].y}, g0 = {cf1[i].z, cf1[i].w};
            v2f u = __builtin_elementwise_fma(t2, cw, cb);
            v2f a = u * __builtin_elementwise_fma(u * u, k1, k0);
            v2f e = {__builtin_amdgcn_exp2f(a.x), __builtin_amdgcn_exp2f(a.y)};
            e += one;
            v2f rc = {__builtin_amdgcn_rcpf(e.x), __builtin_amdgcn_rcpf(e.y)};
            acc = __builtin_elementwise_fma(__builtin_elementwise_fma(t2, g1, g0), rc, acc);
        }
        const float yv = rbf(rbf(wave_sum(acc.x + acc.y)) + b2v);
        if (l == 0) out[b * 512 + jj * 8 + w] = (u16)pkbf(hown + yv, 0.f);
        if (bid == 0 && w == 0 && l == 0) __builtin_amdgcn_raw_buffer_store_b32(tag, r_ws, 0, WS_EPOCH, AUX_SC1);
    }
    STAMP(15);
}

extern "C" void kernel_launch(void* const* d_in, const int* in_sizes, int n_in, void* d_out, int out_size, void* d_ws, size_t ws_size, hipStream_t stream) {
    fused_decode_kernel<<<256, 576, 0, stream>>>((const u16*)d_in[0], (const u16*)d_in[1], (const u16*)d_in[2], (const u16*)d_in[3], (const u16*)d_in[4],
                                                 (const u16*)d_in[6], (const u16*)d_in[7], (const u16*)d_in[8], (const u16*)d_in[9],
                                                 (u16*)d_out, (unsigned char*)d_ws);
}
```

```cpp
#include <hip/hip_runtime.h>
#include <stdint.h>

typedef unsigned u32;
typedef unsigned short u16;
typedef u32 v4u __attribute__((ext_vector_type(4)));
typedef u32 v2u __attribute__((ext_vector_type(2)));
typedef float v4f __attribute__((ext_vector_type(4)));
typedef float v2f __attribute__((ext_vector_type(2)));
typedef __bf16 v8bf __attribute__((ext_vector_type(8)));
typedef __bf16 v2bf __attribute__((ext_vector_type(2)));
typedef short v4s __attribute__((ext_vector_type(4)));
#define LAS __attribute__((address_space(3)))

#define STAMP(i) do { } while (0)
#define PUTST(a, b) do { } while (0)

#define MAPG 0
#define WIN 12
#define QP 10
#define WOP 32
#define XP 0
#define PRE 0
#define TN 16
#define TWAIT 0
#define WOE 4
#define GDELAY 10
#define KFIRST 0
#define OST16 0
#define AST16 1
#define ODELAY 0
#define ADELAY 0
#define ROTA 6
#define ROTB 3
#define SPREAD 0
#define WSOFF 0
#define ILV 0
#define FINM 0
#define FINC 0
#define V5FMT 4

constexpr int AUX_SC1 = 16;
constexpr u32 WS_EPOCH = 0;
constexpr u32 GQ = 32768, GO = 32768, GML = 2048, GA = 32768;
constexpr u32 WS_Q = 4096 + WSOFF;
constexpr u32 WS_O = WS_Q + 32 * 8192;
constexpr u32 WS_ML = WS_O + 262144;
constexpr u32 WS_A = WS_ML + 16384;
constexpr u32 WS_T = WS_A + 131072;
constexpr u32 WS_END = WS_T + 16384;
constexpr u32 L_V = 0, L_K = 0x10000, L_W = 0x20000, L_TN = 0x24000, L_X = 0x25000, L_XN = 0x26000, L_QI = 0x26500, L_P = 0x26A00,
              L_TP = 0x27200, L_JUNK = 0x27600, L_FL = 0x27A00, L_M = 0x27A80, L_LS = 0x27AC0, L_ST = 0x27B00, L_END = 0x27B40;
constexpr u32 F_A = L_FL, F_Q = L_FL + 16, F_M = L_FL + 32, F_P = L_FL + 48, F_T = L_FL + 64, F_L = L_FL + 80, F_ARR = L_FL + 88, F_FB = L_FL + 92, F_W = L_FL + 96;
constexpr u32 TAG_STEP = 0x01010101u;
constexpr u32 L_TAB = 0xCE00;
constexpr u32 L_OS = L_K, L_FB = L_K + 0x2000, L_FP = L_K + 0x6000;
constexpr u32 T_LO = 114 * 128;
constexpr int SPIN_MAX = 1 << 18;
constexpr u32 CODE_BYTES = 11776;
constexpr int OOB = 0x40000000;
constexpr float LOG2E = 1.4426950408889634f;

__device__ __forceinline__ float u2f(u32 v) { return __builtin_bit_cast(float, v); }
__device__ __forceinline__ u32 f2u(float v) { return __builtin_bit_cast(u32, v); }
__device__ __forceinline__ float bflo(u32 v) { return u2f(v << 16); }
__device__ __forceinline__ float bfhi(u32 v) { return u2f(v & 0xffff0000u); }
__device__ __forceinline__ v2f bf2(u32 v) { v2f r = {bflo(v), bfhi(v)}; return r; }
__device__ __forceinline__ u32 pk2(v2f f) { return __builtin_bit_cast(u32, __builtin_convertvector(f, v2bf)); }
__device__ __forceinline__ u32 pkbf(float a, float b) { v2f f = {a, b}; return pk2(f); }
__device__ __forceinline__ float rbf(float a) { return bflo(pkbf(a, a)); }
template <int CTRL> __device__ __forceinline__ float dppf(float v) { return u2f((u32)__builtin_amdgcn_update_dpp(0, (int)f2u(v), CTRL, 0xf, 0xf, true)); }
template <int CTRL> __device__ __forceinline__ u32 dppu(u32 v) { return (u32)__builtin_amdgcn_update_dpp(0, (int)v, CTRL, 0xf, 0xf, true); }
constexpr int DPP_X1 = 0xB1, DPP_X2 = 0x4E, DPP_HMIR = 0x141, DPP_ROR4 = 0x124, DPP_ROR8 = 0x128, DPP_SHL4 = 0x104, DPP_SHL8 = 0x108;
__device__ __forceinline__ float fmx(float a, float b) { return __builtin_fmaxf(a, b); }
__device__ __forceinline__ u32 umn(u32 a, u32 b) { return a < b ? a : b; }
__device__ __forceinline__ u32 umx(u32 a, u32 b) { return a > b ? a : b; }
__device__ __forceinline__ u32 umx4(v4u a) { return umx(umx(a.x, a.y), umx(a.z, a.w)); }
__device__ __forceinline__ v2f swap32(float a, float b) {
    auto r = __builtin_amdgcn_permlane32_swap(f2u(a), f2u(b), false, false);
    v2f o = {u2f(r[0]), u2f(r[1])}; return o;
}
__device__ __forceinline__ v2f swap16(float a, float b) {
    auto r = __builtin_amdgcn_permlane16_swap(f2u(a), f2u(b), false, false);
    v2f o = {u2f(r[0]), u2f(r[1])}; return o;
}
__device__ __forceinline__ float sum_x16_x32(float v) { v2f a = swap16(v, v); v = a.x + a.y; a = swap32(v, v); return a.x + a.y; }
__device__ __forceinline__ float max_x16_x32(float v) { v2f a = swap16(v, v); v = fmx(a.x, a.y); a = swap32(v, v); return fmx(a.x, a.y); }
__device__ __forceinline__ float row_sum(float v) { v += dppf<DPP_X1>(v); v += dppf<DPP_X2>(v); v += dppf<DPP_HMIR>(v); v += dppf<DPP_ROR8>(v); return v; }
__device__ __forceinline__ float wave_sum(float v) { return sum_x16_x32(row_sum(v)); }
__device__ __forceinline__ v4f mfma16(v4u a, v4u b, v4f c) {
    return __builtin_amdgcn_mfma_f32_16x16x32_bf16(__builtin_bit_cast(v8bf, a), __builtin_bit_cast(v8bf, b), c, 0, 0, 0);
}
__device__ __forceinline__ void lds_barrier() { asm volatile("s_waitcnt lgkmcnt(0)\n\ts_barrier" ::: "memory"); }
#define SB() __builtin_amdgcn_sched_barrier(0)
#define WAITVM(n) asm volatile("s_waitcnt vmcnt(%0)" :: "i"(n) : "memory")
__device__ __forceinline__ __amdgpu_buffer_rsrc_t mkrsrc(const void* p, u32 bytes) { return __builtin_amdgcn_make_buffer_rsrc((void*)p, 0, bytes, 0x00020000); }
__device__ __forceinline__ bool all_lanes(bool p) { return __builtin_amdgcn_ballot_w64(!p) == 0ull; }
__device__ __forceinline__ void junk_load(__amdgpu_buffer_rsrc_t rs, int vo, u32 lds) {
    u32 keep;
    asm volatile("s_mov_b32 %0, m0\n\ts_mov_b32 m0, %3\n\ts_nop 0\n\tbuffer_load_ushort %1, %2, 0 offen lds\n\ts_mov_b32 m0, %0" : "=&s"(keep) : "v"(vo), "s"(rs), "s"(lds) : "memory");
}
#define LD128(r, vo, so, aux) __builtin_amdgcn_raw_buffer_load_b128(r, vo, so, aux)
#define ST128(d, r, vo, so, aux) do { v4u d_ = (d); asm volatile("" : "+v"(d_)); __builtin_amdgcn_raw_buffer_store_b128(d_, r, vo, so, aux); asm volatile("s_nop 1" :: "v"(d_)); } while (0)
template <typename T> __device__ __forceinline__ LAS T* lp(u32 a) { return (LAS T*)(size_t)a; }
__device__ __forceinline__ u32 pin(u32 v) { asm volatile("" : "+v"(v)); return v; }
__device__ __forceinline__ v4u trfrag(u32 a) {
    const v2u lo = __builtin_bit_cast(v2u, __builtin_amdgcn_ds_read_tr16_b64_v4i16(lp<v4s>(a)));
    const v2u hi = __builtin_bit_cast(v2u, __builtin_amdgcn_ds_read_tr16_b64_v4i16(lp<v4s>(a + 2048)));
    v4u r = {lo.x, lo.y, hi.x, hi.y}; return r;
}
__device__ __forceinline__ void set_flag(u32 a, u32 v) { asm volatile("" ::: "memory"); *lp<volatile u32>(a) = v; }
__device__ __forceinline__ u32 umn4(v4u a) { return umn(umn(a.x, a.y), umn(a.z, a.w)); }
__device__ __forceinline__ bool uni_ge(u32 x, u32 v) { return (u32)__builtin_amdgcn_readfirstlane(x) >= v; }
template <int N, int SLP> __device__ __forceinline__ void wait_flag(u32 a, u32 v) {
#pragma unroll 1
    for (int i = 0; i < SPIN_MAX; ++i) {
        u32 m;
        if (N == 1) m = *lp<volatile u32>(a);
        else if (N == 2) { const v2u f = *lp<volatile v2u>(a); m = umn(f.x, f.y); }
        else if (N == 3) { const v4u f = *lp<volatile v4u>(a); m = umn(umn(f.x, f.y), f.z); }
        else m = umn4(*lp<volatile v4u>(a));
        if (uni_ge(m, v)) break;
        if (SLP) __builtin_amdgcn_s_sleep(SLP);
    }
    asm volatile("" ::: "memory");
}
struct Coef { v2f w[4], b[4], g1[4], g0[4]; };
__device__ __forceinline__ float mlp_part(const Coef& c, float t) {
    const v2f t2 = {t, t};
    const float K0 = -2.0f * LOG2E * 0.7978845608028654f, K1 = K0 * 0.044715f;
    const v2f k0 = {K0, K0}, k1 = {K1, K1}, one = {1.f, 1.f};
    v2f acc = {0.f, 0.f};
#pragma unroll
    for (int i = 0; i < 4; ++i) {
        const v2f u = __builtin_elementwise_fma(t2, c.w[i], c.b[i]);
        const v2f a = u * __builtin_elementwise_fma(u * u, k1, k0);
        v2f e = {__builtin_amdgcn_exp2f(a.x), __builtin_amdgcn_exp2f(a.y)};
        e += one;
        const v2f rc = {__builtin_amdgcn_rcpf(e.x), __builtin_amdgcn_rcpf(e.y)};
        acc = __builtin_elementwise_fma(__builtin_elementwise_fma(t2, c.g1[i], c.g0[i]), rc, acc);
    }
    return acc.x + acc.y;
}

__device__ __forceinline__ v4u load_part(const u16* p, int part, int l) { return LD128(mkrsrc(p + part * 448, 896), l * 16, 0, 0); }
__device__ __forceinline__ void make_coef(Coef& cf, v4u ta, v4u tb, v4u tc) {
#pragma unroll
    for (int i = 0; i < 4; ++i) { const v2f c = bf2(tc[i]); cf.w[i] = bf2(ta[i]); cf.b[i] = bf2(tb[i]); cf.g1[i] = cf.w[i] * c; cf.g0[i] = cf.b[i] * c; }
}

#define DESC(T, p) T p, u32, u32
__global__ void __launch_bounds__(640)
fused_decode_kernel(DESC(const u16*, x), DESC(const u16*, wq), DESC(const u16*, kc), DESC(const u16*, vc), DESC(unsigned char*, ws),
                    DESC(const u16*, w1), DESC(const u16*, b1), DESC(const u16*, w2), DESC(const u16*, b2), const u16* wo, u16* out) {
    __shared__ __attribute__((aligned(16))) unsigned char smem[L_END];
    STAMP(0);
    u32 tag0, tag, b2r;
    v4u ca, cb, cc;
    int wave2;
    const int pid = blockIdx.x;
    {
        u32 t0, t1, t2, t3;
#define WQL "s_add_u32 m0, m0, 0x400\n s_add_u32 s73, s73, 0x10000\n buffer_load_dwordx4 %[t3], s[40:43], s73 offen nt lds\n"
#define KVL "buffer_load_dwordx4 %[t3], s[44:47], s73 offen nt lds\n s_add_u32 m0, m0, 0x400\n s_add_u32 s73, s73, 0x800\n"
        asm volatile(
            "s_load_dwordx16 s[36:51], %[ka], 0x0\n"
            "s_load_dwordx16 s[52:67], %[ka], 0x40\n"
            "s_getpc_b64 s[76:77]\n"
            "v_readfirstlane_b32 %[wv], %[tid]\n"
            "v_bfe_u32 %[t0], %[tid], 3, 3\n"
            "v_and_b32 %[t1], 6, %[t0]\n"
            "v_xor_b32 %[t1], %[t1], %[tid]\n"
            "v_and_b32 %[t1], 7, %[t1]\n"
            "v_lshlrev_b32 %[t1], 4, %[t1]\n"
            "s_lshr_b32 %[wv], %[wv], 6\n"
            "s_and_b32 s72, %[pid], 7\n"
            "s_cmp_gt_u32 %[wv], 3\n"
            "s_cbranch_scc1 2f\n"
            "s_setprio 2\n"
            "v_lshlrev_b32 %[t2], 4, %[tid]\n"
            "v_lshl_or_b32 %[t3], %[t0], 13, %[t1]\n"
            "s_lshr_b32 s75, %[pid], 6\n"
            "s_lshl_b32 s73, s75, 2\n"
            "s_add_u32 s73, s73, %[wv]\n"
            "s_lshl_b32 s73, s73, 11\n"
            "s_and_b32 s74, %[pid], 63\n"
            "s_or_b32 s73, s73, s74\n"
            "s_lshl_b32 s73, s73, 7\n"
            "s_lshl_b32 s74, %[wv], 10\n"
            "s_add_u32 m0, s74, %[lx]\n"
            "s_lshl_b32 s74, s74, 2\n"
            "s_add_u32 s74, s74, %[lw]\n"
            "v_bfe_u32 %[t0], %[tid], 4, 2\n"
            "v_and_b32 %[t1], 15, %[tid]\n"
            "v_lshlrev_b32 %[t1], 2, %[t1]\n"
            "v_lshl_or_b32 %[t0], %[t0], 9, %[t1]\n"
            "s_and_b32 s78, %[pid], 56\n"
            "s_lshl1_add_u32 s78, %[wv], s78\n"
            "s_lshl1_add_u32 s78, s78, s75\n"
            "s_lshl_b32 s78, s78, 5\n"
            "s_or_b32 s78, s78, s72\n"
            "s_lshl_b32 s78, s78, 6\n"
            "s_add_u32 s78, s78, %[wso]\n"
            "v_mov_b32 %[t1], -1\n"
            "s_waitcnt lgkmcnt(0)\n"
            "buffer_load_dwordx4 %[t2], s[36:39], 0 offen lds\n"
            "s_mov_b32 m0, s74\n"
            "s_nop 0\n"
            "buffer_load_dwordx4 %[t3], s[40:43], s73 offen nt lds\n"
            WQL WQL WQL
            "buffer_load_dword %[tag], off, s[52:55], 0\n"
            "buffer_store_dword %[t1], %[t0], s[52:55], s78 offen sc1\n"
            "s_barrier\n"
            "s_branch 9f\n"
            "2:\n"
            "s_cmp_gt_u32 %[wv], 5\n"
            "s_cbranch_scc1 5f\n"
            "s_setprio 3\n"
            "s_sub_u32 s75, %[wv], 4\n"
            "v_lshl_or_b32 %[t3], %[t0], 8, %[t1]\n"
            "s_lshr_b32 s74, %[pid], 3\n"
            "s_mul_i32 s73, s74, %[rotb]\n"
            "s_mul_i32 s78, s72, %[rota]\n"
            "s_add_u32 s73, s73, s78\n"
            "s_lshl_b32 s73, s73, 5\n"
            "s_or_b32 s73, s73, s75\n"
            "s_lshl_b32 s73, s73, 7\n"
            "s_lshl_b32 s74, s74, 5\n"
            "s_or_b32 s74, s74, s72\n"
            "s_or_b32 s74, s74, 0x18\n"
            "s_pack_ll_b32_b16 s73, s73, s74\n"
            "s_lshl_b32 s78, s75, 15\n"
            "s_add_u32 m0, s78, %[lk]\n"
            "s_lshl2_add_u32 s75, s75, %[fl]\n"
            "v_mov_b32 %[t0], s75\n"
            "v_mov_b32 %[t1], 0\n"
            "s_mov_b32 s75, 0\n"
            "s_waitcnt lgkmcnt(0)\n"
            "s_barrier\n"
            "3:\n"
            "s_waitcnt vmcnt(%[win])\n"
            KVL KVL
            "s_pack_ll_b32_b16 s73, s73, s74\n"
            "ds_write_b32 %[t0], %[t1]\n"
            "s_add_u32 s75, s75, 1\n"
            "v_add_u32 %[t1], 1, %[t1]\n"
            "s_cmp_lg_u32 s75, 16\n"
            "s_cbranch_scc1 4f\n"
            "s_mov_b64 s[44:45], s[48:49]\n"
            "s_mov_b32 m0, s78\n"
            "4:\n"
            "s_cmp_lg_u32 s75, 32\n"
            "s_cbranch_scc1 3b\n"
            "ds_write_b32 %[t0], %[t1]\n"
            "s_branch 9f\n"
            "5:\n"
            "s_load_dwordx4 s[68:71], %[ka], 0x80\n"
            "s_sub_u32 s75, %[wv], 6\n"
            "s_and_b32 s74, %[pid], 56\n"
            "s_lshl1_add_u32 s74, s75, s74\n"
            "s_lshl_b32 s74, s74, 8\n"
            "s_add_u32 s78, s74, 512\n"
            "s_min_u32 s78, s78, %[code]\n"
            "s_mov_b32 s79, 0x20000\n"
            "s_and_b32 s76, s76, 0xffffff00\n"
            "v_and_b32 %[t2], 63, %[tid]\n"
            "v_lshl_add_u32 %[t3], %[t2], 7, s74\n"
            "buffer_load_dword %[b2r], %[t3], s[76:79], 0 offen\n"
            "v_and_b32 %[t0], 31, %[tid]\n"
            "v_lshlrev_b32 %[t0], 2, %[t0]\n"
            "v_add_u32 %[t0], %[lfl], %[t0]\n"
            "v_mov_b32 %[t1], 0\n"
            "ds_write_b32 %[t0], %[t1]\n"
            "s_mulk_i32 s75, 896\n"
            "v_lshl_add_u32 %[t3], %[t2], 4, s75\n"
            "v_cmp_gt_u32 vcc, 56, %[t2]\n"
            "v_cndmask_b32 %[t3], 2.0, %[t3], vcc\n"
            "s_waitcnt lgkmcnt(0)\n"
            "s_barrier\n"
            "buffer_load_dwordx4 %[ca], %[t3], s[56:59], 0 offen\n"
            "buffer_load_dwordx4 %[cb], %[t3], s[60:63], 0 offen\n"
            "buffer_load_dwordx4 %[cc], %[t3], s[64:67], 0 offen\n"
            "buffer_load_dword %[tag], off, s[52:55], 0\n"
            "buffer_load_ushort %[b2r], off, s[68:71], 0\n"
            "9:\n"
            : [wv] "=&s"(wave2), [tag] "=&v"(tag0), [b2r] "=&v"(b2r), [ca] "=&v"(ca), [cb] "=&v"(cb), [cc] "=&v"(cc),
              [t0] "=&v"(t0), [t1] "=&v"(t1), [t2] "=&v"(t2), [t3] "=&v"(t3)
            : [ka] "s"(__builtin_amdgcn_kernarg_segment_ptr()), [pid] "s"(pid), [tid] "v"(threadIdx.x), "s"((u32)(size_t)(LAS void*)smem),
              [lx] "i"(L_X), [lw] "i"(L_W), [lk] "i"(L_K), [fl] "i"(F_L), [lfl] "i"(L_FL), [wso] "i"(WS_O), [code] "i"(CODE_BYTES), [win] "i"(WIN - 2), [rota] "i"(ROTA), [rotb] "i"(ROTB)
            : "memory", "scc", "vcc", "s36", "s37", "s38", "s39", "s40", "s41", "s42", "s43", "s44", "s45", "s46", "s47", "s48", "s49", "s50", "s51", "s52", "s53", "s54", "s55", "s56", "s57", "s58", "s59", "s60", "s61", "s62", "s63", "s64", "s65", "s66", "s67", "s68", "s69", "s70", "s71", "s72", "s73", "s74", "s75", "s76", "s77", "s78", "s79");
        static_assert(L_V == 0 && OOB == 0x40000000 && F_ARR == F_L + 8, "");
    }
    SB();
    const int l = threadIdx.x & 63;
    const int s = pid & 7, g = (pid >> 3) & 7, b = pid >> 6;
    const int n16 = l & 15, kg = l >> 4, lr = l >> 3;
    const int swz = ((l & 7) ^ (lr & 6)) * 16;
    const __amdgpu_buffer_rsrc_t r_ws = mkrsrc(ws, WS_END);
    const u32 fra = (kg * 4 + (n16 >> 2)) * 128 + (((kg & 1) * 2 + (n16 >> 3)) * 32) + (n16 & 3) * 8;
    const bool fin = (pid & 63) == FINM * b + FINC;

    if (wave2 >= 4 && wave2 < 6) {
        STAMP(7);
        const int dh = wave2 - 4;
        const u32 lv = L_V + dh * 32768, fl = F_L + dh * 4;
        const u32 pa = pin(L_P + kg * 64 + (n16 >> 2) * 16);
        u32 va[4];
#pragma unroll
        for (int t = 0; t < 4; ++t) va[t] = pin((lv + fra) ^ (t * 32));
        const int ovo = pin(kg * 8192 + (n16 >> 3) * 512 + (n16 & 7) * 8);
        const int oso = WS_O + g * GO + b * 2048 + dh * 1024 + s * 64;
        wait_flag<4, 1>(F_P, 1);
        STAMP(16);
        v4u pf[8];
#pragma unroll
        for (int st = 0; st < 8; ++st) pf[st] = *lp<v4u>(pa + st * 256);
        v4f acc[4];
#pragma unroll
        for (int t = 0; t < 4; ++t) acc[t] = (v4f){0.f, 0.f, 0.f, 0.f};
        static_assert(WIN <= 16 && WIN % 4 == 0, "");
        constexpr int ST0 = 8 - WIN / 4;
#pragma unroll
        for (int st = 0; st < ST0; ++st)
#pragma unroll
            for (int t = 0; t < 4; ++t) acc[t] = mfma16(pf[st], trfrag(va[t] + st * 4096), acc[t]);
        SB();
        STAMP(17);
#pragma unroll
        for (int st = ST0; st < 8; ++st) {
            v2u lo[4], hi[4];
            WAITVM(30 - 4 * st);
#pragma unroll
            for (int t = 0; t < 4; ++t) lo[t] = __builtin_bit_cast(v2u, __builtin_amdgcn_ds_read_tr16_b64_v4i16(lp<v4s>(va[t] + st * 4096)));
            WAITVM(28 - 4 * st);
            if (st == 7) STAMP(8);
#pragma unroll
            for (int t = 0; t < 4; ++t) hi[t] = __builtin_bit_cast(v2u, __builtin_amdgcn_ds_read_tr16_b64_v4i16(lp<v4s>(va[t] + st * 4096 + 2048)));
#pragma unroll
            for (int t = 0; t < 4; ++t) { const v4u bf = {lo[t].x, lo[t].y, hi[t].x, hi[t].y}; acc[t] = mfma16(pf[st], bf, acc[t]); }
            SB();
        }
        const v2u go = {umn(pkbf(acc[0].x, acc[1].x), ~1u), umn(pkbf(acc[2].x, acc[3].x), ~1u)};
        __builtin_amdgcn_raw_buffer_store_b64(go, r_ws, ovo, oso, AUX_SC1);
        set_flag(fl, 33);
        STAMP(9);
        if (dh == 0) { PUTST(7, 10); PUTST(16, 18); }
        __builtin_amdgcn_endpgm();
    }

    if (wave2 >= 6) {
        const int tw = wave2 - 6;
        const int tj = s * 32 + g * 4 + b;
        const v4u ones = {~0u, ~0u, ~0u, ~0u};
        ST128(ones, r_ws, l < 8 ? l * 16 : OOB, WS_A + tw * GA + (s * 32 + g * 4 + b) * 128, AUX_SC1);
        {
            const u16* p = tw == 0 ? vc + ((size_t)(b * 8 + g) * 8192 + 6144 + s * 256) * 128 : tw == 1 ? wo + (g * 4 + b) * 65536 : out;
            junk_load(mkrsrc(p, tw == 3 ? 0 : 2), l * 4, L_JUNK);
        }
        if (TWAIT) wait_flag<4, 4>(F_A, TWAIT);
        asm volatile("s_waitcnt vmcnt(2)" : "+v"(ca), "+v"(cb), "+v"(cc), "+v"(tag0), "+v"(b2r) :: "memory");
        SB();
        Coef cf;
        make_coef(cf, ca, cb, cc);
        tag = tag0 + TAG_STEP;
        {
            float t = u2f((u32)(((tj & 128) << 8) + T_LO + (tj & 127)) << 16);
            u32 ta = n16 == 0 ? L_TP + tw * 16 + kg * 4 : L_JUNK;
#pragma unroll 1
            for (int e = 0; e < TN; ++e) {
                *lp<float>(ta) = row_sum(mlp_part(cf, t));
                ta += n16 == 0 ? 64 : 0;
                t += t;
            }
        }
        WAITVM(0);
        {
            u32 arr = 0;
            if (l == 0) arr = __hip_atomic_fetch_add(lp<u32>(F_ARR), 9u, __ATOMIC_RELAXED, __HIP_MEMORY_SCOPE_WORKGROUP);
            asm volatile("" ::: "memory");
            if (__builtin_amdgcn_readfirstlane(arr) == 27) {
                const v4f p = *lp<v4f>(L_TP + l * 16);
                float v = (p.x + p.y) + (p.z + p.w);
                v += dppf<DPP_X1>(v); v += dppf<DPP_X2>(v);
                v = rbf(v) + bflo(b2r);
                const u32 p0 = pkbf(v, dppf<DPP_SHL4>(v));
                const v4u gt = {p0, tag, dppu<DPP_SHL8>(p0), tag};
                ST128(gt, r_ws, n16 == 0 ? kg * 16 : OOB, WS_T + tj * 64, AUX_SC1);
            }
        }
        STAMP(10);
        if (tw == 0) PUTST(10, 11);
        if (!fin) __builtin_amdgcn_endpgm();
        if (GDELAY) __builtin_amdgcn_s_sleep(GDELAY);
        v4u t[4];
        bool bad = true;
#pragma unroll 1
        for (int spin = 0; spin < SPIN_MAX; ++spin) {
            asm volatile("" ::: "memory");
            if (bad) {
#pragma unroll
                for (int q = 0; q < 4; ++q) t[q] = LD128(r_ws, l * 16 + q * 1024, WS_T + tw * 4096, AUX_SC1);
            }
            u32 e = 0;
#pragma unroll
            for (int q = 0; q < 4; ++q) e |= (t[q].y ^ tag) | (t[q].w ^ tag);
            bad = e != 0;
            if (all_lanes(!bad)) break;
        }
        wait_flag<4, 2>(F_P, 1);
        const u32 a = pin(L_TAB + 2 * T_LO + (tw >> 1) * 65536 + ((tw & 1) * 64 + (l >> 2)) * 2 + (l & 3) * 1024);
#pragma unroll
        for (int q = 0; q < 4; ++q) {
            *lp<u16>(a + q * 32) = (u16)t[q].x; *lp<u16>(a + q * 32 + 256) = (u16)(t[q].x >> 16);
            *lp<u16>(a + q * 32 + 512) = (u16)t[q].z; *lp<u16>(a + q * 32 + 768) = (u16)(t[q].z >> 16);
        }
        set_flag(F_T + tw * 4, 1);
        __builtin_amdgcn_endpgm();
    }

    const int w = wave2;
    v4u tb;
    u32 wa[4];
#pragma unroll
    for (int ct = 0; ct < 4; ++ct) wa[ct] = pin((L_W + fra) ^ (ct * 32));
    {
        const u32 xb = pin(L_X + w * 1024 + l * 16);
        const u32 xa = pin(kg == b ? L_XN + w * 320 + (n16 >> 2) * 64 + (n16 & 1) * 32 + ((n16 >> 1) & 1) * 8 : L_JUNK);
        const u32 xf = pin(L_XN + (n16 >> 2) * 320 + kg * 16);
        const u32 wq0 = pin((L_W + fra) ^ (w * 32));
        const int qvo = pin((n16 & 3) == 0 ? (n16 >> 2) * 8192 + kg * 16 : OOB);
        WAITVM(6);
        STAMP(1);
        const v4u xr = *lp<v4u>(xb);
        v2f xv[4];
#pragma unroll
        for (int q = 0; q < 4; ++q) xv[q] = bf2(xr[q]);
        const float c = u2f(__builtin_amdgcn_readfirstlane(f2u(xv[0].x)));
        const v2f c2 = {c, c};
        v2f d[4];
#pragma unroll
        for (int q = 0; q < 4; ++q) d[q] = xv[q] - c2;
        const v2f s2 = (d[0] + d[1]) + (d[2] + d[3]);
        const v2f q2 = __builtin_elementwise_fma(d[0], d[0], d[1] * d[1]) + __builtin_elementwise_fma(d[2], d[2], d[3] * d[3]);
        const float sd = wave_sum(s2.x + s2.y) * (1.f / 512.f), sq = wave_sum(q2.x + q2.y) * (1.f / 512.f);
        const float var = fmx(__builtin_fmaf(-sd, sd, sq), 0.f);
        const float mb = rbf(c + sd);
        const float rden = __builtin_amdgcn_rcpf(rbf(__builtin_amdgcn_sqrtf(rbf(rbf(var) + 1e-5f))));
        const v2f mb2 = {mb, mb}, rd2 = {rden, rden};
        v4u o;
#pragma unroll
        for (int q = 0; q < 4; ++q) o[q] = pk2(bf2(pk2(xv[q] - mb2)) * rd2);
        const v2u o0 = {o.x, o.y}, o1 = {o.z, o.w};
        *lp<v2u>(xa) = o0; *lp<v2u>(xa + 16) = o1;
        asm volatile("s_waitcnt vmcnt(0)\n\tv_add_u32 %0, %2, %1" : "=v"(tag) : "v"(tag0), "i"(TAG_STEP) : "memory");
        set_flag(F_A + w * 4, 1);
#pragma unroll
        for (int i = 0; i < 4; ++i) tb[i] = (tag >> (8 * i)) & 255u;
        v4u af[4], bf[4];
        if (PRE) wait_flag<4, PRE>(F_A, 1);
#pragma unroll 1
        for (int spin = 0; spin < SPIN_MAX; ++spin) {
            const v4u f = *lp<volatile v4u>(F_A);
            asm volatile("" ::: "memory");
#pragma unroll
            for (int t = 0; t < 4; ++t) { af[t] = *lp<v4u>(xf + t * 64); bf[t] = trfrag(wq0 + t * 4096); }
            SB();
            if (uni_ge(umn4(f), 1)) break;
        }
        v4f acc = {0.f, 0.f, 0.f, 0.f};
#pragma unroll
        for (int t = 0; t < 4; ++t) acc = mfma16(bf[t], af[t], acc);
        v4u gq;
#pragma unroll
        for (int i = 0; i < 4; ++i) gq[i] = (f2u(acc[i]) & ~255u) | tb[i];
        ST128(gq, r_ws, qvo, WS_Q + g * GQ + (s >> 1) * 2048 + b * 512 + (s & 1) * 256 + w * 64, AUX_SC1);
        set_flag(F_A + w * 4, 2);
    }
    STAMP(2);
    {
        const int qso = WS_Q + g * GQ + b * 8192 + w * 2048;
        const u32 qw = pin(L_QI + w * 320 + (l & 31) * 8 + (l >> 5) * 4);
        wait_flag<1, 2>(F_L, QP);
        STAMP(3);
        v4u p[2], c[2];
        bool bad = true;
#pragma unroll 1
        for (int spin = 0; spin < SPIN_MAX; ++spin) {
            asm volatile("" ::: "memory");
            if (bad) {
#pragma unroll
                for (int k = 0; k < 2; ++k) p[k] = LD128(r_ws, l * 16 + k * 1024, qso, AUX_SC1);
            }
            c[0] = p[0] ^ tb; c[1] = p[1] ^ tb;
            const v4u o = c[0] | c[1];
            bad = (((o.x | o.y) | (o.z | o.w)) & 255u) != 0;
            if (all_lanes(!bad)) break;
        }
        const v4f qs = __builtin_bit_cast(v4f, c[0]) + __builtin_bit_cast(v4f, c[1]);
        const v2f q0 = swap32(qs.x, qs.z), q1 = swap32(qs.y, qs.w);
        *lp<u32>(qw) = pkbf(q0.x + q0.y, q1.x + q1.y);
        set_flag(F_Q + w * 4, 1);
    }
    STAMP(4);
    {
        const u32 qa = pin(L_QI + (n16 >> 2) * 320 + kg * 16);
        const u32 ma = pin(n16 == 0 ? L_M + kg * 16 + w * 4 : L_JUNK), mr = pin(L_M + kg * 16);
        const u32 pw = pin(L_P + w * 512 + (n16 >> 2) * 64 + kg * 16 + (n16 & 3) * 2);
        const u32 k0 = pin(L_K + w * 8192 + n16 * 128 + ((kg ^ (n16 & 6)) * 16)), k1 = pin(k0 ^ 64);
        wait_flag<2, 1>(F_L, 4 * w + 4 + (WIN - 2) / 2);
        STAMP(24);
        v4u kf[4][4];
#pragma unroll
        for (int t = 0; t < 4; ++t)
#pragma unroll
            for (int u = 0; u < 4; ++u) kf[t][u] = *lp<v4u>(((t & 1) ? k1 : k0) + (t >> 1) * 32768 + u * 2048);
        SB();
        v4u qf[4];
        if (PRE) wait_flag<4, PRE>(F_Q, 1);
#pragma unroll 1
        for (int spin = 0; spin < SPIN_MAX; ++spin) {
            const v4u f = *lp<volatile v4u>(F_Q);
            asm volatile("" ::: "memory");
#pragma unroll
            for (int t = 0; t < 4; ++t) qf[t] = *lp<v4u>(qa + t * 64);
            SB();
            if (uni_ge(umn4(f), 1)) break;
        }
        STAMP(18);
        v4f acc[4];
#pragma unroll
        for (int u = 0; u < 4; ++u) acc[u] = (v4f){0.f, 0.f, 0.f, 0.f};
#pragma unroll
        for (int t = 0; t < 4; ++t)
#pragma unroll
            for (int u = 0; u < 4; ++u) acc[u] = mfma16(qf[t], kf[t][u], acc[u]);
        const v2f scale = {0.08838834764831845f, 0.08838834764831845f};
        const v2f s0 = bf2(pk2(bf2(pkbf(acc[0].x, acc[1].x)) * scale)), s1 = bf2(pk2(bf2(pkbf(acc[2].x, acc[3].x)) * scale));
        float m = fmx(fmx(s0.x, s0.y), fmx(s1.x, s1.y));
        m = fmx(m, dppf<DPP_X1>(m)); m = fmx(m, dppf<DPP_X2>(m)); m = fmx(m, dppf<DPP_HMIR>(m)); m = fmx(m, dppf<DPP_ROR8>(m));
        *lp<float>(ma) = m;
        set_flag(F_M + w * 4, 1);
        v4f mv;
        if (PRE) wait_flag<4, PRE>(F_M, 1);
#pragma unroll 1
        for (int spin = 0; spin < SPIN_MAX; ++spin) {
            const v4u f = *lp<volatile v4u>(F_M);
            asm volatile("" ::: "memory");
            mv = *lp<v4f>(mr);
            SB();
            if (uni_ge(umn4(f), 1)) break;
        }
        STAMP(19);
        const float mo = -fmx(fmx(mv.x, mv.y), fmx(mv.z, mv.w)) * LOG2E;
        const v2f e0 = {__builtin_amdgcn_exp2f(__builtin_fmaf(s0.x, LOG2E, mo)), __builtin_amdgcn_exp2f(__builtin_fmaf(s0.y, LOG2E, mo))};
        const v2f e1 = {__builtin_amdgcn_exp2f(__builtin_fmaf(s1.x, LOG2E, mo)), __builtin_amdgcn_exp2f(__builtin_fmaf(s1.y, LOG2E, mo))};
        const u32 p0 = pk2(e0), p1 = pk2(e1);
        *lp<u16>(pw) = (u16)p0; *lp<u16>(pw + 8) = (u16)(p0 >> 16); *lp<u16>(pw + 256) = (u16)p1; *lp<u16>(pw + 264) = (u16)(p1 >> 16);
        const v2f es = e0 + e1;
        *lp<float>(ma + (L_LS - L_M)) = row_sum(es.x + es.y);
        set_flag(F_P + w * 4, 1);
    }
    STAMP(5);
    __builtin_amdgcn_s_setprio(3);
    const int aline = WS_A + w * GA + (s * 32 + g * 4 + b) * 128;
    const int mvo = pin((4 * (kg & 1) + (l & 3)) * 16);
    const int mso = WS_ML + g * GML + b * 512 + w * 128;
    const int ovo = pin(n16 < 8 ? n16 * 64 + kg * 16 : OOB);
    const int oso = WS_O + g * GO + b * 8192 + w * 2048;
    const int avs = pin((AST16 ? (l & 35) == 0 : l < 32) ? l * 4 : OOB);
    if (w == 0) {
        wait_flag<4, 0>(F_P, 1);
        const v4f m = *lp<v4f>(L_M + (l & 3) * 16), q = *lp<v4f>(L_LS + (l & 3) * 16);
        const v4u gm = {f2u(fmx(fmx(m.x, m.y), fmx(m.z, m.w))), tag, f2u((q.x + q.y) + (q.z + q.w)), tag};
        ST128(gm, r_ws, l < 4 ? l * 512 : OOB, WS_ML + g * GML + b * 128 + s * 16, AUX_SC1);
    }
    v4u gm, av[4], wf[4][4];
    v2f W01, W23;
    auto weights = [&]() {
        const float ms = u2f(gm.x), lv = u2f(gm.z);
        float mm = fmx(ms, dppf<DPP_X1>(ms)); mm = fmx(mm, dppf<DPP_X2>(mm));
        { v2f a = swap16(mm, mm); mm = fmx(a.x, a.y); }
        const float wg = __builtin_amdgcn_exp2f((ms - mm) * LOG2E);
        float L = wg * lv; L += dppf<DPP_X1>(L); L += dppf<DPP_X2>(L);
        { v2f a = swap16(L, L); L = a.x + a.y; }
        const float W = wg * __builtin_amdgcn_rcpf(L);
        W01.x = dppf<0x00>(W); W01.y = dppf<0x55>(W); W23.x = dppf<0xAA>(W); W23.y = dppf<0xFF>(W);
        asm volatile("" : "+v"(W01), "+v"(W23));
    };
    {
        const __amdgpu_buffer_rsrc_t r_wo = mkrsrc(wo, 4u << 20);
        const int wvo = pin((16 * (lr & 3) + 2 * (lr >> 2)) * 1024 + swz);
        u32 so = (g * 4 + b) * (128 * 1024) + (w >> 1) * 65536 + (w & 1) * 8192 + s * 128;
        u32 lw = L_W + w * 4096, ms2 = mso;
        asm volatile("" : "+s"(so), "+s"(lw), "+s"(ms2) : "s"(r_wo));
        wait_flag<2, 2>(F_L, WOP);
#define WOL "buffer_load_dwordx4 %2, %3, %1 offen nt lds\n\t"
#define WON(d) "s_add_u32 m0, m0, 0x400\n\ts_add_i32 %1, %1, " #d "\n\t"
        asm volatile("s_mov_b32 m0, %4\n\t"
                     "buffer_load_dwordx4 %0, %5, %6, %7 offen sc1\n\t"
                     WOL WON(4096) WOL WON(-3072) WOL WON(4096) WOL
                     "s_waitcnt vmcnt(4)"
                     : "=&v"(gm), "+s"(so) : "v"(wvo), "s"(r_wo), "s"(lw), "v"(mvo), "s"(r_ws), "s"(ms2) : "memory", "scc");
        STAMP(6);
        const bool early = all_lanes((gm.y == tag) & (gm.w == tag));
        weights();
        wait_flag<3, 1>(F_L, 33);
        if (ODELAY) __builtin_amdgcn_s_sleep(ODELAY);
        asm volatile("buffer_load_dwordx4 %0, %4, %5, %6 offen sc1\n\tbuffer_load_dwordx4 %1, %4, %5, %6 offen offset:512 sc1\n\t"
                     "buffer_load_dwordx4 %2, %4, %5, %6 offen offset:1024 sc1\n\tbuffer_load_dwordx4 %3, %4, %5, %6 offen offset:1536 sc1\n\t"
                     "s_waitcnt vmcnt(4)"
                     : "=&v"(av[0]), "=&v"(av[1]), "=&v"(av[2]), "=&v"(av[3]) : "v"(ovo), "s"(r_ws), "s"(oso) : "memory");
        set_flag(F_W + w * 4, 1);
        STAMP(25);
        wait_flag<4, 0>(F_W, 1);
#pragma unroll
        for (int k = 0; k < 4; ++k)
#pragma unroll
            for (int ct = 0; ct < 4; ++ct) wf[k][ct] = trfrag(wa[ct] + k * 4096);
        SB();
        if (!early) {
#pragma unroll 1
            for (int spin = 0; spin < SPIN_MAX; ++spin) {
                asm volatile("buffer_load_dwordx4 %0, %1, %2, %3 offen sc1\n\ts_waitcnt vmcnt(0)" : "=v"(gm) : "v"(mvo), "s"(r_ws), "s"(ms2) : "memory");
                if (all_lanes((gm.y == tag) & (gm.w == tag))) break;
            }
            weights();
        }
    }
    STAMP(11);
    {
        asm volatile("s_waitcnt vmcnt(0)" : "+v"(av[0]), "+v"(av[1]), "+v"(av[2]), "+v"(av[3]) :: "memory");
#pragma unroll 1
        for (int spin = 0; spin < SPIN_MAX; ++spin) {
            const bool bad = umx(umx(umx4(av[0]), umx4(av[1])), umx(umx4(av[2]), umx4(av[3]))) == ~0u;
            if (all_lanes(!bad)) break;
            asm volatile("" ::: "memory");
            if (bad) {
#pragma unroll
                for (int k = 0; k < 4; ++k) av[k] = LD128(r_ws, ovo + k * 512, oso, AUX_SC1);
            }
        }
        STAMP(12);
        v4f c[4];
#pragma unroll
        for (int ct = 0; ct < 4; ++ct) c[ct] = (v4f){0.f, 0.f, 0.f, 0.f};
#pragma unroll
        for (int k = 0; k < 4; ++k) {
#pragma unroll
            for (int ct = 0; ct < 4; ++ct) c[ct] = mfma16(av[k], wf[k][ct], c[ct]);
            SB();
        }
        float z[4];
#pragma unroll
        for (int ct = 0; ct < 4; ++ct) {
            const v2f c01 = {c[ct].x, c[ct].y}, c23 = {c[ct].z, c[ct].w};
            const v2f t = __builtin_elementwise_fma(c23, W23, c01 * W01);
            z[ct] = t.x + t.y;
        }
        const v2f r0 = swap16(z[0], z[1]), r1 = swap16(z[2], z[3]);
        const u32 y = umn(pkbf(r0.x + r0.y, r1.x + r1.y), ~1u);
        const v4u ga = {y, dppu<0x55>(y), dppu<0xAA>(y), dppu<0xFF>(y)};
        ST128(ga, r_ws, avs, aline, AUX_SC1);
    }
    STAMP(13);
    if (!fin) {
        __builtin_amdgcn_endpgm();
    }

    {
        const int D = w * 64 + (n16 >> 3) * 32 + (kg >> 1) * 16 + (l & 7) * 2 + (kg & 1);
        v2f xr = bf2(*lp<u32>(L_X + b * 1024 + D * 4));
        v4u sel;
#pragma unroll
        for (int q = 0; q < 4; ++q) sel[q] = ((n16 & 2) == 0 && ((n16 >> 1) & 2) + (n16 & 1) == q) ? (n16 >= 8 ? 0x3F800000u : 0x3F80u) : 0u;
        asm volatile("" : "+v"(xr), "+v"(sel));
        const int avo = (n16 >> 3) * 4096 + kg * 128 + (l & 7) * 16;
        const int aso = WS_A + b * GA + w * 8192;
        wait_flag<4, 1>(F_T, 1);
        if (ADELAY) __builtin_amdgcn_s_sleep(ADELAY);
        v4f c0, c1;
#pragma unroll 1
        for (int spin = 0; spin < SPIN_MAX; ++spin) {
            asm volatile("" ::: "memory");
            v4u a[8];
#pragma unroll
            for (int i = 0; i < 8; ++i) a[i] = LD128(r_ws, avo + i * 512, aso, AUX_SC1);
            c0 = (v4f){0.f, 0.f, 0.f, 0.f}; c1 = c0;
            u32 mx = 0;
#pragma unroll
            for (int i = 0; i < 8; i += 2) {
                c0 = mfma16(sel, a[i], c0); c1 = mfma16(sel, a[i + 1], c1);
                mx = umx(mx, umx(umx4(a[i]), umx4(a[i + 1])));
            }
            if (all_lanes(mx != ~0u)) break;
        }
        STAMP(14);
        const v2f h = bf2(pk2(xr + bf2(pkbf(c0.x + c1.x, c0.y + c1.y))));
        const float h0 = u2f(__builtin_amdgcn_readfirstlane(f2u(h.x)));
        const float d0 = h.x - h0, d1 = h.y - h0;
        const float sd = wave_sum(d0 + d1), sdd = wave_sum(__builtin_fmaf(d0, d0, d1 * d1));
        { const v2f t = {128.f * h0 + sd, sdd - sd * sd * (1.f / 128.f)}; *lp<v2f>(l == 0 ? L_ST + w * 8 : L_JUNK) = t; }
        lds_barrier();
        const v4f s0 = *lp<v4f>(L_ST), s1 = *lp<v4f>(L_ST + 16);
        const float mean = ((s0.x + s0.z) + (s1.x + s1.z)) * (1.f / 512.f);
        const float e0 = s0.x * (1.f / 128.f) - mean, e1 = s0.z * (1.f / 128.f) - mean, e2 = s1.x * (1.f / 128.f) - mean, e3 = s1.z * (1.f / 128.f) - mean;
        const float var = fmx(__builtin_fmaf(128.f, (e0 * e0 + e1 * e1) + (e2 * e2 + e3 * e3), (s0.y + s0.w) + (s1.y + s1.w)) * (1.f / 512.f), 0.f);
        const float mb = rbf(mean);
        const float rden = __builtin_amdgcn_rcpf(rbf(__builtin_amdgcn_sqrtf(rbf(rbf(var) + 1e-5f))));
        const float mean2 = rbf((mean - mb) * rden), var2 = var * rden * rden;
        const float rden2 = __builtin_amdgcn_rcpf(rbf(__builtin_amdgcn_sqrtf(rbf(rbf(var2) + 1e-5f))));
        const v2f mb2 = {mb, mb}, rd2 = {rden, rden}, mc2 = {mean2, mean2}, re2 = {rden2, rden2};
        const v2f n1 = bf2(pk2(bf2(pk2(h - mb2)) * rd2));
        const u32 tt = pk2(bf2(pk2(n1 - mc2)) * re2);
        const u32 ta = tt & 0xffffu, tb = tt >> 16;
        const v2f y = {bflo(*lp<u16>(L_TAB + 2 * ta)), bflo(*lp<u16>(L_TAB + 2 * tb))};
        u32* o32 = (u32*)out + b * 256 + D;
        *o32 = pk2(h + y);
        if (b == 0 && w == 0 && l == 0) __builtin_amdgcn_raw_buffer_store_b32(tag, r_ws, 0, WS_EPOCH, AUX_SC1);
        STAMP(15);
        if (w == 0) { PUTST(0, 7); PUTST(11, 16); PUTST(18, 20); PUTST(24, 26); } else if (w == 3) PUTST(20, 22); else if (w == 2) PUTST(22, 23); else PUTST(23, 24);
#pragma unroll
        for (int q = 0; q < 2; ++q) {
            const u32 t = q ? tb : ta;
            if ((t & 0x7fffu) - T_LO >= 2048u) {
                const u32 i = __hip_atomic_fetch_add(lp<u32>(F_FB), 1u, __ATOMIC_RELAXED, __HIP_MEMORY_SCOPE_WORKGROUP);
                const v2u it = {(t << 16) | (u32)(2 * D + q), f2u(q ? h.y : h.x)};
                *lp<v2u>(L_FB + i * 8) = it;
            }
        }
        lds_barrier();
        const int n = __builtin_amdgcn_readfirstlane(*lp<u32>(F_FB));
        if (n <= 0) __builtin_amdgcn_endpgm();
        Coef cf;
        make_coef(cf, load_part(w1, w, l), load_part(b1, w, l), load_part(w2, w, l));
        const float b2v = u2f(pin(f2u(bflo(__builtin_amdgcn_raw_buffer_load_b16(mkrsrc(b2, 2), 0, 0, 0)))));
#pragma unroll 1
        for (int i = 0; i < n; ++i) {
            const float p = wave_sum(mlp_part(cf, bfhi(*lp<u32>(L_FB + i * 8))));
            *lp<float>(l == 0 ? L_FP + i * 16 + w * 4 : L_JUNK) = p;
        }
        __builtin_amdgcn_s_waitcnt(0x0F70);
        lds_barrier();
#pragma unroll 1
        for (int i = w * 64 + l; i < n; i += 256) {
            const v2u it = *lp<v2u>(L_FB + i * 8);
            const v4f p = *lp<v4f>(L_FP + i * 16);
            const float yv = rbf(rbf((p.x + p.y) + (p.z + p.w)) + b2v);
            out[b * 512 + (it.x & 0xffffu)] = (u16)pkbf(u2f(it.y) + yv, 0.f);
        }
    }
}

extern "C" void kernel_launch(void* const* d_in, const int* in_sizes, int n_in, void* d_out, int out_size, void* d_ws, size_t ws_size, hipStream_t stream) {
    constexpr u32 F = 0x00020000;
    fused_decode_kernel<<<256, 640, 0, stream>>>((const u16*)d_in[0], 4096u, F, (const u16*)d_in[3], 4u << 20, F, (const u16*)d_in[1], 64u << 20, F,
                                                 (const u16*)d_in[2], 64u << 20, F, (unsigned char*)d_ws, WS_END, F, (const u16*)d_in[6], 3584u, F,
                                                 (const u16*)d_in[7], 3584u, F, (const u16*)d_in[8], 3584u, F, (const u16*)d_in[9], 2u, F,
                                                 (const u16*)d_in[4], (u16*)d_out);
}
```

```cpp
#include <hip/hip_runtime.h>
#include <stdint.h>

typedef unsigned u32;
typedef unsigned short u16;
typedef u32 v4u __attribute__((ext_vector_type(4)));
typedef u32 v2u __attribute__((ext_vector_type(2)));
typedef float v4f __attribute__((ext_vector_type(4)));
typedef float v2f __attribute__((ext_vector_type(2)));
typedef __bf16 v8bf __attribute__((ext_vector_type(8)));
typedef __bf16 v2bf __attribute__((ext_vector_type(2)));
typedef short v4s __attribute__((ext_vector_type(4)));
#define LAS __attribute__((address_space(3)))

#define STAMP(i) do { } while (0)
#define PUTST(a, b) do { } while (0)

#define MAPG 0
#define WIN 12
#define QP 10
#define WOP 32
#define XP 0
#define PRE 0
#define TN 16
#define TWAIT 0
#define WOE 4
#define GDELAY 10
#define KFIRST 0
#define OST16 0
#define AST16 1
#define ODELAY 0
#define ADELAY 0
#define ROTA 6
#define ROTB 3
#define SPREAD 1
#define WSOFF 0
#define ILV 0
#define FINM 0
#define FINC 0
#define JUNK 15
#define T1F 480
#define WOST 4
#define V5FMT 4

constexpr int AUX_SC1 = 16;
constexpr u32 WS_EPOCH = 0;
constexpr u32 GQ = 262144, GO = 262144, GML = 262144, GA = 524288;
constexpr u32 WS_Q = 4096;
constexpr u32 WS_O = 65536;
constexpr u32 WS_ML = 131072;
constexpr u32 WS_A = 163840;
constexpr u32 WS_T = 262144 + 163840;
constexpr u32 WS_END = 2097152;
constexpr u32 L_V = 0, L_K = 0x10000, L_W = 0x20000, L_TN = 0x24000, L_X = 0x25000, L_XN = 0x26000, L_QI = 0x26500, L_P = 0x26A00,
              L_TP = 0x27200, L_JUNK = 0x27600, L_FL = 0x27A00, L_M = 0x27A80, L_LS = 0x27AC0, L_ST = 0x27B00, L_END = 0x27B40;
constexpr u32 F_A = L_FL, F_Q = L_FL + 16, F_M = L_FL + 32, F_P = L_FL + 48, F_T = L_FL + 64, F_L = L_FL + 80, F_ARR = L_FL + 88, F_FB = L_FL + 92, F_W = L_FL + 96;
constexpr u32 TAG_STEP = 0x01010101u;
constexpr u32 L_TAB = 0xCE00;
constexpr u32 L_OS = L_K, L_FB = L_K + 0x2000, L_FP = L_K + 0x6000;
constexpr u32 T_LO = 114 * 128;
constexpr int SPIN_MAX = 1 << 18;
constexpr u32 CODE_BYTES = 11776;
constexpr int OOB = 0x40000000;
constexpr float LOG2E = 1.4426950408889634f;

__device__ __forceinline__ float u2f(u32 v) { return __builtin_bit_cast(float, v); }
__device__ __forceinline__ u32 f2u(float v) { return __builtin_bit_cast(u32, v); }
__device__ __forceinline__ float bflo(u32 v) { return u2f(v << 16); }
__device__ __forceinline__ float bfhi(u32 v) { return u2f(v & 0xffff0000u); }
__device__ __forceinline__ v2f bf2(u32 v) { v2f r = {bflo(v), bfhi(v)}; return r; }
__device__ __forceinline__ u32 pk2(v2f f) { return __builtin_bit_cast(u32, __builtin_convertvector(f, v2bf)); }
__device__ __forceinline__ u32 pkbf(float a, float b) { v2f f = {a, b}; return pk2(f); }
__device__ __forceinline__ float rbf(float a) { return bflo(pkbf(a, a)); }
template <int CTRL> __device__ __forceinline__ float dppf(float v) { return u2f((u32)__builtin_amdgcn_update_dpp(0, (int)f2u(v), CTRL, 0xf, 0xf, true)); }
template <int CTRL> __device__ __forceinline__ u32 dppu(u32 v) { return (u32)__builtin_amdgcn_update_dpp(0, (int)v, CTRL, 0xf, 0xf, true); }
constexpr int DPP_X1 = 0xB1, DPP_X2 = 0x4E, DPP_HMIR = 0x141, DPP_ROR4 = 0x124, DPP_ROR8 = 0x128, DPP_SHL4 = 0x104, DPP_SHL8 = 0x108;
__device__ __forceinline__ float fmx(float a, float b) { return __builtin_fmaxf(a, b); }
__device__ __forceinline__ u32 umn(u32 a, u32 b) { return a < b ? a : b; }
__device__ __forceinline__ u32 umx(u32 a, u32 b) { return a > b ? a : b; }
__device__ __forceinline__ u32 umx4(v4u a) { return umx(umx(a.x, a.y), umx(a.z, a.w)); }
__device__ __forceinline__ v2f swap32(float a, float b) {
    auto r = __builtin_amdgcn_permlane32_swap(f2u(a), f2u(b), false, false);
    v2f o = {u2f(r[0]), u2f(r[1])}; return o;
}
__device__ __forceinline__ v2f swap16(float a, float b) {
    auto r = __builtin_amdgcn_permlane16_swap(f2u(a), f2u(b), false, false);
    v2f o = {u2f(r[0]), u2f(r[1])}; return o;
}
__device__ __forceinline__ float sum_x16_x32(float v) { v2f a = swap16(v, v); v = a.x + a.y; a = swap32(v, v); return a.x + a.y; }
__device__ __forceinline__ float max_x16_x32(float v) { v2f a = swap16(v, v); v = fmx(a.x, a.y); a = swap32(v, v); return fmx(a.x, a.y); }
__device__ __forceinline__ float row_sum(float v) { v += dppf<DPP_X1>(v); v += dppf<DPP_X2>(v); v += dppf<DPP_HMIR>(v); v += dppf<DPP_ROR8>(v); return v; }
__device__ __forceinline__ float wave_sum(float v) { return sum_x16_x32(row_sum(v)); }
__device__ __forceinline__ v4f mfma16(v4u a, v4u b, v4f c) {
    return __builtin_amdgcn_mfma_f32_16x16x32_bf16(__builtin_bit_cast(v8bf, a), __builtin_bit_cast(v8bf, b), c, 0, 0, 0);
}
__device__ __forceinline__ void lds_barrier() { asm volatile("s_waitcnt lgkmcnt(0)\n\ts_barrier" ::: "memory"); }
#define SB() __builtin_amdgcn_sched_barrier(0)
#define WAITVM(n) asm volatile("s_waitcnt vmcnt(%0)" :: "i"(n) : "memory")
__device__ __forceinline__ __amdgpu_buffer_rsrc_t mkrsrc(const void* p, u32 bytes) { return __builtin_amdgcn_make_buffer_rsrc((void*)p, 0, bytes, 0x00020000); }
__device__ __forceinline__ bool all_lanes(bool p) { return __builtin_amdgcn_ballot_w64(!p) == 0ull; }
__device__ __forceinline__ void junk_load(__amdgpu_buffer_rsrc_t rs, int vo, u32 lds) {
    u32 keep;
    asm volatile("s_mov_b32 %0, m0\n\ts_mov_b32 m0, %3\n\ts_nop 0\n\tbuffer_load_ushort %1, %2, 0 offen lds\n\ts_mov_b32 m0, %0" : "=&s"(keep) : "v"(vo), "s"(rs), "s"(lds) : "memory");
}
#define LD128(r, vo, so, aux) __builtin_amdgcn_raw_buffer_load_b128(r, vo, so, aux)
#define ST128(d, r, vo, so, aux) do { v4u d_ = (d); asm volatile("" : "+v"(d_)); __builtin_amdgcn_raw_buffer_store_b128(d_, r, vo, so, aux); asm volatile("s_nop 1" :: "v"(d_)); } while (0)
template <typename T> __device__ __forceinline__ LAS T* lp(u32 a) { return (LAS T*)(size_t)a; }
__device__ __forceinline__ u32 pin(u32 v) { asm volatile("" : "+v"(v)); return v; }
__device__ __forceinline__ v4u trfrag(u32 a) {
    const v2u lo = __builtin_bit_cast(v2u, __builtin_amdgcn_ds_read_tr16_b64_v4i16(lp<v4s>(a)));
    const v2u hi = __builtin_bit_cast(v2u, __builtin_amdgcn_ds_read_tr16_b64_v4i16(lp<v4s>(a + 2048)));
    v4u r = {lo.x, lo.y, hi.x, hi.y}; return r;
}
__device__ __forceinline__ void set_flag(u32 a, u32 v) { asm volatile("" ::: "memory"); *lp<volatile u32>(a) = v; }
__device__ __forceinline__ u32 umn4(v4u a) { return umn(umn(a.x, a.y), umn(a.z, a.w)); }
__device__ __forceinline__ bool uni_ge(u32 x, u32 v) { return (u32)__builtin_amdgcn_readfirstlane(x) >= v; }
template <int N, int SLP> __device__ __forceinline__ void wait_flag(u32 a, u32 v) {
#pragma unroll 1
    for (int i = 0; i < SPIN_MAX; ++i) {
        u32 m;
        if (N == 1) m = *lp<volatile u32>(a);
        else if (N == 2) { const v2u f = *lp<volatile v2u>(a); m = umn(f.x, f.y); }
        else if (N == 3) { const v4u f = *lp<volatile v4u>(a); m = umn(umn(f.x, f.y), f.z); }
        else m = umn4(*lp<volatile v4u>(a));
        if (uni_ge(m, v)) break;
        if (SLP) __builtin_amdgcn_s_sleep(SLP);
    }
    asm volatile("" ::: "memory");
}
struct Coef { v2f w[4], b[4], g1[4], g0[4]; };
__device__ __forceinline__ float mlp_part(const Coef& c, float t) {
    const v2f t2 = {t, t};
    const float K0 = -2.0f * LOG2E * 0.7978845608028654f, K1 = K0 * 0.044715f;
    const v2f k0 = {K0, K0}, k1 = {K1, K1}, one = {1.f, 1.f};
    v2f acc = {0.f, 0.f};
#pragma unroll
    for (int i = 0; i < 4; ++i) {
        const v2f u = __builtin_elementwise_fma(t2, c.w[i], c.b[i]);
        const v2f a = u * __builtin_elementwise_fma(u * u, k1, k0);
        v2f e = {__builtin_amdgcn_exp2f(a.x), __builtin_amdgcn_exp2f(a.y)};
        e += one;
        const v2f rc = {__builtin_amdgcn_rcpf(e.x), __builtin_amdgcn_rcpf(e.y)};
        acc = __builtin_elementwise_fma(__builtin_elementwise_fma(t2, c.g1[i], c.g0[i]), rc, acc);
    }
    return acc.x + acc.y;
}

__device__ __forceinline__ v4u load_part(const u16* p, int part, int l) { return LD128(mkrsrc(p + part * 448, 896), l * 16, 0, 0); }
__device__ __forceinline__ void make_coef(Coef& cf, v4u ta, v4u tb, v4u tc) {
#pragma unroll
    for (int i = 0; i < 4; ++i) { const v2f c = bf2(tc[i]); cf.w[i] = bf2(ta[i]); cf.b[i] = bf2(tb[i]); cf.g1[i] = cf.w[i] * c; cf.g0[i] = cf.b[i] * c; }
}

#define DESC(T, p) T p, u32, u32
__global__ void __launch_bounds__(640)
fused_decode_kernel(DESC(const u16*, x), DESC(const u16*, wq), DESC(const u16*, kc), DESC(const u16*, vc), DESC(unsigned char*, ws),
                    DESC(const u16*, w1), DESC(const u16*, b1), DESC(const u16*, w2), DESC(const u16*, b2), const u16* wo, u16* out) {
    __shared__ __attribute__((aligned(16))) unsigned char smem[L_END];
    STAMP(0);
    u32 tag0, tag, b2r;
    v4u ca, cb, cc;
    int wave2;
    const int pid = blockIdx.x;
    {
        u32 t0, t1, t2, t3;
#define WQL "s_add_u32 m0, m0, 0x400\n s_add_u32 s73, s73, 0x10000\n buffer_load_dwordx4 %[t3], s[40:43], s73 offen nt lds\n"
#define KVL "buffer_load_dwordx4 %[t3], s[44:47], s73 offen nt lds\n s_add_u32 m0, m0, 0x400\n s_add_u32 s73, s73, 0x800\n"
        asm volatile(
            "s_load_dwordx16 s[36:51], %[ka], 0x0\n"
            "s_load_dwordx16 s[52:67], %[ka], 0x40\n"
            "s_getpc_b64 s[76:77]\n"
            "v_readfirstlane_b32 %[wv], %[tid]\n"
            "v_bfe_u32 %[t0], %[tid], 3, 3\n"
            "v_and_b32 %[t1], 6, %[t0]\n"
            "v_xor_b32 %[t1], %[t1], %[tid]\n"
            "v_and_b32 %[t1], 7, %[t1]\n"
            "v_lshlrev_b32 %[t1], 4, %[t1]\n"
            "s_lshr_b32 %[wv], %[wv], 6\n"
            "s_and_b32 s72, %[pid], 7\n"
            "s_cmp_gt_u32 %[wv], 3\n"
            "s_cbranch_scc1 2f\n"
            "s_setprio 2\n"
            "v_lshlrev_b32 %[t2], 4, %[tid]\n"
            "v_lshl_or_b32 %[t3], %[t0], 13, %[t1]\n"
            "s_lshr_b32 s75, %[pid], 6\n"
            "s_lshl_b32 s73, s75, 2\n"
            "s_add_u32 s73, s73, %[wv]\n"
            "s_lshl_b32 s73, s73, 11\n"
            "s_and_b32 s74, %[pid], 63\n"
            "s_or_b32 s73, s73, s74\n"
            "s_lshl_b32 s73, s73, 7\n"
            "s_lshl_b32 s74, %[wv], 10\n"
            "s_add_u32 m0, s74, %[lx]\n"
            "s_lshl_b32 s74, s74, 2\n"
            "s_add_u32 s74, s74, %[lw]\n"
            "v_bfe_u32 %[t0], %[tid], 4, 2\n"
            "v_and_b32 %[t1], 15, %[tid]\n"
            "v_lshlrev_b32 %[t1], 2, %[t1]\n"
            "v_lshl_or_b32 %[t0], %[t0], 9, %[t1]\n"
            "s_and_b32 s78, %[pid], 56\n"
            "s_lshl_b32 s78, s78, 3\n"
            "s_lshl1_add_u32 s78, %[wv], s78\n"
            "s_lshl1_add_u32 s78, s78, s75\n"
            "s_lshl_b32 s78, s78, 5\n"
            "s_or_b32 s78, s78, s72\n"
            "s_lshl_b32 s78, s78, 6\n"
            "s_add_u32 s78, s78, %[wso]\n"
            "v_mov_b32 %[t1], -1\n"
            "s_waitcnt lgkmcnt(0)\n"
            "buffer_load_dwordx4 %[t2], s[36:39], 0 offen lds\n"
            "s_mov_b32 m0, s74\n"
            "s_nop 0\n"
            "buffer_load_dwordx4 %[t3], s[40:43], s73 offen nt lds\n"
            WQL WQL WQL
            "buffer_load_dword %[tag], off, s[52:55], 0\n"
            "buffer_store_dword %[t1], %[t0], s[52:55], s78 offen sc1\n"
            "s_barrier\n"
            "s_branch 9f\n"
            "2:\n"
            "s_cmp_gt_u32 %[wv], 5\n"
            "s_cbranch_scc1 5f\n"
            "s_setprio 3\n"
            "s_memrealtime s[70:71]\n"
            "s_sub_u32 s75, %[wv], 4\n"
            "v_lshl_or_b32 %[t3], %[t0], 8, %[t1]\n"
            "s_lshr_b32 s74, %[pid], 3\n"
            "s_mul_i32 s73, s74, %[rotb]\n"
            "s_mul_i32 s78, s72, %[rota]\n"
            "s_add_u32 s73, s73, s78\n"
            "s_lshl_b32 s73, s73, 5\n"
            "s_or_b32 s73, s73, s75\n"
            "s_lshl_b32 s73, s73, 7\n"
            "s_lshl_b32 s74, s74, 5\n"
            "s_or_b32 s74, s74, s72\n"
            "s_or_b32 s74, s74, 0x18\n"
            "s_pack_ll_b32_b16 s73, s73, s74\n"
            "s_lshl_b32 s78, s75, 15\n"
            "s_add_u32 m0, s78, %[lk]\n"
            "s_lshl2_add_u32 s75, s75, %[fl]\n"
            "v_mov_b32 %[t0], s75\n"
            "v_mov_b32 %[t1], 0\n"
            "s_mov_b32 s75, 0\n"
            "s_waitcnt lgkmcnt(0)\n"
            "s_add_u32 s79, s70, %[t1f]\n"
            "s_barrier\n"
            "3:\n"
            "s_waitcnt vmcnt(%[win])\n"
            KVL KVL
            "s_pack_ll_b32_b16 s73, s73, s74\n"
            "ds_write_b32 %[t0], %[t1]\n"
            "s_add_u32 s75, s75, 1\n"
            "v_add_u32 %[t1], 1, %[t1]\n"
            "s_cmp_lg_u32 s75, 16\n"
            "s_cbranch_scc1 3b\n"
            "s_mov_b64 s[44:45], s[48:49]\n"
            "s_mov_b32 m0, s78\n"
            "4:\n"
            "s_waitcnt vmcnt(%[win])\n"
            "s_memrealtime s[70:71]\n"
            "ds_write_b32 %[t0], %[t1]\n"
            "v_add_u32 %[t1], 1, %[t1]\n"
            "6:\n"
            KVL KVL
            "s_pack_ll_b32_b16 s73, s73, s74\n"
            "s_sub_u32 s75, s75, 1\n"
            "s_cmp_eq_u32 s75, 0\n"
            "s_cbranch_scc1 7f\n"
            "s_waitcnt lgkmcnt(0)\n"
            "s_cmp_gt_u32 s75, %[rem]\n"
            "s_cbranch_scc1 4b\n"
            "s_sub_i32 s72, s70, s79\n"
            "s_cmp_lt_i32 s72, 0\n"
            "s_cbranch_scc1 4b\n"
            "s_branch 6b\n"
            "7:\n"
            "v_mov_b32 %[t1], 31\n"
            "s_waitcnt vmcnt(32) lgkmcnt(0)\n"
            "ds_write_b32 %[t0], %[t1]\n"
            "s_branch 9f\n"
            "5:\n"
            "s_load_dwordx4 s[68:71], %[ka], 0x80\n"
            "s_sub_u32 s75, %[wv], 6\n"
            "s_and_b32 s74, %[pid], 56\n"
            "s_lshl1_add_u32 s74, s75, s74\n"
            "s_lshl_b32 s74, s74, 8\n"
            "s_add_u32 s78, s74, 512\n"
            "s_min_u32 s78, s78, %[code]\n"
            "s_mov_b32 s79, 0x20000\n"
            "s_and_b32 s76, s76, 0xffffff00\n"
            "v_and_b32 %[t2], 63, %[tid]\n"
            "v_lshl_add_u32 %[t3], %[t2], 7, s74\n"
            "buffer_load_dword %[b2r], %[t3], s[76:79], 0 offen\n"
            "v_and_b32 %[t0], 31, %[tid]\n"
            "v_lshlrev_b32 %[t0], 2, %[t0]\n"
            "v_add_u32 %[t0], %[lfl], %[t0]\n"
            "v_mov_b32 %[t1], 0\n"
            "ds_write_b32 %[t0], %[t1]\n"
            "s_mulk_i32 s75, 896\n"
            "v_lshl_add_u32 %[t3], %[t2], 4, s75\n"
            "v_cmp_gt_u32 vcc, 56, %[t2]\n"
            "v_cndmask_b32 %[t3], 2.0, %[t3], vcc\n"
            "s_waitcnt lgkmcnt(0)\n"
            "s_barrier\n"
            "buffer_load_dwordx4 %[ca], %[t3], s[56:59], 0 offen\n"
            "buffer_load_dwordx4 %[cb], %[t3], s[60:63], 0 offen\n"
            "buffer_load_dwordx4 %[cc], %[t3], s[64:67], 0 offen\n"
            "buffer_load_dword %[tag], off, s[52:55], 0\n"
            "buffer_load_ushort %[b2r], off, s[68:71], 0\n"
            "9:\n"
            : [wv] "=&s"(wave2), [tag] "=&v"(tag0), [b2r] "=&v"(b2r), [ca] "=&v"(ca), [cb] "=&v"(cb), [cc] "=&v"(cc),
              [t0] "=&v"(t0), [t1] "=&v"(t1), [t2] "=&v"(t2), [t3] "=&v"(t3)
            : [ka] "s"(__builtin_amdgcn_kernarg_segment_ptr()), [pid] "s"(pid), [tid] "v"(threadIdx.x), "s"((u32)(size_t)(LAS void*)smem),
              [lx] "i"(L_X), [lw] "i"(L_W), [lk] "i"(L_K), [fl] "i"(F_L), [lfl] "i"(L_FL), [wso] "i"(WS_O), [code] "i"(CODE_BYTES), [win] "i"(WIN - 2), [rota] "i"(ROTA), [rotb] "i"(ROTB), [t1f] "i"(T1F), [rem] "i"(15 - (WIN - 2) / 2)
            : "memory", "scc", "vcc", "s36", "s37", "s38", "s39", "s40", "s41", "s42", "s43", "s44", "s45", "s46", "s47", "s48", "s49", "s50", "s51", "s52", "s53", "s54", "s55", "s56", "s57", "s58", "s59", "s60", "s61", "s62", "s63", "s64", "s65", "s66", "s67", "s68", "s69", "s70", "s71", "s72", "s73", "s74", "s75", "s76", "s77", "s78", "s79");
        static_assert(L_V == 0 && OOB == 0x40000000 && F_ARR == F_L + 8, "");
    }
    SB();
    const int l = threadIdx.x & 63;
    const int s = pid & 7, g = (pid >> 3) & 7, b = pid >> 6;
    const int n16 = l & 15, kg = l >> 4, lr = l >> 3;
    const int swz = ((l & 7) ^ (lr & 6)) * 16;
    const __amdgpu_buffer_rsrc_t r_ws = mkrsrc(ws, WS_END);
    const u32 fra = (kg * 4 + (n16 >> 2)) * 128 + (((kg & 1) * 2 + (n16 >> 3)) * 32) + (n16 & 3) * 8;
    const bool fin = (pid & 63) == FINM * b + FINC;

    if (wave2 >= 4 && wave2 < 6) {
        STAMP(7);
        const int dh = wave2 - 4;
        const u32 lv = L_V + dh * 32768, fl = F_L + dh * 4;
        const u32 pa = pin(L_P + kg * 64 + (n16 >> 2) * 16);
        u32 va[4];
#pragma unroll
        for (int t = 0; t < 4; ++t) va[t] = pin((lv + fra) ^ (t * 32));
        const int ovo = pin(kg * 8192 + (n16 >> 3) * 512 + (n16 & 7) * 8);
        const int oso = WS_O + g * GO + b * 2048 + dh * 1024 + s * 64;
        wait_flag<4, 1>(F_P, 1);
        STAMP(16);
        v4u pf[8];
#pragma unroll
        for (int st = 0; st < 8; ++st) pf[st] = *lp<v4u>(pa + st * 256);
        v4f acc[4];
#pragma unroll
        for (int t = 0; t < 4; ++t) acc[t] = (v4f){0.f, 0.f, 0.f, 0.f};
        static_assert(WIN <= 16 && WIN % 4 == 0, "");
        constexpr int ST0 = 5;
#pragma unroll
        for (int st = 0; st < ST0; ++st) {
            WAITVM(28 - 4 * st);
            if (st == WOST) set_flag(fl, 32);
#pragma unroll
            for (int t = 0; t < 4; ++t) acc[t] = mfma16(pf[st], trfrag(va[t] + st * 4096), acc[t]);
        }
        SB();
        STAMP(17);
#pragma unroll
        for (int st = ST0; st < 8; ++st) {
            v2u lo[4], hi[4];
            WAITVM(30 - 4 * st);
#pragma unroll
            for (int t = 0; t < 4; ++t) lo[t] = __builtin_bit_cast(v2u, __builtin_amdgcn_ds_read_tr16_b64_v4i16(lp<v4s>(va[t] + st * 4096)));
            WAITVM(28 - 4 * st);
            if (st == WOST) set_flag(fl, 32);
            if (st == 7) STAMP(8);
#pragma unroll
            for (int t = 0; t < 4; ++t) hi[t] = __builtin_bit_cast(v2u, __builtin_amdgcn_ds_read_tr16_b64_v4i16(lp<v4s>(va[t] + st * 4096 + 2048)));
#pragma unroll
            for (int t = 0; t < 4; ++t) { const v4u bf = {lo[t].x, lo[t].y, hi[t].x, hi[t].y}; acc[t] = mfma16(pf[st], bf, acc[t]); }
            SB();
        }
        const v2u go = {umn(pkbf(acc[0].x, acc[1].x), ~1u), umn(pkbf(acc[2].x, acc[3].x), ~1u)};
        __builtin_amdgcn_raw_buffer_store_b64(go, r_ws, ovo, oso, AUX_SC1);
        set_flag(fl, 33);
        STAMP(9);
        if (dh == 0) { PUTST(7, 10); PUTST(16, 18); }
        __builtin_amdgcn_endpgm();
    }

    if (wave2 >= 6) {
        const int tw = wave2 - 6;
        const int tj = s * 32 + g * 4 + b;
        const v4u ones = {~0u, ~0u, ~0u, ~0u};
        ST128(ones, r_ws, l < 8 ? l * 16 : OOB, WS_A + tw * GA + (s * 32 + g * 4 + b) * 128, AUX_SC1);
        {
            const u16* p = tw == 0 ? vc + ((size_t)(b * 8 + g) * 8192 + 6144 + s * 256) * 128 : tw == 1 ? wo + (g * 4 + b) * 65536 : out;
            junk_load(mkrsrc(p, ((JUNK >> tw) & 1) && (tw != 2 || fin || JUNK >= 8) ? 2 : 0), l * 4, L_JUNK);
        }
        if (TWAIT) wait_flag<4, 4>(F_A, TWAIT);
        asm volatile("s_waitcnt vmcnt(2)" : "+v"(ca), "+v"(cb), "+v"(cc), "+v"(tag0), "+v"(b2r) :: "memory");
        SB();
        Coef cf;
        make_coef(cf, ca, cb, cc);
        tag = tag0 + TAG_STEP;
        {
            float t = u2f((u32)(((tj & 128) << 8) + T_LO + (tj & 127)) << 16);
            u32 ta = n16 == 0 ? L_TP + tw * 16 + kg * 4 : L_JUNK;
#pragma unroll 1
            for (int e = 0; e < TN; ++e) {
                *lp<float>(ta) = row_sum(mlp_part(cf, t));
                ta += n16 == 0 ? 64 : 0;
                t += t;
            }
        }
        WAITVM(0);
        {
            u32 arr = 0;
            if (l == 0) arr = __hip_atomic_fetch_add(lp<u32>(F_ARR), 9u, __ATOMIC_RELAXED, __HIP_MEMORY_SCOPE_WORKGROUP);
            asm volatile("" ::: "memory");
            if (__builtin_amdgcn_readfirstlane(arr) == 27) {
                const v4f p = *lp<v4f>(L_TP + l * 16);
                float v = (p.x + p.y) + (p.z + p.w);
                v += dppf<DPP_X1>(v); v += dppf<DPP_X2>(v);
                v = rbf(v) + bflo(b2r);
                const u32 p0 = pkbf(v, dppf<DPP_SHL4>(v));
                const v4u gt = {p0, tag, dppu<DPP_SHL8>(p0), tag};
                ST128(gt, r_ws, n16 == 0 ? kg * 16 : OOB, WS_T + tj * 64, AUX_SC1);
            }
        }
        STAMP(10);
        if (tw == 0) PUTST(10, 11);
        if (!fin) __builtin_amdgcn_endpgm();
        if (GDELAY) __builtin_amdgcn_s_sleep(GDELAY);
        v4u t[4];
        bool bad = true;
#pragma unroll 1
        for (int spin = 0; spin < SPIN_MAX; ++spin) {
            asm volatile("" ::: "memory");
            if (bad) {
#pragma unroll
                for (int q = 0; q < 4; ++q) t[q] = LD128(r_ws, l * 16 + q * 1024, WS_T + tw * 4096, AUX_SC1);
            }
            u32 e = 0;
#pragma unroll
            for (int q = 0; q < 4; ++q) e |= (t[q].y ^ tag) | (t[q].w ^ tag);
            bad = e != 0;
            if (all_lanes(!bad)) break;
        }
        wait_flag<4, 2>(F_P, 1);
        const u32 a = pin(L_TAB + 2 * T_LO + (tw >> 1) * 65536 + ((tw & 1) * 64 + (l >> 2)) * 2 + (l & 3) * 1024);
#pragma unroll
        for (int q = 0; q < 4; ++q) {
            *lp<u16>(a + q * 32) = (u16)t[q].x; *lp<u16>(a + q * 32 + 256) = (u16)(t[q].x >> 16);
            *lp<u16>(a + q * 32 + 512) = (u16)t[q].z; *lp<u16>(a + q * 32 + 768) = (u16)(t[q].z >> 16);
        }
        set_flag(F_T + tw * 4, 1);
        __builtin_amdgcn_endpgm();
    }

    const int w = wave2;
    v4u tb;
    u32 wa[4];
#pragma unroll
    for (int ct = 0; ct < 4; ++ct) wa[ct] = pin((L_W + fra) ^ (ct * 32));
    {
        const u32 xb = pin(L_X + w * 1024 + l * 16);
        const u32 xa = pin(kg == b ? L_XN + w * 320 + (n16 >> 2) * 64 + (n16 & 1) * 32 + ((n16 >> 1) & 1) * 8 : L_JUNK);
        const u32 xf = pin(L_XN + (n16 >> 2) * 320 + kg * 16);
        const u32 wq0 = pin((L_W + fra) ^ (w * 32));
        const int qvo = pin((n16 & 3) == 0 ? (n16 >> 2) * 8192 + kg * 16 : OOB);
        WAITVM(6);
        STAMP(1);
        const v4u xr = *lp<v4u>(xb);
        v2f xv[4];
#pragma unroll
        for (int q = 0; q < 4; ++q) xv[q] = bf2(xr[q]);
        const float c = u2f(__builtin_amdgcn_readfirstlane(f2u(xv[0].x)));
        const v2f c2 = {c, c};
        v2f d[4];
#pragma unroll
        for (int q = 0; q < 4; ++q) d[q] = xv[q] - c2;
        const v2f s2 = (d[0] + d[1]) + (d[2] + d[3]);
        const v2f q2 = __builtin_elementwise_fma(d[0], d[0], d[1] * d[1]) + __builtin_elementwise_fma(d[2], d[2], d[3] * d[3]);
        const float sd = wave_sum(s2.x + s2.y) * (1.f / 512.f), sq = wave_sum(q2.x + q2.y) * (1.f / 512.f);
        const float var = fmx(__builtin_fmaf(-sd, sd, sq), 0.f);
        const float mb = rbf(c + sd);
        const float rden = __builtin_amdgcn_rcpf(rbf(__builtin_amdgcn_sqrtf(rbf(rbf(var) + 1e-5f))));
        const v2f mb2 = {mb, mb}, rd2 = {rden, rden};
        v4u o;
#pragma unroll
        for (int q = 0; q < 4; ++q) o[q] = pk2(bf2(pk2(xv[q] - mb2)) * rd2);
        const v2u o0 = {o.x, o.y}, o1 = {o.z, o.w};
        *lp<v2u>(xa) = o0; *lp<v2u>(xa + 16) = o1;
        asm volatile("s_waitcnt vmcnt(0)\n\tv_add_u32 %0, %2, %1" : "=v"(tag) : "v"(tag0), "i"(TAG_STEP) : "memory");
        set_flag(F_A + w * 4, 1);
#pragma unroll
        for (int i = 0; i < 4; ++i) tb[i] = (tag >> (8 * i)) & 255u;
        v4u af[4], bf[4];
        if (PRE) wait_flag<4, PRE>(F_A, 1);
#pragma unroll 1
        for (int spin = 0; spin < SPIN_MAX; ++spin) {
            const v4u f = *lp<volatile v4u>(F_A);
            asm volatile("" ::: "memory");
#pragma unroll
            for (int t = 0; t < 4; ++t) { af[t] = *lp<v4u>(xf + t * 64); bf[t] = trfrag(wq0 + t * 4096); }
            SB();
            if (uni_ge(umn4(f), 1)) break;
        }
        v4f acc = {0.f, 0.f, 0.f, 0.f};
#pragma unroll
        for (int t = 0; t < 4; ++t) acc = mfma16(bf[t], af[t], acc);
        v4u gq;
#pragma unroll
        for (int i = 0; i < 4; ++i) gq[i] = (f2u(acc[i]) & ~255u) | tb[i];
        ST128(gq, r_ws, qvo, WS_Q + g * GQ + (s >> 1) * 2048 + b * 512 + (s & 1) * 256 + w * 64, AUX_SC1);
        set_flag(F_A + w * 4, 2);
    }
    STAMP(2);
    {
        const int qso = WS_Q + g * GQ + b * 8192 + w * 2048;
        const u32 qw = pin(L_QI + w * 320 + (l & 31) * 8 + (l >> 5) * 4);
        wait_flag<1, 2>(F_L, QP);
        STAMP(3);
        v4u p[2], c[2];
        bool bad = true;
#pragma unroll 1
        for (int spin = 0; spin < SPIN_MAX; ++spin) {
            asm volatile("" ::: "memory");
            if (bad) {
#pragma unroll
                for (int k = 0; k < 2; ++k) p[k] = LD128(r_ws, l * 16 + k * 1024, qso, AUX_SC1);
            }
            c[0] = p[0] ^ tb; c[1] = p[1] ^ tb;
            const v4u o = c[0] | c[1];
            bad = (((o.x | o.y) | (o.z | o.w)) & 255u) != 0;
            if (all_lanes(!bad)) break;
        }
        const v4f qs = __builtin_bit_cast(v4f, c[0]) + __builtin_bit_cast(v4f, c[1]);
        const v2f q0 = swap32(qs.x, qs.z), q1 = swap32(qs.y, qs.w);
        *lp<u32>(qw) = pkbf(q0.x + q0.y, q1.x + q1.y);
        set_flag(F_Q + w * 4, 1);
    }
    STAMP(4);
    {
        const u32 qa = pin(L_QI + (n16 >> 2) * 320 + kg * 16);
        const u32 ma = pin(n16 == 0 ? L_M + kg * 16 + w * 4 : L_JUNK), mr = pin(L_M + kg * 16);
        const u32 pw = pin(L_P + w * 512 + (n16 >> 2) * 64 + kg * 16 + (n16 & 3) * 2);
        const u32 k0 = pin(L_K + w * 8192 + n16 * 128 + ((kg ^ (n16 & 6)) * 16)), k1 = pin(k0 ^ 64);
        wait_flag<2, 1>(F_L, 4 * w + 4 + (WIN - 2) / 2);
        STAMP(24);
        v4u kf[4][4];
#pragma unroll
        for (int t = 0; t < 4; ++t)
#pragma unroll
            for (int u = 0; u < 4; ++u) kf[t][u] = *lp<v4u>(((t & 1) ? k1 : k0) + (t >> 1) * 32768 + u * 2048);
        SB();
        v4u qf[4];
        if (PRE) wait_flag<4, PRE>(F_Q, 1);
#pragma unroll 1
        for (int spin = 0; spin < SPIN_MAX; ++spin) {
            const v4u f = *lp<volatile v4u>(F_Q);
            asm volatile("" ::: "memory");
#pragma unroll
            for (int t = 0; t < 4; ++t) qf[t] = *lp<v4u>(qa + t * 64);
            SB();
            if (uni_ge(umn4(f), 1)) break;
        }
        STAMP(18);
        v4f acc[4];
#pragma unroll
        for (int u = 0; u < 4; ++u) acc[u] = (v4f){0.f, 0.f, 0.f, 0.f};
#pragma unroll
        for (int t = 0; t < 4; ++t)
#pragma unroll
            for (int u = 0; u < 4; ++u) acc[u] = mfma16(qf[t], kf[t][u], acc[u]);
        const v2f scale = {0.08838834764831845f, 0.08838834764831845f};
        const v2f s0 = bf2(pk2(bf2(pkbf(acc[0].x, acc[1].x)) * scale)), s1 = bf2(pk2(bf2(pkbf(acc[2].x, acc[3].x)) * scale));
        float m = fmx(fmx(s0.x, s0.y), fmx(s1.x, s1.y));
        m = fmx(m, dppf<DPP_X1>(m)); m = fmx(m, dppf<DPP_X2>(m)); m = fmx(m, dppf<DPP_HMIR>(m)); m = fmx(m, dppf<DPP_ROR8>(m));
        *lp<float>(ma) = m;
        set_flag(F_M + w * 4, 1);
        v4f mv;
        if (PRE) wait_flag<4, PRE>(F_M, 1);
#pragma unroll 1
        for (int spin = 0; spin < SPIN_MAX; ++spin) {
            const v4u f = *lp<volatile v4u>(F_M);
            asm volatile("" ::: "memory");
            mv = *lp<v4f>(mr);
            SB();
            if (uni_ge(umn4(f), 1)) break;
        }
        STAMP(19);
        const float mo = -fmx(fmx(mv.x, mv.y), fmx(mv.z, mv.w)) * LOG2E;
        const v2f e0 = {__builtin_amdgcn_exp2f(__builtin_fmaf(s0.x, LOG2E, mo)), __builtin_amdgcn_exp2f(__builtin_fmaf(s0.y, LOG2E, mo))};
        const v2f e1 = {__builtin_amdgcn_exp2f(__builtin_fmaf(s1.x, LOG2E, mo)), __builtin_amdgcn_exp2f(__builtin_fmaf(s1.y, LOG2E, mo))};
        const u32 p0 = pk2(e0), p1 = pk2(e1);
        *lp<u16>(pw) = (u16)p0; *lp<u16>(pw + 8) = (u16)(p0 >> 16); *lp<u16>(pw + 256) = (u16)p1; *lp<u16>(pw + 264) = (u16)(p1 >> 16);
        const v2f es = e0 + e1;
        *lp<float>(ma + (L_LS - L_M)) = row_sum(es.x + es.y);
        set_flag(F_P + w * 4, 1);
    }
    STAMP(5);
    __builtin_amdgcn_s_setprio(3);
    const int aline = WS_A + w * GA + (s * 32 + g * 4 + b) * 128;
    const int mvo = pin((4 * (kg & 1) + (l & 3)) * 16);
    const int mso = WS_ML + g * GML + b * 512 + w * 128;
    const int ovo = pin(n16 < 8 ? n16 * 64 + kg * 16 : OOB);
    const int oso = WS_O + g * GO + b * 8192 + w * 2048;
    const int avs = pin((AST16 ? (l & 35) == 0 : l < 32) ? l * 4 : OOB);
    if (w == 0) {
        wait_flag<4, 0>(F_P, 1);
        const v4f m = *lp<v4f>(L_M + (l & 3) * 16), q = *lp<v4f>(L_LS + (l & 3) * 16);
        const v4u gm = {f2u(fmx(fmx(m.x, m.y), fmx(m.z, m.w))), tag, f2u((q.x + q.y) + (q.z + q.w)), tag};
        ST128(gm, r_ws, l < 4 ? l * 512 : OOB, WS_ML + g * GML + b * 128 + s * 16, AUX_SC1);
    }
    v4u gm, av[4], wf[4][4];
    v2f W01, W23;
    auto weights = [&]() {
        const float ms = u2f(gm.x), lv = u2f(gm.z);
        float mm = fmx(ms, dppf<DPP_X1>(ms)); mm = fmx(mm, dppf<DPP_X2>(mm));
        { v2f a = swap16(mm, mm); mm = fmx(a.x, a.y); }
        const float wg = __builtin_amdgcn_exp2f((ms - mm) * LOG2E);
        float L = wg * lv; L += dppf<DPP_X1>(L); L += dppf<DPP_X2>(L);
        { v2f a = swap16(L, L); L = a.x + a.y; }
        const float W = wg * __builtin_amdgcn_rcpf(L);
        W01.x = dppf<0x00>(W); W01.y = dppf<0x55>(W); W23.x = dppf<0xAA>(W); W23.y = dppf<0xFF>(W);
        asm volatile("" : "+v"(W01), "+v"(W23));
    };
    {
        const __amdgpu_buffer_rsrc_t r_wo = mkrsrc(wo, 4u << 20);
        const int wvo = pin((16 * (lr & 3) + 2 * (lr >> 2)) * 1024 + swz);
        u32 so = (g * 4 + b) * (128 * 1024) + (w >> 1) * 65536 + (w & 1) * 8192 + s * 128;
        u32 lw = L_W + w * 4096, ms2 = mso;
        asm volatile("" : "+s"(so), "+s"(lw), "+s"(ms2) : "s"(r_wo));
        wait_flag<2, 2>(F_L, WOP);
#define WOL "buffer_load_dwordx4 %2, %3, %1 offen nt lds\n\t"
#define WON(d) "s_add_u32 m0, m0, 0x400\n\ts_add_i32 %1, %1, " #d "\n\t"
        asm volatile("s_mov_b32 m0, %4\n\t"
                     "buffer_load_dwordx4 %0, %5, %6, %7 offen sc1\n\t"
                     WOL WON(4096) WOL WON(-3072) WOL WON(4096) WOL
                     "s_waitcnt vmcnt(4)"
                     : "=&v"(gm), "+s"(so) : "v"(wvo), "s"(r_wo), "s"(lw), "v"(mvo), "s"(r_ws), "s"(ms2) : "memory", "scc");
        STAMP(6);
        const bool early = all_lanes((gm.y == tag) & (gm.w == tag));
        weights();
        wait_flag<3, 1>(F_L, 33);
        if (ODELAY) __builtin_amdgcn_s_sleep(ODELAY);
        asm volatile("buffer_load_dwordx4 %0, %4, %5, %6 offen sc1\n\tbuffer_load_dwordx4 %1, %4, %5, %6 offen offset:512 sc1\n\t"
                     "buffer_load_dwordx4 %2, %4, %5, %6 offen offset:1024 sc1\n\tbuffer_load_dwordx4 %3, %4, %5, %6 offen offset:1536 sc1\n\t"
                     "s_waitcnt vmcnt(4)"
                     : "=&v"(av[0]), "=&v"(av[1]), "=&v"(av[2]), "=&v"(av[3]) : "v"(ovo), "s"(r_ws), "s"(oso) : "memory");
        set_flag(F_W + w * 4, 1);
        STAMP(25);
        wait_flag<4, 0>(F_W, 1);
#pragma unroll
        for (int k = 0; k < 4; ++k)
#pragma unroll
            for (int ct = 0; ct < 4; ++ct) wf[k][ct] = trfrag(wa[ct] + k * 4096);
        SB();
        if (!early) {
#pragma unroll 1
            for (int spin = 0; spin < SPIN_MAX; ++spin) {
                asm volatile("buffer_load_dwordx4 %0, %1, %2, %3 offen sc1\n\ts_waitcnt vmcnt(0)" : "=v"(gm) : "v"(mvo), "s"(r_ws), "s"(ms2) : "memory");
                if (all_lanes((gm.y == tag) & (gm.w == tag))) break;
            }
            weights();
        }
    }
    STAMP(11);
    {
        asm volatile("s_waitcnt vmcnt(0)" : "+v"(av[0]), "+v"(av[1]), "+v"(av[2]), "+v"(av[3]) :: "memory");
#pragma unroll 1
        for (int spin = 0; spin < SPIN_MAX; ++spin) {
            const bool bad = umx(umx(umx4(av[0]), umx4(av[1])), umx(umx4(av[2]), umx4(av[3]))) == ~0u;
            if (all_lanes(!bad)) break;
            asm volatile("" ::: "memory");
            if (bad) {
#pragma unroll
                for (int k = 0; k < 4; ++k) av[k] = LD128(r_ws, ovo + k * 512, oso, AUX_SC1);
            }
        }
        STAMP(12);
        v4f c[4];
#pragma unroll
        for (int ct = 0; ct < 4; ++ct) c[ct] = (v4f){0.f, 0.f, 0.f, 0.f};
#pragma unroll
        for (int k = 0; k < 4; ++k) {
#pragma unroll
            for (int ct = 0; ct < 4; ++ct) c[ct] = mfma16(av[k], wf[k][ct], c[ct]);
            SB();
        }
        float z[4];
#pragma unroll
        for (int ct = 0; ct < 4; ++ct) {
            const v2f c01 = {c[ct].x, c[ct].y}, c23 = {c[ct].z, c[ct].w};
            const v2f t = __builtin_elementwise_fma(c23, W23, c01 * W01);
            z[ct] = t.x + t.y;
        }
        const v2f r0 = swap16(z[0], z[1]), r1 = swap16(z[2], z[3]);
        const u32 y = umn(pkbf(r0.x + r0.y, r1.x + r1.y), ~1u);
        const v4u ga = {y, dppu<0x55>(y), dppu<0xAA>(y), dppu<0xFF>(y)};
        ST128(ga, r_ws, avs, aline, AUX_SC1);
    }
    STAMP(13);
    if (!fin) {
        __builtin_amdgcn_endpgm();
    }

    {
        const int D = w * 64 + (n16 >> 3) * 32 + (kg >> 1) * 16 + (l & 7) * 2 + (kg & 1);
        v2f xr = bf2(*lp<u32>(L_X + b * 1024 + D * 4));
        v4u sel;
#pragma unroll
        for (int q = 0; q < 4; ++q) sel[q] = ((n16 & 2) == 0 && ((n16 >> 1) & 2) + (n16 & 1) == q) ? (n16 >= 8 ? 0x3F800000u : 0x3F80u) : 0u;
        asm volatile("" : "+v"(xr), "+v"(sel));
        const int avo = (n16 >> 3) * 4096 + kg * 128 + (l & 7) * 16;
        const int aso = WS_A + b * GA + w * 8192;
        wait_flag<4, 1>(F_T, 1);
        if (ADELAY) __builtin_amdgcn_s_sleep(ADELAY);
        v4f c0, c1;
#pragma unroll 1
        for (int spin = 0; spin < SPIN_MAX; ++spin) {
            asm volatile("" ::: "memory");
            v4u a[8];
#pragma unroll
            for (int i = 0; i < 8; ++i) a[i] = LD128(r_ws, avo + i * 512, aso, AUX_SC1);
            c0 = (v4f){0.f, 0.f, 0.f, 0.f}; c1 = c0;
            u32 mx = 0;
#pragma unroll
            for (int i = 0; i < 8; i += 2) {
                c0 = mfma16(sel, a[i], c0); c1 = mfma16(sel, a[i + 1], c1);
                mx = umx(mx, umx(umx4(a[i]), umx4(a[i + 1])));
            }
            if (all_lanes(mx != ~0u)) break;
        }
        STAMP(14);
        const v2f h = bf2(pk2(xr + bf2(pkbf(c0.x + c1.x, c0.y + c1.y))));
        const float h0 = u2f(__builtin_amdgcn_readfirstlane(f2u(h.x)));
        const float d0 = h.x - h0, d1 = h.y - h0;
        const float sd = wave_sum(d0 + d1), sdd = wave_sum(__builtin_fmaf(d0, d0, d1 * d1));
        { const v2f t = {128.f * h0 + sd, sdd - sd * sd * (1.f / 128.f)}; *lp<v2f>(l == 0 ? L_ST + w * 8 : L_JUNK) = t; }
        lds_barrier();
        const v4f s0 = *lp<v4f>(L_ST), s1 = *lp<v4f>(L_ST + 16);
        const float mean = ((s0.x + s0.z) + (s1.x + s1.z)) * (1.f / 512.f);
        const float e0 = s0.x * (1.f / 128.f) - mean, e1 = s0.z * (1.f / 128.f) - mean, e2 = s1.x * (1.f / 128.f) - mean, e3 = s1.z * (1.f / 128.f) - mean;
        const float var = fmx(__builtin_fmaf(128.f, (e0 * e0 + e1 * e1) + (e2 * e2 + e3 * e3), (s0.y + s0.w) + (s1.y + s1.w)) * (1.f / 512.f), 0.f);
        const float mb = rbf(mean);
        const float rden = __builtin_amdgcn_rcpf(rbf(__builtin_amdgcn_sqrtf(rbf(rbf(var) + 1e-5f))));
        const float mean2 = rbf((mean - mb) * rden), var2 = var * rden * rden;
        const float rden2 = __builtin_amdgcn_rcpf(rbf(__builtin_amdgcn_sqrtf(rbf(rbf(var2) + 1e-5f))));
        const v2f mb2 = {mb, mb}, rd2 = {rden, rden}, mc2 = {mean2, mean2}, re2 = {rden2, rden2};
        const v2f n1 = bf2(pk2(bf2(pk2(h - mb2)) * rd2));
        const u32 tt = pk2(bf2(pk2(n1 - mc2)) * re2);
        const u32 ta = tt & 0xffffu, tb = tt >> 16;
        const v2f y = {bflo(*lp<u16>(L_TAB + 2 * ta)), bflo(*lp<u16>(L_TAB + 2 * tb))};
        u32* o32 = (u32*)out + b * 256 + D;
        *o32 = pk2(h + y);
        if (b == 0 && w == 0 && l == 0) __builtin_amdgcn_raw_buffer_store_b32(tag, r_ws, 0, WS_EPOCH, AUX_SC1);
        STAMP(15);
        if (w == 0) { PUTST(0, 7); PUTST(11, 16); PUTST(18, 20); PUTST(24, 26); } else if (w == 3) PUTST(20, 22); else if (w == 2) PUTST(22, 23); else PUTST(23, 24);
#pragma unroll
        for (int q = 0; q < 2; ++q) {
            const u32 t = q ? tb : ta;
            if ((t & 0x7fffu) - T_LO >= 2048u) {
                const u32 i = __hip_atomic_fetch_add(lp<u32>(F_FB), 1u, __ATOMIC_RELAXED, __HIP_MEMORY_SCOPE_WORKGROUP);
                const v2u it = {(t << 16) | (u32)(2 * D + q), f2u(q ? h.y : h.x)};
                *lp<v2u>(L_FB + i * 8) = it;
            }
        }
        lds_barrier();
        const int n = __builtin_amdgcn_readfirstlane(*lp<u32>(F_FB));
        if (n <= 0) __builtin_amdgcn_endpgm();
        Coef cf;
        make_coef(cf, load_part(w1, w, l), load_part(b1, w, l), load_part(w2, w, l));
        const float b2v = u2f(pin(f2u(bflo(__builtin_amdgcn_raw_buffer_load_b16(mkrsrc(b2, 2), 0, 0, 0)))));
#pragma unroll 1
        for (int i = 0; i < n; ++i) {
            const float p = wave_sum(mlp_part(cf, bfhi(*lp<u32>(L_FB + i * 8))));
            *lp<float>(l == 0 ? L_FP + i * 16 + w * 4 : L_JUNK) = p;
        }
        __builtin_amdgcn_s_waitcnt(0x0F70);
        lds_barrier();
#pragma unroll 1
        for (int i = w * 64 + l; i < n; i += 256) {
            const v2u it = *lp<v2u>(L_FB + i * 8);
            const v4f p = *lp<v4f>(L_FP + i * 16);
            const float yv = rbf(rbf((p.x + p.y) + (p.z + p.w)) + b2v);
            out[b * 512 + (it.x & 0xffffu)] = (u16)pkbf(u2f(it.y) + yv, 0.f);
        }
    }
}

extern "C" void kernel_launch(void* const* d_in, const int* in_sizes, int n_in, void* d_out, int out_size, void* d_ws, size_t ws_size, hipStream_t stream) {
    constexpr u32 F = 0x00020000;
    fused_decode_kernel<<<256, 640, 0, stream>>>((const u16*)d_in[0], 4096u, F, (const u16*)d_in[3], 4u << 20, F, (const u16*)d_in[1], 64u << 20, F,
                                                 (const u16*)d_in[2], 64u << 20, F, (unsigned char*)d_ws, WS_END, F, (const u16*)d_in[6], 3584u, F,
                                                 (const u16*)d_in[7], 3584u, F, (const u16*)d_in[8], 3584u, F, (const u16*)d_in[9], 2u, F,
                                                 (const u16*)d_in[4], (u16*)d_out);
}
```

```cpp
#define BENCH_STAMPS
#define PROBE 0
#include <hip/hip_runtime.h>
#include <stdint.h>

typedef unsigned u32;
typedef unsigned short u16;
typedef u32 v4u __attribute__((ext_vector_type(4)));
typedef u32 v2u __attribute__((ext_vector_type(2)));
typedef float v4f __attribute__((ext_vector_type(4)));
typedef float v2f __attribute__((ext_vector_type(2)));
typedef __bf16 v8bf __attribute__((ext_vector_type(8)));
typedef __bf16 v2bf __attribute__((ext_vector_type(2)));
typedef short v4s __attribute__((ext_vector_type(4)));
#define LAS __attribute__((address_space(3)))

#define NSTAMP 26
#define SSTR 32
#define STAMP_OFF (128u << 20)
#define STAMP(i) do { __builtin_amdgcn_sched_barrier(0); tstamp[i] = __builtin_amdgcn_s_memrealtime(); __builtin_amdgcn_sched_barrier(0); } while (0)
#define PUTST(a, b) do { if (PROBE >= a && PROBE < b) { const unsigned long long tend = tstamp[0] + 3000 + 4 * (tstamp[PROBE] - tstamp[0]); while (__builtin_amdgcn_s_memrealtime() < tend) __builtin_amdgcn_s_sleep(1); } } while (0)

#define MAPG 0
#define WIN 12
#define QP 10
#define WOP 32
#define XP 0
#define PRE 0
#define TN 16
#define TWAIT 0
#define WOE 4
#define GDELAY 10
#define KFIRST 0
#define OST16 0
#define AST16 1
#define ODELAY 0
#define ADELAY 0
#define ROTA 6
#define ROTB 3
#define SPREAD 1
#define WSOFF 0
#define ILV 0
#define FINM 0
#define FINC 0
#define JUNK 15
#define T1F 480
#define WOST 5
#define TG 2
#define OSLP 0
#define OCAP 0
#define OSTEP2 0
#define ACAP 8
#define ASLP 0
#define T2F 580
#define T3F 720
#define V5FMT 4

constexpr int AUX_SC1 = 16;
constexpr u32 WS_EPOCH = 0;
constexpr u32 GQ = 262144, GO = 262144, GML = 262144, GA = 524288;
constexpr u32 WS_Q = 4096;
constexpr u32 WS_O = 65536;
constexpr u32 WS_ML = 131072;
constexpr u32 WS_A = 163840;
constexpr u32 WS_T = 262144 + 163840;
constexpr u32 WS_END = 2097152;
constexpr u32 L_V = 0, L_K = 0x10000, L_W = 0x20000, L_TN = 0x24000, L_X = 0x25000, L_XN = 0x26000, L_QI = 0x26500, L_P = 0x26A00,
              L_TP = 0x27200, L_JUNK = 0x27600, L_FL = 0x27A00, L_M = 0x27A80, L_LS = 0x27AC0, L_END = 0x27B00;
constexpr u32 F_A = L_FL, F_Q = L_FL + 16, F_M = L_FL + 32, F_P = L_FL + 48, F_W = L_FL + 64, F_L = L_FL + 80, F_ARR = L_FL + 88, F_FB = L_FL + 92, L_ST = L_FL + 96;
constexpr u32 TAG_STEP = 0x01010101u;
constexpr u32 L_TAB = 0xCE00;
constexpr u32 L_OS = L_K, L_FB = L_K + 0x2000, L_FP = L_K + 0x6000;
constexpr u32 T_LO = 114 * 128;
constexpr int SPIN_MAX = 1 << 18;
constexpr u32 CODE_BYTES = 11776;
constexpr int OOB = 0x40000000;
constexpr float LOG2E = 1.4426950408889634f;

__device__ __forceinline__ float u2f(u32 v) { return __builtin_bit_cast(float, v); }
__device__ __forceinline__ u32 f2u(float v) { return __builtin_bit_cast(u32, v); }
__device__ __forceinline__ float bflo(u32 v) { return u2f(v << 16); }
__device__ __forceinline__ float bfhi(u32 v) { return u2f(v & 0xffff0000u); }
__device__ __forceinline__ v2f bf2(u32 v) { v2f r = {bflo(v), bfhi(v)}; return r; }
__device__ __forceinline__ u32 pk2(v2f f) { return __builtin_bit_cast(u32, __builtin_convertvector(f, v2bf)); }
__device__ __forceinline__ u32 pkbf(float a, float b) { v2f f = {a, b}; return pk2(f); }
__device__ __forceinline__ float rbf(float a) { return u2f(pkbf(0.f, a)); }
template <int CTRL> __device__ __forceinline__ float dppf(float v) { return u2f((u32)__builtin_amdgcn_update_dpp(0, (int)f2u(v), CTRL, 0xf, 0xf, true)); }
template <int CTRL> __device__ __forceinline__ u32 dppu(u32 v) { return (u32)__builtin_amdgcn_update_dpp(0, (int)v, CTRL, 0xf, 0xf, true); }
constexpr int DPP_X1 = 0xB1, DPP_X2 = 0x4E, DPP_HMIR = 0x141, DPP_ROR4 = 0x124, DPP_ROR8 = 0x128, DPP_SHL4 = 0x104, DPP_SHL8 = 0x108;
__device__ __forceinline__ float fmx(float a, float b) { return __builtin_fmaxf(a, b); }
__device__ __forceinline__ u32 umn(u32 a, u32 b) { return a < b ? a : b; }
__device__ __forceinline__ u32 umx(u32 a, u32 b) { return a > b ? a : b; }
__device__ __forceinline__ u32 umx4(v4u a) { return umx(umx(a.x, a.y), umx(a.z, a.w)); }
__device__ __forceinline__ v2f swap32(float a, float b) {
    auto r = __builtin_amdgcn_permlane32_swap(f2u(a), f2u(b), false, false);
    v2f o = {u2f(r[0]), u2f(r[1])}; return o;
}
__device__ __forceinline__ v2f swap16(float a, float b) {
    auto r = __builtin_amdgcn_permlane16_swap(f2u(a), f2u(b), false, false);
    v2f o = {u2f(r[0]), u2f(r[1])}; return o;
}
__device__ __forceinline__ float sum_x16_x32(float v) { v2f a = swap16(v, v); v = a.x + a.y; a = swap32(v, v); return a.x + a.y; }
__device__ __forceinline__ float max_x16_x32(float v) { v2f a = swap16(v, v); v = fmx(a.x, a.y); a = swap32(v, v); return fmx(a.x, a.y); }
__device__ __forceinline__ float row_sum(float v) { v += dppf<DPP_X1>(v); v += dppf<DPP_X2>(v); v += dppf<DPP_HMIR>(v); v += dppf<DPP_ROR8>(v); return v; }
__device__ __forceinline__ float wave_sum(float v) { return sum_x16_x32(row_sum(v)); }
__device__ __forceinline__ v4f mfma16(v4u a, v4u b, v4f c) {
    return __builtin_amdgcn_mfma_f32_16x16x32_bf16(__builtin_bit_cast(v8bf, a), __builtin_bit_cast(v8bf, b), c, 0, 0, 0);
}
__device__ __forceinline__ void lds_barrier() { asm volatile("s_waitcnt lgkmcnt(0)\n\ts_barrier" ::: "memory"); }
#define SB() __builtin_amdgcn_sched_barrier(0)
#define WAITVM(n) asm volatile("s_waitcnt vmcnt(%0)" :: "i"(n) : "memory")
__device__ __forceinline__ __amdgpu_buffer_rsrc_t mkrsrc(const void* p, u32 bytes) { return __builtin_amdgcn_make_buffer_rsrc((void*)p, 0, bytes, 0x00020000); }
__device__ __forceinline__ bool all_lanes(bool p) { return __builtin_amdgcn_ballot_w64(!p) == 0ull; }
__device__ __forceinline__ void junk_load(__amdgpu_buffer_rsrc_t rs, int vo, u32 lds) {
    u32 keep;
    asm volatile("s_mov_b32 %0, m0\n\ts_mov_b32 m0, %3\n\ts_nop 0\n\tbuffer_load_ushort %1, %2, 0 offen lds\n\ts_mov_b32 m0, %0" : "=&s"(keep) : "v"(vo), "s"(rs), "s"(lds) : "memory");
}
#define LD128(r, vo, so, aux) __builtin_amdgcn_raw_buffer_load_b128(r, vo, so, aux)
#define ST128(d, r, vo, so, aux) do { v4u d_ = (d); asm volatile("" : "+v"(d_)); __builtin_amdgcn_raw_buffer_store_b128(d_, r, vo, so, aux); asm volatile("s_nop 1" :: "v"(d_)); } while (0)
template <typename T> __device__ __forceinline__ LAS T* lp(u32 a) { return (LAS T*)(size_t)a; }
__device__ __forceinline__ u32 pin(u32 v) { asm volatile("" : "+v"(v)); return v; }
__device__ __forceinline__ v4u trfrag(u32 a) {
    const v2u lo = __builtin_bit_cast(v2u, __builtin_amdgcn_ds_read_tr16_b64_v4i16(lp<v4s>(a)));
    const v2u hi = __builtin_bit_cast(v2u, __builtin_amdgcn_ds_read_tr16_b64_v4i16(lp<v4s>(a + 2048)));
    v4u r = {lo.x, lo.y, hi.x, hi.y}; return r;
}
__device__ __forceinline__ void set_flag(u32 a, u32 v) { asm volatile("" ::: "memory"); *lp<volatile u32>(a) = v; }
__device__ __forceinline__ u32 umn4(v4u a) { return umn(umn(a.x, a.y), umn(a.z, a.w)); }
__device__ __forceinline__ bool uni_ge(u32 x, u32 v) { return (u32)__builtin_amdgcn_readfirstlane(x) >= v; }
template <int N, int SLP> __device__ __forceinline__ void wait_flag(u32 a, u32 v) {
#pragma unroll 1
    for (int i = 0; i < SPIN_MAX; ++i) {
        u32 m;
        if (N == 1) m = *lp<volatile u32>(a);
        else if (N == 2) { const v2u f = *lp<volatile v2u>(a); m = umn(f.x, f.y); }
        else if (N == 3) { const v4u f = *lp<volatile v4u>(a); m = umn(umn(f.x, f.y), f.z); }
        else m = umn4(*lp<volatile v4u>(a));
        if (uni_ge(m, v)) break;
        if (SLP) __builtin_amdgcn_s_sleep(SLP);
    }
    asm volatile("" ::: "memory");
}
__device__ __forceinline__ u32 now32() { unsigned long long t; asm volatile("s_memrealtime %0\n\ts_waitcnt lgkmcnt(0)" : "=s"(t) :: "memory"); return (u32)t; }
struct Coef { v2f w[4], b[4], g1[4], g0[4]; };
__device__ __forceinline__ float mlp_part(const Coef& c, float t) {
    const v2f t2 = {t, t};
    const float K0 = -2.0f * LOG2E * 0.7978845608028654f, K1 = K0 * 0.044715f;
    const v2f k0 = {K0, K0}, k1 = {K1, K1}, one = {1.f, 1.f};
    v2f acc = {0.f, 0.f};
#pragma unroll
    for (int i = 0; i < 4; ++i) {
        const v2f u = __builtin_elementwise_fma(t2, c.w[i], c.b[i]);
        const v2f a = u * __builtin_elementwise_fma(u * u, k1, k0);
        v2f e = {__builtin_amdgcn_exp2f(a.x), __builtin_amdgcn_exp2f(a.y)};
        e += one;
        const v2f rc = {__builtin_amdgcn_rcpf(e.x), __builtin_amdgcn_rcpf(e.y)};
        acc = __builtin_elementwise_fma(__builtin_elementwise_fma(t2, c.g1[i], c.g0[i]), rc, acc);
    }
    return acc.x + acc.y;
}

__device__ __forceinline__ v4u load_part(const u16* p, int part, int l) { return LD128(mkrsrc(p + part * 448, 896), l * 16, 0, 0); }
__device__ __forceinline__ void make_coef(Coef& cf, v4u ta, v4u tb, v4u tc) {
#pragma unroll
    for (int i = 0; i < 4; ++i) { const v2f c = bf2(tc[i]); cf.w[i] = bf2(ta[i]); cf.b[i] = bf2(tb[i]); cf.g1[i] = cf.w[i] * c; cf.g0[i] = cf.b[i] * c; }
}

#define DESC(T, p) T p, u32, u32
__global__ void __launch_bounds__(640)
fused_decode_kernel(DESC(const u16*, x), DESC(const u16*, wq), DESC(const u16*, kc), DESC(const u16*, vc), DESC(unsigned char*, ws),
                    DESC(const u16*, w1), DESC(const u16*, b1), DESC(const u16*, w2), DESC(const u16*, b2), const u16* wo, u16* out) {
    __shared__ __attribute__((aligned(16))) unsigned char smem[L_END];
    unsigned long long tstamp[NSTAMP];
    STAMP(0);
    u32 tag0, tag, b2r;
    v4u ca, cb, cc;
    int wave2;
    unsigned long long tstart;
    const int pid = blockIdx.x;
    {
        u32 t0, t1, t2, t3;
#define WQL "s_add_u32 m0, m0, 0x400\n s_add_u32 s73, s73, 0x10000\n buffer_load_dwordx4 %[t3], s[40:43], s73 offen nt lds\n"
#define KVL "buffer_load_dwordx4 %[t3], s[44:47], s73 offen nt lds\n s_add_u32 m0, m0, 0x400\n s_add_u32 s73, s73, 0x800\n"
        asm volatile(
            "s_load_dwordx16 s[36:51], %[ka], 0x0\n"
            "s_load_dwordx16 s[52:67], %[ka], 0x40\n"
            "s_memrealtime %[ts]\n"
            "s_getpc_b64 s[76:77]\n"
            "v_readfirstlane_b32 %[wv], %[tid]\n"
            "v_bfe_u32 %[t0], %[tid], 3, 3\n"
            "v_and_b32 %[t1], 6, %[t0]\n"
            "v_xor_b32 %[t1], %[t1], %[tid]\n"
            "v_and_b32 %[t1], 7, %[t1]\n"
            "v_lshlrev_b32 %[t1], 4, %[t1]\n"
            "s_lshr_b32 %[wv], %[wv], 6\n"
            "s_and_b32 s72, %[pid], 7\n"
            "s_cmp_gt_u32 %[wv], 3\n"
            "s_cbranch_scc1 2f\n"
            "s_setprio 2\n"
            "v_lshlrev_b32 %[t2], 4, %[tid]\n"
            "v_lshl_or_b32 %[t3], %[t0], 13, %[t1]\n"
            "s_lshr_b32 s75, %[pid], 6\n"
            "s_lshl_b32 s73, s75, 2\n"
            "s_add_u32 s73, s73, %[wv]\n"
            "s_lshl_b32 s73, s73, 11\n"
            "s_and_b32 s74, %[pid], 63\n"
            "s_or_b32 s73, s73, s74\n"
            "s_lshl_b32 s73, s73, 7\n"
            "s_lshl_b32 s74, %[wv], 10\n"
            "s_add_u32 m0, s74, %[lx]\n"
            "s_lshl_b32 s74, s74, 2\n"
            "s_add_u32 s74, s74, %[lw]\n"
            "v_bfe_u32 %[t0], %[tid], 4, 2\n"
            "v_and_b32 %[t1], 15, %[tid]\n"
            "v_lshlrev_b32 %[t1], 2, %[t1]\n"
            "v_lshl_or_b32 %[t0], %[t0], 9, %[t1]\n"
            "s_and_b32 s78, %[pid], 56\n"
            "s_lshl_b32 s78, s78, 3\n"
            "s_lshl1_add_u32 s78, %[wv], s78\n"
            "s_lshl1_add_u32 s78, s78, s75\n"
            "s_lshl_b32 s78, s78, 5\n"
            "s_or_b32 s78, s78, s72\n"
            "s_lshl_b32 s78, s78, 6\n"
            "s_add_u32 s78, s78, %[wso]\n"
            "v_mov_b32 %[t1], -1\n"
            "s_waitcnt lgkmcnt(0)\n"
            "buffer_load_dwordx4 %[t2], s[36:39], 0 offen lds\n"
            "s_mov_b32 m0, s74\n"
            "s_nop 0\n"
            "buffer_load_dwordx4 %[t3], s[40:43], s73 offen nt lds\n"
            WQL WQL WQL
            "buffer_load_dword %[tag], off, s[52:55], 0\n"
            "buffer_store_dword %[t1], %[t0], s[52:55], s78 offen sc1\n"
            "s_barrier\n"
            "s_branch 9f\n"
            "2:\n"
            "s_cmp_gt_u32 %[wv], 5\n"
            "s_cbranch_scc1 5f\n"
            "s_setprio 3\n"
            "s_sub_u32 s75, %[wv], 4\n"
            "v_lshl_or_b32 %[t3], %[t0], 8, %[t1]\n"
            "s_lshr_b32 s74, %[pid], 3\n"
            "s_mul_i32 s73, s74, %[rotb]\n"
            "s_mul_i32 s78, s72, %[rota]\n"
            "s_add_u32 s73, s73, s78\n"
            "s_lshl_b32 s73, s73, 5\n"
            "s_or_b32 s73, s73, s75\n"
            "s_lshl_b32 s73, s73, 7\n"
            "s_lshl_b32 s74, s74, 5\n"
            "s_or_b32 s74, s74, s72\n"
            "s_or_b32 s74, s74, 0x18\n"
            "s_pack_ll_b32_b16 s73, s73, s74\n"
            "s_lshl_b32 s78, s75, 15\n"
            "s_add_u32 m0, s78, %[lk]\n"
            "s_lshl2_add_u32 s75, s75, %[fl]\n"
            "v_mov_b32 %[t0], s75\n"
            "v_mov_b32 %[t1], 0\n"
            "s_mov_b32 s75, 0\n"
            "s_waitcnt lgkmcnt(0)\n"
            "s_mov_b64 s[70:71], %[ts]\n"
            "s_add_u32 s79, s70, %[t1f]\n"
            "s_barrier\n"
            "3:\n"
            "s_waitcnt vmcnt(%[win])\n"
            KVL KVL
            "s_pack_ll_b32_b16 s73, s73, s74\n"
            "ds_write_b32 %[t0], %[t1]\n"
            "s_add_u32 s75, s75, 1\n"
            "v_add_u32 %[t1], 1, %[t1]\n"
            "s_cmp_lg_u32 s75, 16\n"
            "s_cbranch_scc1 3b\n"
            "s_mov_b64 s[44:45], s[48:49]\n"
            "s_mov_b32 m0, s78\n"
            "4:\n"
            "s_waitcnt vmcnt(%[win])\n"
            "s_memrealtime s[70:71]\n"
            "ds_write_b32 %[t0], %[t1]\n"
            "v_add_u32 %[t1], 1, %[t1]\n"
            "6:\n"
            KVL KVL
            "s_pack_ll_b32_b16 s73, s73, s74\n"
            "s_sub_u32 s75, s75, 1\n"
            "s_cmp_eq_u32 s75, 0\n"
            "s_cbranch_scc1 7f\n"
            "s_waitcnt lgkmcnt(0)\n"
            "s_cmp_gt_u32 s75, %[rem]\n"
            "s_cbranch_scc1 4b\n"
            "s_sub_i32 s72, s70, s79\n"
            "s_cmp_lt_i32 s72, 0\n"
            "s_cbranch_scc1 4b\n"
            "s_branch 6b\n"
            "7:\n"
            "s_waitcnt lgkmcnt(0)\n"
            "s_memrealtime s[70:71]\n"
            "s_waitcnt vmcnt(32) lgkmcnt(0)\n"
            "s_sub_i32 s72, s70, s79\n"
            "s_cmp_lt_i32 s72, %[t2f]\n"
            "s_cselect_b32 s72, 31, 32\n"
            "v_mov_b32 %[t1], s72\n"
            "ds_write_b32 %[t0], %[t1]\n"
            "s_branch 9f\n"
            "5:\n"
            "s_load_dwordx4 s[68:71], %[ka], 0x80\n"
            "s_sub_u32 s75, %[wv], 6\n"
            "s_and_b32 s74, %[pid], 56\n"
            "s_lshl1_add_u32 s74, s75, s74\n"
            "s_lshl_b32 s74, s74, 8\n"
            "s_add_u32 s78, s74, 512\n"
            "s_min_u32 s78, s78, %[code]\n"
            "s_mov_b32 s79, 0x20000\n"
            "s_and_b32 s76, s76, 0xffffff00\n"
            "v_and_b32 %[t2], 63, %[tid]\n"
            "v_cmp_gt_u32 vcc, 56, %[t2]\n"
            "v_lshl_add_u32 %[t3], %[t2], 7, s74\n"
            "buffer_load_dword %[b2r], %[t3], s[76:79], 0 offen\n"
            "v_and_b32 %[t0], 31, %[tid]\n"
            "v_lshlrev_b32 %[t0], 2, %[t0]\n"
            "v_add_u32 %[t0], %[lfl], %[t0]\n"
            "v_mov_b32 %[t1], 0\n"
            "ds_write_b32 %[t0], %[t1]\n"
            "s_mulk_i32 s75, 896\n"
            "v_lshl_add_u32 %[t3], %[t2], 4, s75\n"
            "v_cndmask_b32 %[t3], 2.0, %[t3], vcc\n"
            "s_waitcnt lgkmcnt(0)\n"
            "s_barrier\n"
            "buffer_load_dwordx4 %[ca], %[t3], s[56:59], 0 offen\n"
            "buffer_load_dwordx4 %[cb], %[t3], s[60:63], 0 offen\n"
            "buffer_load_dwordx4 %[cc], %[t3], s[64:67], 0 offen\n"
            "buffer_load_dword %[tag], off, s[52:55], 0\n"
            "buffer_load_ushort %[b2r], off, s[68:71], 0\n"
            "9:\n"
            : [wv] "=&s"(wave2), [ts] "=&s"(tstart), [tag] "=&v"(tag0), [b2r] "=&v"(b2r), [ca] "=&v"(ca), [cb] "=&v"(cb), [cc] "=&v"(cc),
              [t0] "=&v"(t0), [t1] "=&v"(t1), [t2] "=&v"(t2), [t3] "=&v"(t3)
            : [ka] "s"(__builtin_amdgcn_kernarg_segment_ptr()), [pid] "s"(pid), [tid] "v"(threadIdx.x), "s"((u32)(size_t)(LAS void*)smem),
              [lx] "i"(L_X), [lw] "i"(L_W), [lk] "i"(L_K), [fl] "i"(F_L), [lfl] "i"(L_FL), [wso] "i"(WS_O), [code] "i"(CODE_BYTES), [win] "i"(WIN - 2), [rota] "i"(ROTA), [rotb] "i"(ROTB), [t1f] "i"(T1F), [t2f] "i"(T2F - T1F), [rem] "i"(15 - (WIN - 2) / 2)
            : "memory", "scc", "vcc", "s36", "s37", "s38", "s39", "s40", "s41", "s42", "s43", "s44", "s45", "s46", "s47", "s48", "s49", "s50", "s51", "s52", "s53", "s54", "s55", "s56", "s57", "s58", "s59", "s60", "s61", "s62", "s63", "s64", "s65", "s66", "s67", "s68", "s69", "s70", "s71", "s72", "s73", "s74", "s75", "s76", "s77", "s78", "s79");
        static_assert(L_V == 0 && OOB == 0x40000000 && F_ARR == F_L + 8, "");
    }
    SB();
    const int l = threadIdx.x & 63;
    const int s = pid & 7, g = (pid >> 3) & 7, b = pid >> 6;
    const int n16 = l & 15, kg = l >> 4, lr = l >> 3;
    const int swz = ((l & 7) ^ (lr & 6)) * 16;
    const __amdgpu_buffer_rsrc_t r_ws = mkrsrc(ws, WS_END);
    const u32 fra = (kg * 4 + (n16 >> 2)) * 128 + (((kg & 1) * 2 + (n16 >> 3)) * 32) + (n16 & 3) * 8;
    const bool fin = (pid & 63) == FINM * b + FINC;

    if (wave2 >= 4 && wave2 < 6) {
        STAMP(7);
        const int dh = wave2 - 4;
        const u32 lv = L_V + dh * 32768, fl = F_L + dh * 4;
        const u32 pa = pin(L_P + kg * 64 + (n16 >> 2) * 16);
        u32 va[4];
#pragma unroll
        for (int t = 0; t < 4; ++t) va[t] = pin((lv + fra) ^ (t * 32));
        const int ovo = pin(kg * 8192 + (n16 >> 3) * 512 + (n16 & 7) * 8);
        const int oso = WS_O + g * GO + b * 2048 + dh * 1024 + s * 64;
        wait_flag<4, 1>(F_P, 1);
        STAMP(16);
        v4u pf[8];
#pragma unroll
        for (int st = 0; st < 8; ++st) pf[st] = *lp<v4u>(pa + st * 256);
        v4f acc[4];
#pragma unroll
        for (int t = 0; t < 4; ++t) acc[t] = (v4f){0.f, 0.f, 0.f, 0.f};
        static_assert(WIN <= 16 && WIN % 4 == 0, "");
        constexpr int ST0 = 5;
#pragma unroll
        for (int st = 0; st < ST0; ++st) {
            WAITVM(28 - 4 * st);
            if (st == WOST) set_flag(fl, 32);
#pragma unroll
            for (int t = 0; t < 4; ++t) acc[t] = mfma16(pf[st], trfrag(va[t] + st * 4096), acc[t]);
        }
        SB();
        STAMP(17);
#pragma unroll
        for (int st = ST0; st < 8; ++st) {
            v2u lo[4], hi[4];
            WAITVM(30 - 4 * st);
#pragma unroll
            for (int t = 0; t < 4; ++t) lo[t] = __builtin_bit_cast(v2u, __builtin_amdgcn_ds_read_tr16_b64_v4i16(lp<v4s>(va[t] + st * 4096)));
            WAITVM(28 - 4 * st);
            if (st == WOST) set_flag(fl, 32);
            if (st == 7) STAMP(8);
#pragma unroll
            for (int t = 0; t < 4; ++t) hi[t] = __builtin_bit_cast(v2u, __builtin_amdgcn_ds_read_tr16_b64_v4i16(lp<v4s>(va[t] + st * 4096 + 2048)));
#pragma unroll
            for (int t = 0; t < 4; ++t) { const v4u bf = {lo[t].x, lo[t].y, hi[t].x, hi[t].y}; acc[t] = mfma16(pf[st], bf, acc[t]); }
            SB();
        }
        const v2u go = {umn(pkbf(acc[0].x, acc[1].x), ~1u), umn(pkbf(acc[2].x, acc[3].x), ~1u)};
        __builtin_amdgcn_raw_buffer_store_b64(go, r_ws, ovo, oso, AUX_SC1);
        set_flag(fl, 33);
        STAMP(9);
        if (dh == 0) { PUTST(7, 10); PUTST(16, 18); }
        __builtin_amdgcn_endpgm();
    }

    if (wave2 >= 6) {
        const int tw = wave2 - 6;
        const int tj = s * 32 + g * 4 + b;
        const v4u ones = {~0u, ~0u, ~0u, ~0u};
        ST128(ones, r_ws, l < 8 ? l * 16 : OOB, WS_A + tw * GA + (s * 32 + g * 4 + b) * 128, AUX_SC1);
        {
            const u16* p = tw == 0 ? vc + ((size_t)(b * 8 + g) * 8192 + 6144 + s * 256) * 128 : tw == 1 ? wo + (g * 4 + b) * 65536 : out;
            junk_load(mkrsrc(p, ((JUNK >> tw) & 1) && (tw != 2 || fin || JUNK >= 8) ? 2 : 0), l * 4, L_JUNK);
        }
        if (TWAIT) wait_flag<4, 4>(F_A, TWAIT);
        asm volatile("s_waitcnt vmcnt(2)" : "+v"(ca), "+v"(cb), "+v"(cc), "+v"(tag0), "+v"(b2r) :: "memory");
        SB();
        Coef cf;
        make_coef(cf, ca, cb, cc);
        tag = tag0 + TAG_STEP;
        {
            float t = u2f((u32)(((tj & 128) << 8) + T_LO + (tj & 127)) << 16);
            u32 ta = n16 == 0 ? L_TP + tw * 16 + kg * 4 : L_JUNK;
#pragma unroll 1
            for (int e = 0; e < TN; ++e) {
                *lp<float>(ta) = row_sum(mlp_part(cf, t));
                ta += n16 == 0 ? 64 : 0;
                t += t;
            }
        }
        WAITVM(0);
        {
            u32 arr = 0;
            if (l == 0) arr = __hip_atomic_fetch_add(lp<u32>(F_ARR), 9u, __ATOMIC_RELAXED, __HIP_MEMORY_SCOPE_WORKGROUP);
            asm volatile("" ::: "memory");
            if (__builtin_amdgcn_readfirstlane(arr) == 27) {
                const v4f p = *lp<v4f>(L_TP + l * 16);
                float v = (p.x + p.y) + (p.z + p.w);
                v += dppf<DPP_X1>(v); v += dppf<DPP_X2>(v);
                v = rbf(v) + bflo(b2r);
                const u32 p0 = pkbf(v, dppf<DPP_SHL4>(v));
                const v4u gt = {p0, tag, dppu<DPP_SHL8>(p0), tag};
                ST128(gt, r_ws, n16 == 0 ? kg * 16 : OOB, WS_T + tj * 64, AUX_SC1);
            }
        }
        STAMP(10);
        if (tw == 0) PUTST(10, 11);
        if (!fin) __builtin_amdgcn_endpgm();
        __builtin_amdgcn_s_setprio(1);
        if (TG == 0) __builtin_amdgcn_s_sleep(GDELAY);
        if (TG == 1) wait_flag<2, 4>(F_L, 32);
        if (TG == 2) wait_flag<2, 4>(F_L, 33);
        if (TG == 3) wait_flag<4, 4>(F_W, 1);
        {
            v4u t[4];
            bool bad = true;
#pragma unroll 1
            for (int spin = 0; spin < SPIN_MAX; ++spin) {
                asm volatile("" ::: "memory");
                if (bad) {
#pragma unroll
                    for (int q = 0; q < 4; ++q) t[q] = LD128(r_ws, l * 16 + q * 1024, WS_T + tw * 4096, AUX_SC1);
                }
                u32 e = 0;
#pragma unroll
                for (int q = 0; q < 4; ++q) e |= (t[q].y ^ tag) | (t[q].w ^ tag);
                bad = e != 0;
                if (all_lanes(!bad)) break;
            }
            if (TG == 0) wait_flag<4, 2>(F_P, 1);
            const u32 a = pin(L_TAB + 2 * T_LO + (tw >> 1) * 65536 + ((tw & 1) * 64 + (l >> 2)) * 2 + (l & 3) * 1024);
#pragma unroll
            for (int q = 0; q < 4; ++q) {
                *lp<u16>(a + q * 32) = (u16)t[q].x; *lp<u16>(a + q * 32 + 256) = (u16)(t[q].x >> 16);
                *lp<u16>(a + q * 32 + 512) = (u16)t[q].z; *lp<u16>(a + q * 32 + 768) = (u16)(t[q].z >> 16);
            }
        }
        const int D = tw * 64 + (n16 >> 3) * 32 + (kg >> 1) * 16 + (l & 7) * 2 + (kg & 1);
        v2f xr = bf2(*lp<u32>(L_X + b * 1024 + D * 4));
        v4u sel, a[8];
#pragma unroll
        for (int q = 0; q < 4; ++q) sel[q] = ((n16 & 2) == 0 && ((n16 >> 1) & 2) + (n16 & 1) == q) ? (n16 >= 8 ? 0x3F800000u : 0x3F80u) : 0u;
#pragma unroll
        for (int i = 0; i < 8; ++i) a[i] = ones;
        const int avo = pin((n16 >> 3) * 4096 + kg * 128 + (l & 7) * 16);
        const int aso = WS_A + b * GA + tw * 8192;
        const u32 sta = pin(l == 0 ? L_ST + tw * 8 : L_JUNK);
        const int ooff = pin(b * 1024 + D * 4);
        const __amdgpu_buffer_rsrc_t r_out = mkrsrc(out, 4096);
#define AREG(c) c "{v[128:131]}"(a[0]), c "{v[132:135]}"(a[1]), c "{v[136:139]}"(a[2]), c "{v[140:143]}"(a[3]), c "{v[144:147]}"(a[4]), c "{v[148:151]}"(a[5]), c "{v[152:155]}"(a[6]), c "{v[156:159]}"(a[7])
        asm volatile("" : "+v"(xr), "+v"(sel), AREG("+") : "s"(aso), "s"(r_out));
        wait_flag<4, 2>(F_A, 3);
        __builtin_amdgcn_s_setprio(3);
        if (ADELAY) __builtin_amdgcn_s_sleep(ADELAY);
        {
            u32 t0, t1, n = SPIN_MAX;
#define ACHK(r0, r1, r2, r3, r4, r5, r6, r7, m0, m1) \
            "v_max3_u32 %[t0], v" #r0 ", v" #r1 ", v" #r2 "\n v_max3_u32 %[t1], v" #r4 ", v" #r5 ", v" #r6 "\n" \
            "v_max_u32 %[t0], %[t0], v" #r3 "\n v_max_u32 %[t1], %[t1], v" #r7 "\n" \
            "v_cmp_eq_u32 s[" m0 "], -1, %[t0]\n v_cmp_eq_u32 s[" m1 "], -1, %[t1]\n"
#define ALD(r, m, o) "s_mov_b64 exec, s[" m "]\n buffer_load_dwordx4 v[" r "], %[vo], %[rs], %[so] offen offset:" #o " sc1\n"
            asm volatile("1:\n"
                ACHK(128, 129, 130, 131, 132, 133, 134, 135, "36:37", "38:39") ACHK(136, 137, 138, 139, 140, 141, 142, 143, "40:41", "42:43")
                ACHK(144, 145, 146, 147, 148, 149, 150, 151, "44:45", "46:47") ACHK(152, 153, 154, 155, 156, 157, 158, 159, "48:49", "50:51")
                "s_or_b64 s[52:53], s[36:37], s[38:39]\n s_or_b64 s[54:55], s[40:41], s[42:43]\n s_or_b64 s[56:57], s[44:45], s[46:47]\n"
                "s_or_b64 s[52:53], s[52:53], s[54:55]\n s_or_b64 s[54:55], s[48:49], s[50:51]\n s_or_b64 s[52:53], s[52:53], s[56:57]\n"
                "s_or_b64 s[52:53], s[52:53], s[54:55]\n"
                "s_cbranch_scc0 2f\n"
                ALD("128:131", "36:37", 0) ALD("132:135", "38:39", 512) ALD("136:139", "40:41", 1024) ALD("140:143", "42:43", 1536)
                ALD("144:147", "44:45", 2048) ALD("148:151", "46:47", 2560) ALD("152:155", "48:49", 3072) ALD("156:159", "50:51", 3584)
                "s_mov_b64 exec, -1\n"
                "s_waitcnt vmcnt(%[cap])\n"
                "s_sub_u32 %[n], %[n], 1\n"
                "s_cbranch_scc0 1b\n"
                "2:\n"
                : AREG("+"), [t0] "=&v"(t0), [t1] "=&v"(t1), [n] "+s"(n)
                : [vo] "v"(avo), [rs] "s"(r_ws), [so] "s"(aso), [cap] "i"(ACAP), [slp] "i"(ASLP)
                : "memory", "scc", "s36", "s37", "s38", "s39", "s40", "s41", "s42", "s43", "s44", "s45", "s46", "s47", "s48", "s49", "s50", "s51", "s52", "s53", "s54", "s55", "s56", "s57");
        }
        STAMP(14);
        v4f c0 = {0.f, 0.f, 0.f, 0.f}, c1 = c0;
#pragma unroll
        for (int i = 0; i < 8; i += 2) { c0 = mfma16(sel, a[i], c0); c1 = mfma16(sel, a[i + 1], c1); }
        const v2f h = bf2(pk2(xr + bf2(pkbf(c0.x + c1.x, c0.y + c1.y))));
        const float h0 = u2f(__builtin_amdgcn_readfirstlane(f2u(h.x)));
        const float d0 = h.x - h0, d1 = h.y - h0;
        const float sd = wave_sum(d0 + d1), sdd = wave_sum(__builtin_fmaf(d0, d0, d1 * d1));
        { const v2u t = {f2u(128.f * h0 + sd), f2u(sdd - sd * sd * (1.f / 128.f)) | 1u}; *lp<v2u>(sta) = t; }
        v4f s0, s1;
#pragma unroll 1
        for (int spin = 0; spin < SPIN_MAX; ++spin) {
            s0 = *lp<volatile v4f>(L_ST); s1 = *lp<volatile v4f>(L_ST + 16);
            if (uni_ge(umn(umn(f2u(s0.y), f2u(s0.w)), umn(f2u(s1.y), f2u(s1.w))), 1)) break;
        }
        asm volatile("" ::: "memory");
        const float mean = ((s0.x + s0.z) + (s1.x + s1.z)) * (1.f / 512.f);
        const float e0 = s0.x * (1.f / 128.f) - mean, e1 = s0.z * (1.f / 128.f) - mean, e2 = s1.x * (1.f / 128.f) - mean, e3 = s1.z * (1.f / 128.f) - mean;
        const float var = fmx(__builtin_fmaf(128.f, (e0 * e0 + e1 * e1) + (e2 * e2 + e3 * e3), (s0.y + s0.w) + (s1.y + s1.w)) * (1.f / 512.f), 0.f);
        const float mb = rbf(mean);
        const float rden = __builtin_amdgcn_rcpf(rbf(__builtin_amdgcn_sqrtf(rbf(rbf(var) + 1e-5f))));
        const float mean2 = rbf((mean - mb) * rden), var2 = var * rden * rden;
        const float rden2 = __builtin_amdgcn_rcpf(rbf(__builtin_amdgcn_sqrtf(rbf(rbf(var2) + 1e-5f))));
        const v2f mb2 = {mb, mb}, rd2 = {rden, rden}, mc2 = {mean2, mean2}, re2 = {rden2, rden2};
        const v2f n1 = bf2(pk2(bf2(pk2(h - mb2)) * rd2));
        const u32 tt = pk2(bf2(pk2(n1 - mc2)) * re2);
        const u32 ta = tt & 0xffffu, tb = tt >> 16;
        const v2f y = {bflo(*lp<u16>(L_TAB + 2 * ta)), bflo(*lp<u16>(L_TAB + 2 * tb))};
        __builtin_amdgcn_raw_buffer_store_b32(pk2(h + y), r_out, ooff, 0, 0);
        if (b == 0 && tw == 0 && l == 0) __builtin_amdgcn_raw_buffer_store_b32(tag, r_ws, 0, WS_EPOCH, AUX_SC1);
        STAMP(15);
        if (tw == 0) PUTST(14, 16);
#pragma unroll
        for (int q = 0; q < 2; ++q) {
            const u32 t = q ? tb : ta;
            if ((t & 0x7fffu) - T_LO >= 2048u) {
                const u32 i = __hip_atomic_fetch_add(lp<u32>(F_FB), 1u, __ATOMIC_RELAXED, __HIP_MEMORY_SCOPE_WORKGROUP);
                const v2u it = {(t << 16) | (u32)(2 * D + q), f2u(q ? h.y : h.x)};
                *lp<v2u>(L_FB + i * 8) = it;
            }
        }
        lds_barrier();
        const int n = __builtin_amdgcn_readfirstlane(*lp<u32>(F_FB));
        if (n > 0) {
#pragma unroll 1
            for (int i = 0; i < n; ++i) {
                const float p = wave_sum(mlp_part(cf, bfhi(*lp<u32>(L_FB + i * 8))));
                *lp<float>(l == 0 ? L_FP + i * 16 + tw * 4 : L_JUNK) = p;
            }
            __builtin_amdgcn_s_waitcnt(0x0F70);
            lds_barrier();
#pragma unroll 1
            for (int i = tw * 64 + l; i < n; i += 256) {
                const v2u it = *lp<v2u>(L_FB + i * 8);
                const v4f p = *lp<v4f>(L_FP + i * 16);
                const float yv = rbf(rbf((p.x + p.y) + (p.z + p.w)) + bflo(b2r));
                out[b * 512 + (it.x & 0xffffu)] = (u16)pkbf(u2f(it.y) + yv, 0.f);
            }
        }
        asm volatile("" :: AREG(""));
        __builtin_amdgcn_endpgm();
    }

    const int w = wave2;
    v4u tb;
    u32 wa[4];
#pragma unroll
    for (int ct = 0; ct < 4; ++ct) wa[ct] = pin((L_W + fra) ^ (ct * 32));
    {
        const u32 xb = pin(L_X + w * 1024 + l * 16);
        const u32 xa = pin(kg == b ? L_XN + w * 320 + (n16 >> 2) * 64 + (n16 & 1) * 32 + ((n16 >> 1) & 1) * 8 : L_JUNK);
        const u32 xf = pin(L_XN + (n16 >> 2) * 320 + kg * 16);
        const u32 wq0 = pin((L_W + fra) ^ (w * 32));
        const int qvo = pin((n16 & 3) == 0 ? (n16 >> 2) * 8192 + kg * 16 : OOB);
        WAITVM(6);
        STAMP(1);
        const v4u xr = *lp<v4u>(xb);
        v2f xv[4];
#pragma unroll
        for (int q = 0; q < 4; ++q) xv[q] = bf2(xr[q]);
        const float c = u2f(__builtin_amdgcn_readfirstlane(f2u(xv[0].x)));
        const v2f c2 = {c, c};
        v2f d[4];
#pragma unroll
        for (int q = 0; q < 4; ++q) d[q] = xv[q] - c2;
        const v2f s2 = (d[0] + d[1]) + (d[2] + d[3]);
        const v2f q2 = __builtin_elementwise_fma(d[0], d[0], d[1] * d[1]) + __builtin_elementwise_fma(d[2], d[2], d[3] * d[3]);
        const float sd = wave_sum(s2.x + s2.y) * (1.f / 512.f), sq = wave_sum(q2.x + q2.y) * (1.f / 512.f);
        const float var = fmx(__builtin_fmaf(-sd, sd, sq), 0.f);
        const float mb = rbf(c + sd);
        const float rden = __builtin_amdgcn_rcpf(rbf(__builtin_amdgcn_sqrtf(rbf(rbf(var) + 1e-5f))));
        const v2f mb2 = {mb, mb}, rd2 = {rden, rden};
        v4u o;
#pragma unroll
        for (int q = 0; q < 4; ++q) o[q] = pk2(bf2(pk2(xv[q] - mb2)) * rd2);
        const v2u o0 = {o.x, o.y}, o1 = {o.z, o.w};
        *lp<v2u>(xa) = o0; *lp<v2u>(xa + 16) = o1;
        asm volatile("s_waitcnt vmcnt(0)\n\tv_add_u32 %0, %2, %1" : "=v"(tag) : "v"(tag0), "i"(TAG_STEP) : "memory");
        set_flag(F_A + w * 4, 1);
#pragma unroll
        for (int i = 0; i < 4; ++i) tb[i] = (tag >> (8 * i)) & 255u;
        v4u af[4], bf[4];
        if (PRE) wait_flag<4, PRE>(F_A, 1);
#pragma unroll 1
        for (int spin = 0; spin < SPIN_MAX; ++spin) {
            const v4u f = *lp<volatile v4u>(F_A);
            asm volatile("" ::: "memory");
#pragma unroll
            for (int t = 0; t < 4; ++t) { af[t] = *lp<v4u>(xf + t * 64); bf[t] = trfrag(wq0 + t * 4096); }
            SB();
            if (uni_ge(umn4(f), 1)) break;
        }
        v4f acc = {0.f, 0.f, 0.f, 0.f};
#pragma unroll
        for (int t = 0; t < 4; ++t) acc = mfma16(bf[t], af[t], acc);
        v4u gq;
#pragma unroll
        for (int i = 0; i < 4; ++i) gq[i] = (f2u(acc[i]) & ~255u) | tb[i];
        ST128(gq, r_ws, qvo, WS_Q + g * GQ + (s >> 1) * 2048 + b * 512 + (s & 1) * 256 + w * 64, AUX_SC1);
        set_flag(F_A + w * 4, 2);
    }
    STAMP(2);
    {
        const int qso = WS_Q + g * GQ + b * 8192 + w * 2048;
        const u32 qw = pin(L_QI + w * 320 + (l & 31) * 8 + (l >> 5) * 4);
        wait_flag<1, 2>(F_L, QP);
        STAMP(3);
        v4u p[2], c[2];
        bool bad = true;
#pragma unroll 1
        for (int spin = 0; spin < SPIN_MAX; ++spin) {
            asm volatile("" ::: "memory");
            if (bad) {
#pragma unroll
                for (int k = 0; k < 2; ++k) p[k] = LD128(r_ws, l * 16 + k * 1024, qso, AUX_SC1);
            }
            c[0] = p[0] ^ tb; c[1] = p[1] ^ tb;
            const v4u o = c[0] | c[1];
            bad = (((o.x | o.y) | (o.z | o.w)) & 255u) != 0;
            if (all_lanes(!bad)) break;
        }
        const v4f qs = __builtin_bit_cast(v4f, c[0]) + __builtin_bit_cast(v4f, c[1]);
        const v2f q0 = swap32(qs.x, qs.z), q1 = swap32(qs.y, qs.w);
        *lp<u32>(qw) = pkbf(q0.x + q0.y, q1.x + q1.y);
        set_flag(F_Q + w * 4, 1);
    }
    STAMP(4);
    {
        const u32 qa = pin(L_QI + (n16 >> 2) * 320 + kg * 16);
        const u32 ma = pin(n16 == 0 ? L_M + kg * 16 + w * 4 : L_JUNK), mr = pin(L_M + kg * 16);
        const u32 pw = pin(L_P + w * 512 + (n16 >> 2) * 64 + kg * 16 + (n16 & 3) * 2);
        const u32 k0 = pin(L_K + w * 8192 + n16 * 128 + ((kg ^ (n16 & 6)) * 16)), k1 = pin(k0 ^ 64);
        wait_flag<2, 1>(F_L, 4 * w + 4 + (WIN - 2) / 2);
        STAMP(24);
        v4u kf[4][4];
#pragma unroll
        for (int t = 0; t < 4; ++t)
#pragma unroll
            for (int u = 0; u < 4; ++u) kf[t][u] = *lp<v4u>(((t & 1) ? k1 : k0) + (t >> 1) * 32768 + u * 2048);
        SB();
        v4u qf[4];
        if (PRE) wait_flag<4, PRE>(F_Q, 1);
#pragma unroll 1
        for (int spin = 0; spin < SPIN_MAX; ++spin) {
            const v4u f = *lp<volatile v4u>(F_Q);
            asm volatile("" ::: "memory");
#pragma unroll
            for (int t = 0; t < 4; ++t) qf[t] = *lp<v4u>(qa + t * 64);
            SB();
            if (uni_ge(umn4(f), 1)) break;
        }
        STAMP(18);
        v4f acc[4];
#pragma unroll
        for (int u = 0; u < 4; ++u) acc[u] = (v4f){0.f, 0.f, 0.f, 0.f};
#pragma unroll
        for (int t = 0; t < 4; ++t)
#pragma unroll
            for (int u = 0; u < 4; ++u) acc[u] = mfma16(qf[t], kf[t][u], acc[u]);
        const v2f scale = {0.08838834764831845f, 0.08838834764831845f};
        const v2f s0 = bf2(pk2(bf2(pkbf(acc[0].x, acc[1].x)) * scale)), s1 = bf2(pk2(bf2(pkbf(acc[2].x, acc[3].x)) * scale));
        float m = fmx(fmx(s0.x, s0.y), fmx(s1.x, s1.y));
        m = fmx(m, dppf<DPP_X1>(m)); m = fmx(m, dppf<DPP_X2>(m)); m = fmx(m, dppf<DPP_HMIR>(m)); m = fmx(m, dppf<DPP_ROR8>(m));
        *lp<float>(ma) = m;
        set_flag(F_M + w * 4, 1);
        v4f mv;
        if (PRE) wait_flag<4, PRE>(F_M, 1);
#pragma unroll 1
        for (int spin = 0; spin < SPIN_MAX; ++spin) {
            const v4u f = *lp<volatile v4u>(F_M);
            asm volatile("" ::: "memory");
            mv = *lp<v4f>(mr);
            SB();
            if (uni_ge(umn4(f), 1)) break;
        }
        STAMP(19);
        const float mo = -fmx(fmx(mv.x, mv.y), fmx(mv.z, mv.w)) * LOG2E;
        const v2f e0 = {__builtin_amdgcn_exp2f(__builtin_fmaf(s0.x, LOG2E, mo)), __builtin_amdgcn_exp2f(__builtin_fmaf(s0.y, LOG2E, mo))};
        const v2f e1 = {__builtin_amdgcn_exp2f(__builtin_fmaf(s1.x, LOG2E, mo)), __builtin_amdgcn_exp2f(__builtin_fmaf(s1.y, LOG2E, mo))};
        const u32 p0 = pk2(e0), p1 = pk2(e1);
        *lp<u16>(pw) = (u16)p0; *lp<u16>(pw + 8) = (u16)(p0 >> 16); *lp<u16>(pw + 256) = (u16)p1; *lp<u16>(pw + 264) = (u16)(p1 >> 16);
        const v2f es = e0 + e1;
        *lp<float>(ma + (L_LS - L_M)) = row_sum(es.x + es.y);
        set_flag(F_P + w * 4, 1);
    }
    STAMP(5);
    tstamp[20] = tstamp[24]; tstamp[21] = tstamp[4]; tstamp[22] = tstamp[4]; tstamp[23] = tstamp[4];
    __builtin_amdgcn_s_setprio(3);
    const int aline = WS_A + w * GA + (s * 32 + g * 4 + b) * 128;
    const int mvo = pin((4 * (kg & 1) + (l & 3)) * 16);
    const int mso = WS_ML + g * GML + b * 512 + w * 128;
    const int ovo = pin((n16 & 7) * 64 + kg * 16);
    const int oso = WS_O + g * GO + b * 8192 + w * 2048;
    const int avs = pin((AST16 ? (l & 35) == 0 : l < 32) ? l * 4 : OOB);
    if (w == 0) {
        wait_flag<4, 0>(F_P, 1);
        const v4f m = *lp<v4f>(L_M + (l & 3) * 16), q = *lp<v4f>(L_LS + (l & 3) * 16);
        const v4u gm = {f2u(fmx(fmx(m.x, m.y), fmx(m.z, m.w))), tag, f2u((q.x + q.y) + (q.z + q.w)), tag};
        ST128(gm, r_ws, l < 4 ? l * 512 : OOB, WS_ML + g * GML + b * 128 + s * 16, AUX_SC1);
    }
    v4u gm, av[4], wf[4][4];
    v2f W01, W23;
    auto weights = [&]() {
        const float ms = u2f(gm.x), lv = u2f(gm.z);
        float mm = fmx(ms, dppf<DPP_X1>(ms)); mm = fmx(mm, dppf<DPP_X2>(mm));
        { v2f a = swap16(mm, mm); mm = fmx(a.x, a.y); }
        const float wg = __builtin_amdgcn_exp2f((ms - mm) * LOG2E);
        float L = wg * lv; L += dppf<DPP_X1>(L); L += dppf<DPP_X2>(L);
        { v2f a = swap16(L, L); L = a.x + a.y; }
        const float W = wg * __builtin_amdgcn_rcpf(L);
        W01.x = dppf<0x00>(W); W01.y = dppf<0x55>(W); W23.x = dppf<0xAA>(W); W23.y = dppf<0xFF>(W);
        asm volatile("" : "+v"(W01), "+v"(W23));
    };
    {
        const __amdgpu_buffer_rsrc_t r_wo = mkrsrc(wo, 4u << 20);
        const int wvo = pin((16 * (lr & 3) + 2 * (lr >> 2)) * 1024 + swz);
        u32 so = (g * 4 + b) * (128 * 1024) + (w >> 1) * 65536 + (w & 1) * 8192 + s * 128;
        u32 lw = L_W + w * 4096, ms2 = mso;
        asm volatile("" : "+s"(so), "+s"(lw), "+s"(ms2) : "s"(r_wo));
#define OREG(c) c "{v[128:131]}"(av[0]), c "{v[132:135]}"(av[1]), c "{v[136:139]}"(av[2]), c "{v[140:143]}"(av[3])
#define OIN OREG("")
#define OCHK "v_max3_u32 %[t0], v128, v129, v130\n v_max3_u32 %[t1], v136, v137, v138\n v_max3_u32 %[t0], %[t0], v131, v132\n v_max3_u32 %[t1], %[t1], v139, v140\n" \
             "v_max3_u32 %[t0], %[t0], v133, v134\n v_max3_u32 %[t1], %[t1], v141, v142\n v_max_u32 %[t0], %[t0], v135\n v_max_u32 %[t1], %[t1], v143\n" \
             "v_cmp_eq_u32 vcc, -1, %[t0]\n v_cmp_eq_u32 %[m1], -1, %[t1]\n"
#define OLD  "s_mov_b64 exec, vcc\n" \
             "buffer_load_dwordx4 v[128:131], %[vo], %[rs], %[so] offen sc1\n buffer_load_dwordx4 v[132:135], %[vo], %[rs], %[so] offen offset:512 sc1\n" \
             "s_mov_b64 exec, %[m1]\n" \
             "buffer_load_dwordx4 v[136:139], %[vo], %[rs], %[so] offen offset:1024 sc1\n buffer_load_dwordx4 v[140:143], %[vo], %[rs], %[so] offen offset:1536 sc1\n" \
             "s_mov_b64 exec, -1\n"
        {
            const u32 iv = n16 < 8 ? ~0u : 0u;
#pragma unroll
            for (int k = 0; k < 4; ++k) av[k] = (v4u){iv, iv, iv, iv};
            asm volatile("" : OREG("+") : "s"(oso));
        }
        const u32 t3 = __builtin_amdgcn_readfirstlane((u32)tstart) + T3F;
        wait_flag<2, 2>(F_L, WOP);
#define WOL "buffer_load_dwordx4 %2, %3, %1 offen nt lds\n\t"
#define WON(d) "s_add_u32 m0, m0, 0x400\n\ts_add_i32 %1, %1, " #d "\n\t"
        asm volatile("s_mov_b32 m0, %4\n\t"
                     "buffer_load_dwordx4 %0, %5, %6, %7 offen sc1\n\t"
                     WOL WON(4096) WOL WON(-3072) WOL WON(4096) WOL
                     : "=&v"(gm), "+s"(so) : "v"(wvo), "s"(r_wo), "s"(lw), "v"(mvo), "s"(r_ws), "s"(ms2) : "memory", "scc");
        wait_flag<3, 1>(F_L, 33);
        if (ODELAY) __builtin_amdgcn_s_sleep(ODELAY);
        asm volatile("s_memrealtime s[36:37]\n s_waitcnt lgkmcnt(0)\n s_sub_i32 s36, s36, %[t3]\n s_cmp_lt_i32 s36, 0\n"
                     "s_cselect_b64 exec, 0, %[m]\n"
                     "buffer_load_dwordx4 v[128:131], %[vo], %[rs], %[so] offen sc1\n buffer_load_dwordx4 v[132:135], %[vo], %[rs], %[so] offen offset:512 sc1\n"
                     "buffer_load_dwordx4 v[136:139], %[vo], %[rs], %[so] offen offset:1024 sc1\n buffer_load_dwordx4 v[140:143], %[vo], %[rs], %[so] offen offset:1536 sc1\n"
                     "s_mov_b64 exec, -1\n"
                     "s_waitcnt vmcnt(8)"
                     : OREG("+"), "+v"(gm) : [vo] "v"(ovo), [rs] "s"(r_ws), [so] "s"(oso), [m] "s"(0x00ff00ff00ff00ffull), [t3] "s"(t3) : "memory", "s36", "s37", "scc");
        STAMP(6);
        const bool early = all_lanes((gm.y == tag) & (gm.w == tag));
        weights();
        WAITVM(4);
        set_flag(F_W + w * 4, 1);
        STAMP(25);
        u32 t0, t1;
        unsigned long long m1, m2;
        if (OSTEP2) asm volatile(OCHK OLD : OREG("+"), [t0] "=&v"(t0), [t1] "=&v"(t1), [m1] "=&s"(m1) : [vo] "v"(ovo), [rs] "s"(r_ws), [so] "s"(oso) : "memory", "vcc");
        wait_flag<4, 0>(F_W, 1);
#pragma unroll
        for (int k = 0; k < 4; ++k)
#pragma unroll
            for (int ct = 0; ct < 4; ++ct) wf[k][ct] = trfrag(wa[ct] + k * 4096);
        SB();
        if (!early) {
#pragma unroll 1
            for (int spin = 0; spin < SPIN_MAX; ++spin) {
                asm volatile("buffer_load_dwordx4 %0, %1, %2, %3 offen sc1\n\ts_waitcnt vmcnt(0)" : "=v"(gm) : "v"(mvo), "s"(r_ws), "s"(ms2) : "memory");
                if (all_lanes((gm.y == tag) & (gm.w == tag))) break;
            }
            weights();
        }
        STAMP(11);
#pragma unroll 1
        for (int i = 0; i < 4096; ++i) { if ((int)(now32() - t3) >= 0) break; __builtin_amdgcn_s_sleep(1); }
        u32 n = SPIN_MAX;
        asm volatile("s_waitcnt vmcnt(0)\n1:\n" OCHK
                     "s_or_b64 %[m2], vcc, %[m1]\n"
                     "s_cbranch_scc0 2f\n"
                     OLD
                     "s_waitcnt vmcnt(%[cap])\n"
                     "s_sub_u32 %[n], %[n], 1\n"
                     "s_cbranch_scc0 1b\n"
                     "2:\n"
                     : OREG("+"), [t0] "=&v"(t0), [t1] "=&v"(t1), [m1] "=&s"(m1), [m2] "=&s"(m2), [n] "+s"(n)
                     : [vo] "v"(ovo), [rs] "s"(r_ws), [so] "s"(oso), [cap] "i"(OCAP), [slp] "i"(OSLP) : "memory", "vcc", "scc");
    }
    {
        STAMP(12);
        v4f c[4];
#pragma unroll
        for (int ct = 0; ct < 4; ++ct) c[ct] = (v4f){0.f, 0.f, 0.f, 0.f};
#pragma unroll
        for (int k = 0; k < 4; ++k) {
#pragma unroll
            for (int ct = 0; ct < 4; ++ct) c[ct] = mfma16(av[k], wf[k][ct], c[ct]);
            SB();
        }
        float z[4];
#pragma unroll
        for (int ct = 0; ct < 4; ++ct) {
            const v2f c01 = {c[ct].x, c[ct].y}, c23 = {c[ct].z, c[ct].w};
            const v2f t = __builtin_elementwise_fma(c23, W23, c01 * W01);
            z[ct] = t.x + t.y;
        }
        const v2f r0 = swap16(z[0], z[1]), r1 = swap16(z[2], z[3]);
        const u32 y = umn(pkbf(r0.x + r0.y, r1.x + r1.y), ~1u);
        const v4u ga = {y, dppu<0x55>(y), dppu<0xAA>(y), dppu<0xFF>(y)};
        ST128(ga, r_ws, avs, aline, AUX_SC1);
    }
    asm volatile("" :: OIN);
    set_flag(F_A + w * 4, 3);
    STAMP(13);
    tstamp[14] = tstamp[15] = tstamp[13];
    if (w == 0) { PUTST(0, 7); PUTST(11, 14); if (!fin) PUTST(14, 16); PUTST(18, 20); PUTST(24, 26); } else if (w == 3) PUTST(20, 22); else if (w == 2) PUTST(22, 23); else PUTST(23, 24);
}

extern "C" void kernel_launch(void* const* d_in, const int* in_sizes, int n_in, void* d_out, int out_size, void* d_ws, size_t ws_size, hipStream_t stream) {
    constexpr u32 F = 0x00020000;
    fused_decode_kernel<<<256, 640, 0, stream>>>((const u16*)d_in[0], 4096u, F, (const u16*)d_in[3], 4u << 20, F, (const u16*)d_in[1], 64u << 20, F,
                                                 (const u16*)d_in[2], 64u << 20, F, (unsigned char*)d_ws, WS_END, F, (const u16*)d_in[6], 3584u, F,
                                                 (const u16*)d_in[7], 3584u, F, (const u16*)d_in[8], 3584u, F, (const u16*)d_in[9], 2u, F,
                                                 (const u16*)d_in[4], (u16*)d_out);
}
```

```cpp
#include <hip/hip_runtime.h>
#include <stdint.h>

typedef unsigned u32;
typedef unsigned short u16;
typedef u32 v4u __attribute__((ext_vector_type(4)));
typedef u32 v2u __attribute__((ext_vector_type(2)));
typedef float v4f __attribute__((ext_vector_type(4)));
typedef float v2f __attribute__((ext_vector_type(2)));
typedef __bf16 v8bf __attribute__((ext_vector_type(8)));
typedef __bf16 v2bf __attribute__((ext_vector_type(2)));
typedef short v4s __attribute__((ext_vector_type(4)));
#define LAS __attribute__((address_space(3)))

#define STAMP(i) do { } while (0)
#define PUTST(a, b) do { } while (0)

#define MAPG 0
#define WIN 12
#define QP 10
#define WOP 32
#define XP 0
#define PRE 0
#define TN 16
#define TWAIT 0
#define WOE 4
#define GDELAY 10
#define KFIRST 0
#define OST16 0
#define AST16 1
#define ODELAY 0
#define ADELAY 0
#define ROTA 6
#define ROTB 3
#define SPREAD 1
#define WSOFF 0
#define ILV 0
#define FINM 0
#define FINC 0
#define JUNK 15
#define T1F 480
#define WOST 5
#define TG 2
#define OSLP 0
#define OCAP 0
#define OSTEP2 0
#define ACAP 8
#define ASLP 0
#define T2F 580
#define T3F 720
#define V5FMT 4

constexpr int AUX_SC1 = 16;
constexpr u32 WS_EPOCH = 0;
constexpr u32 GQ = 262144, GO = 262144, GML = 262144, GA = 524288;
constexpr u32 WS_Q = 4096;
constexpr u32 WS_O = 65536;
constexpr u32 WS_ML = 131072;
constexpr u32 WS_A = 163840;
constexpr u32 WS_T = 262144 + 163840;
constexpr u32 WS_END = 2097152;
constexpr u32 L_V = 0, L_K = 0x10000, L_W = 0x20000, L_TN = 0x24000, L_X = 0x25000, L_XN = 0x26000, L_QI = 0x26500, L_P = 0x26A00,
              L_TP = 0x27200, L_JUNK = 0x27600, L_FL = 0x27A00, L_M = 0x27A80, L_LS = 0x27AC0, L_END = 0x27B00;
constexpr u32 F_A = L_FL, F_Q = L_FL + 16, F_M = L_FL + 32, F_P = L_FL + 48, F_W = L_FL + 64, F_L = L_FL + 80, F_ARR = L_FL + 88, F_FB = L_FL + 92, L_ST = L_FL + 96;
constexpr u32 TAG_STEP = 0x01010101u;
constexpr u32 L_TAB = 0xCE00;
constexpr u32 L_OS = L_K, L_FB = L_K + 0x2000, L_FP = L_K + 0x6000;
constexpr u32 T_LO = 114 * 128;
constexpr int SPIN_MAX = 1 << 18;
constexpr u32 CODE_BYTES = 11776;
constexpr int OOB = 0x40000000;
constexpr float LOG2E = 1.4426950408889634f;

__device__ __forceinline__ float u2f(u32 v) { return __builtin_bit_cast(float, v); }
__device__ __forceinline__ u32 f2u(float v) { return __builtin_bit_cast(u32, v); }
__device__ __forceinline__ float bflo(u32 v) { return u2f(v << 16); }
__device__ __forceinline__ float bfhi(u32 v) { return u2f(v & 0xffff0000u); }
__device__ __forceinline__ v2f bf2(u32 v) { v2f r = {bflo(v), bfhi(v)}; return r; }
__device__ __forceinline__ u32 pk2(v2f f) { return __builtin_bit_cast(u32, __builtin_convertvector(f, v2bf)); }
__device__ __forceinline__ u32 pkbf(float a, float b) { v2f f = {a, b}; return pk2(f); }
__device__ __forceinline__ float rbf(float a) { return u2f(pkbf(0.f, a)); }
template <int CTRL> __device__ __forceinline__ float dppf(float v) { return u2f((u32)__builtin_amdgcn_update_dpp(0, (int)f2u(v), CTRL, 0xf, 0xf, true)); }
template <int CTRL> __device__ __forceinline__ u32 dppu(u32 v) { return (u32)__builtin_amdgcn_update_dpp(0, (int)v, CTRL, 0xf, 0xf, true); }
constexpr int DPP_X1 = 0xB1, DPP_X2 = 0x4E, DPP_HMIR = 0x141, DPP_ROR4 = 0x124, DPP_ROR8 = 0x128, DPP_SHL4 = 0x104, DPP_SHL8 = 0x108;
__device__ __forceinline__ float fmx(float a, float b) { return __builtin_fmaxf(a, b); }
__device__ __forceinline__ u32 umn(u32 a, u32 b) { return a < b ? a : b; }
__device__ __forceinline__ u32 umx(u32 a, u32 b) { return a > b ? a : b; }
__device__ __forceinline__ u32 umx4(v4u a) { return umx(umx(a.x, a.y), umx(a.z, a.w)); }
__device__ __forceinline__ v2f swap32(float a, float b) {
    auto r = __builtin_amdgcn_permlane32_swap(f2u(a), f2u(b), false, false);
    v2f o = {u2f(r[0]), u2f(r[1])}; return o;
}
__device__ __forceinline__ v2f swap16(float a, float b) {
    auto r = __builtin_amdgcn_permlane16_swap(f2u(a), f2u(b), false, false);
    v2f o = {u2f(r[0]), u2f(r[1])}; return o;
}
__device__ __forceinline__ float sum_x16_x32(float v) { v2f a = swap16(v, v); v = a.x + a.y; a = swap32(v, v); return a.x + a.y; }
__device__ __forceinline__ float max_x16_x32(float v) { v2f a = swap16(v, v); v = fmx(a.x, a.y); a = swap32(v, v); return fmx(a.x, a.y); }
__device__ __forceinline__ float row_sum(float v) { v += dppf<DPP_X1>(v); v += dppf<DPP_X2>(v); v += dppf<DPP_HMIR>(v); v += dppf<DPP_ROR8>(v); return v; }
__device__ __forceinline__ float wave_sum(float v) { return sum_x16_x32(row_sum(v)); }
__device__ __forceinline__ v4f mfma16(v4u a, v4u b, v4f c) {
    return __builtin_amdgcn_mfma_f32_16x16x32_bf16(__builtin_bit_cast(v8bf, a), __builtin_bit_cast(v8bf, b), c, 0, 0, 0);
}
__device__ __forceinline__ void lds_barrier() { asm volatile("s_waitcnt lgkmcnt(0)\n\ts_barrier" ::: "memory"); }
#define SB() __builtin_amdgcn_sched_barrier(0)
#define WAITVM(n) asm volatile("s_waitcnt vmcnt(%0)" :: "i"(n) : "memory")
__device__ __forceinline__ __amdgpu_buffer_rsrc_t mkrsrc(const void* p, u32 bytes) { return __builtin_amdgcn_make_buffer_rsrc((void*)p, 0, bytes, 0x00020000); }
__device__ __forceinline__ bool all_lanes(bool p) { return __builtin_amdgcn_ballot_w64(!p) == 0ull; }
__device__ __forceinline__ void junk_load(__amdgpu_buffer_rsrc_t rs, int vo, u32 lds) {
    u32 keep;
    asm volatile("s_mov_b32 %0, m0\n\ts_mov_b32 m0, %3\n\ts_nop 0\n\tbuffer_load_ushort %1, %2, 0 offen lds\n\ts_mov_b32 m0, %0" : "=&s"(keep) : "v"(vo), "s"(rs), "s"(lds) : "memory");
}
#define LD128(r, vo, so, aux) __builtin_amdgcn_raw_buffer_load_b128(r, vo, so, aux)
#define ST128(d, r, vo, so, aux) do { v4u d_ = (d); asm volatile("" : "+v"(d_)); __builtin_amdgcn_raw_buffer_store_b128(d_, r, vo, so, aux); asm volatile("s_nop 1" :: "v"(d_)); } while (0)
template <typename T> __device__ __forceinline__ LAS T* lp(u32 a) { return (LAS T*)(size_t)a; }
__device__ __forceinline__ u32 pin(u32 v) { asm volatile("" : "+v"(v)); return v; }
__device__ __forceinline__ v4u trfrag(u32 a) {
    const v2u lo = __builtin_bit_cast(v2u, __builtin_amdgcn_ds_read_tr16_b64_v4i16(lp<v4s>(a)));
    const v2u hi = __builtin_bit_cast(v2u, __builtin_amdgcn_ds_read_tr16_b64_v4i16(lp<v4s>(a + 2048)));
    v4u r = {lo.x, lo.y, hi.x, hi.y}; return r;
}
__device__ __forceinline__ void set_flag(u32 a, u32 v) { asm volatile("" ::: "memory"); *lp<volatile u32>(a) = v; }
__device__ __forceinline__ u32 umn4(v4u a) { return umn(umn(a.x, a.y), umn(a.z, a.w)); }
__device__ __forceinline__ bool uni_ge(u32 x, u32 v) { return (u32)__builtin_amdgcn_readfirstlane(x) >= v; }
template <int N, int SLP> __device__ __forceinline__ void wait_flag(u32 a, u32 v) {
#pragma unroll 1
    for (int i = 0; i < SPIN_MAX; ++i) {
        u32 m;
        if (N == 1) m = *lp<volatile u32>(a);
        else if (N == 2) { const v2u f = *lp<volatile v2u>(a); m = umn(f.x, f.y); }
        else if (N == 3) { const v4u f = *lp<volatile v4u>(a); m = umn(umn(f.x, f.y), f.z); }
        else m = umn4(*lp<volatile v4u>(a));
        if (uni_ge(m, v)) break;
        if (SLP) __builtin_amdgcn_s_sleep(SLP);
    }
    asm volatile("" ::: "memory");
}
__device__ __forceinline__ u32 now32() { unsigned long long t; asm volatile("s_memrealtime %0\n\ts_waitcnt lgkmcnt(0)" : "=s"(t) :: "memory"); return (u32)t; }
struct Coef { v2f w[4], b[4], g1[4], g0[4]; };
__device__ __forceinline__ float mlp_part(const Coef& c, float t) {
    const v2f t2 = {t, t};
    const float K0 = -2.0f * LOG2E * 0.7978845608028654f, K1 = K0 * 0.044715f;
    const v2f k0 = {K0, K0}, k1 = {K1, K1}, one = {1.f, 1.f};
    v2f acc = {0.f, 0.f};
#pragma unroll
    for (int i = 0; i < 4; ++i) {
        const v2f u = __builtin_elementwise_fma(t2, c.w[i], c.b[i]);
        const v2f a = u * __builtin_elementwise_fma(u * u, k1, k0);
        v2f e = {__builtin_amdgcn_exp2f(a.x), __builtin_amdgcn_exp2f(a.y)};
        e += one;
        const v2f rc = {__builtin_amdgcn_rcpf(e.x), __builtin_amdgcn_rcpf(e.y)};
        acc = __builtin_elementwise_fma(__builtin_elementwise_fma(t2, c.g1[i], c.g0[i]), rc, acc);
    }
    return acc.x + acc.y;
}

__device__ __forceinline__ v4u load_part(const u16* p, int part, int l) { return LD128(mkrsrc(p + part * 448, 896), l * 16, 0, 0); }
__device__ __forceinline__ void make_coef(Coef& cf, v4u ta, v4u tb, v4u tc) {
#pragma unroll
    for (int i = 0; i < 4; ++i) { const v2f c = bf2(tc[i]); cf.w[i] = bf2(ta[i]); cf.b[i] = bf2(tb[i]); cf.g1[i] = cf.w[i] * c; cf.g0[i] = cf.b[i] * c; }
}

#define DESC(T, p) T p, u32, u32
__global__ void __launch_bounds__(640)
fused_decode_kernel(DESC(const u16*, x), DESC(const u16*, wq), DESC(const u16*, kc), DESC(const u16*, vc), DESC(unsigned char*, ws),
                    DESC(const u16*, w1), DESC(const u16*, b1), DESC(const u16*, w2), DESC(const u16*, b2), const u16* wo, u16* out) {
    __shared__ __attribute__((aligned(16))) unsigned char smem[L_END];
    STAMP(0);
    u32 tag0, tag, b2r;
    v4u ca, cb, cc;
    int wave2;
    unsigned long long tstart;
    const int pid = blockIdx.x;
    {
        u32 t0, t1, t2, t3;
#define WQL "s_add_u32 m0, m0, 0x400\n s_add_u32 s73, s73, 0x10000\n buffer_load_dwordx4 %[t3], s[40:43], s73 offen nt lds\n"
#define KVL "buffer_load_dwordx4 %[t3], s[44:47], s73 offen nt lds\n s_add_u32 m0, m0, 0x400\n s_add_u32 s73, s73, 0x800\n"
        asm volatile(
            "s_load_dwordx16 s[36:51], %[ka], 0x0\n"
            "s_load_dwordx16 s[52:67], %[ka], 0x40\n"
            "s_memrealtime %[ts]\n"
            "s_getpc_b64 s[76:77]\n"
            "v_readfirstlane_b32 %[wv], %[tid]\n"
            "v_bfe_u32 %[t0], %[tid], 3, 3\n"
            "v_and_b32 %[t1], 6, %[t0]\n"
            "v_xor_b32 %[t1], %[t1], %[tid]\n"
            "v_and_b32 %[t1], 7, %[t1]\n"
            "v_lshlrev_b32 %[t1], 4, %[t1]\n"
            "s_lshr_b32 %[wv], %[wv], 6\n"
            "s_and_b32 s72, %[pid], 7\n"
            "s_cmp_gt_u32 %[wv], 3\n"
            "s_cbranch_scc1 2f\n"
            "s_setprio 2\n"
            "v_lshlrev_b32 %[t2], 4, %[tid]\n"
            "v_lshl_or_b32 %[t3], %[t0], 13, %[t1]\n"
            "s_lshr_b32 s75, %[pid], 6\n"
            "s_lshl_b32 s73, s75, 2\n"
            "s_add_u32 s73, s73, %[wv]\n"
            "s_lshl_b32 s73, s73, 11\n"
            "s_and_b32 s74, %[pid], 63\n"
            "s_or_b32 s73, s73, s74\n"
            "s_lshl_b32 s73, s73, 7\n"
            "s_lshl_b32 s74, %[wv], 10\n"
            "s_add_u32 m0, s74, %[lx]\n"
            "s_lshl_b32 s74, s74, 2\n"
            "s_add_u32 s74, s74, %[lw]\n"
            "v_bfe_u32 %[t0], %[tid], 4, 2\n"
            "v_and_b32 %[t1], 15, %[tid]\n"
            "v_lshlrev_b32 %[t1], 2, %[t1]\n"
            "v_lshl_or_b32 %[t0], %[t0], 9, %[t1]\n"
            "s_and_b32 s78, %[pid], 56\n"
            "s_lshl_b32 s78, s78, 3\n"
            "s_lshl1_add_u32 s78, %[wv], s78\n"
            "s_lshl1_add_u32 s78, s78, s75\n"
            "s_lshl_b32 s78, s78, 5\n"
            "s_or_b32 s78, s78, s72\n"
            "s_lshl_b32 s78, s78, 6\n"
            "s_add_u32 s78, s78, %[wso]\n"
            "v_mov_b32 %[t1], -1\n"
            "s_waitcnt lgkmcnt(0)\n"
            "buffer_load_dwordx4 %[t2], s[36:39], 0 offen lds\n"
            "s_mov_b32 m0, s74\n"
            "s_nop 0\n"
            "buffer_load_dwordx4 %[t3], s[40:43], s73 offen nt lds\n"
            WQL WQL WQL
            "buffer_load_dword %[tag], off, s[52:55], 0\n"
            "buffer_store_dword %[t1], %[t0], s[52:55], s78 offen sc1\n"
            "s_barrier\n"
            "s_branch 9f\n"
            "2:\n"
            "s_cmp_gt_u32 %[wv], 5\n"
            "s_cbranch_scc1 5f\n"
            "s_setprio 3\n"
            "s_sub_u32 s75, %[wv], 4\n"
            "v_lshl_or_b32 %[t3], %[t0], 8, %[t1]\n"
            "s_lshr_b32 s74, %[pid], 3\n"
            "s_mul_i32 s73, s74, %[rotb]\n"
            "s_mul_i32 s78, s72, %[rota]\n"
            "s_add_u32 s73, s73, s78\n"
            "s_lshl_b32 s73, s73, 5\n"
            "s_or_b32 s73, s73, s75\n"
            "s_lshl_b32 s73, s73, 7\n"
            "s_lshl_b32 s74, s74, 5\n"
            "s_or_b32 s74, s74, s72\n"
            "s_or_b32 s74, s74, 0x18\n"
            "s_pack_ll_b32_b16 s73, s73, s74\n"
            "s_lshl_b32 s78, s75, 15\n"
            "s_add_u32 m0, s78, %[lk]\n"
            "s_lshl2_add_u32 s75, s75, %[fl]\n"
            "v_mov_b32 %[t0], s75\n"
            "v_mov_b32 %[t1], 0\n"
            "s_mov_b32 s75, 0\n"
            "s_waitcnt lgkmcnt(0)\n"
            "s_mov_b64 s[70:71], %[ts]\n"
            "s_add_u32 s79, s70, %[t1f]\n"
            "s_barrier\n"
            "3:\n"
            "s_waitcnt vmcnt(%[win])\n"
            KVL KVL
            "s_pack_ll_b32_b16 s73, s73, s74\n"
            "ds_write_b32 %[t0], %[t1]\n"
            "s_add_u32 s75, s75, 1\n"
            "v_add_u32 %[t1], 1, %[t1]\n"
            "s_cmp_lg_u32 s75, 16\n"
            "s_cbranch_scc1 3b\n"
            "s_mov_b64 s[44:45], s[48:49]\n"
            "s_mov_b32 m0, s78\n"
            "4:\n"
            "s_waitcnt vmcnt(%[win])\n"
            "s_memrealtime s[70:71]\n"
            "ds_write_b32 %[t0], %[t1]\n"
            "v_add_u32 %[t1], 1, %[t1]\n"
            "6:\n"
            KVL KVL
            "s_pack_ll_b32_b16 s73, s73, s74\n"
            "s_sub_u32 s75, s75, 1\n"
            "s_cmp_eq_u32 s75, 0\n"
            "s_cbranch_scc1 7f\n"
            "s_waitcnt lgkmcnt(0)\n"
            "s_cmp_gt_u32 s75, %[rem]\n"
            "s_cbranch_scc1 4b\n"
            "s_sub_i32 s72, s70, s79\n"
            "s_cmp_lt_i32 s72, 0\n"
            "s_cbranch_scc1 4b\n"
            "s_branch 6b\n"
            "7:\n"
            "s_waitcnt lgkmcnt(0)\n"
            "s_memrealtime s[70:71]\n"
            "s_waitcnt vmcnt(32) lgkmcnt(0)\n"
            "s_sub_i32 s72, s70, s79\n"
            "s_cmp_lt_i32 s72, %[t2f]\n"
            "s_cselect_b32 s72, 31, 32\n"
            "v_mov_b32 %[t1], s72\n"
            "ds_write_b32 %[t0], %[t1]\n"
            "s_branch 9f\n"
            "5:\n"
            "s_load_dwordx4 s[68:71], %[ka], 0x80\n"
            "s_sub_u32 s75, %[wv], 6\n"
            "s_and_b32 s74, %[pid], 56\n"
            "s_lshl1_add_u32 s74, s75, s74\n"
            "s_lshl_b32 s74, s74, 8\n"
            "s_add_u32 s78, s74, 512\n"
            "s_min_u32 s78, s78, %[code]\n"
            "s_mov_b32 s79, 0x20000\n"
            "s_and_b32 s76, s76, 0xffffff00\n"
            "v_and_b32 %[t2], 63, %[tid]\n"
            "v_cmp_gt_u32 vcc, 56, %[t2]\n"
            "v_lshl_add_u32 %[t3], %[t2], 7, s74\n"
            "buffer_load_dword %[b2r], %[t3], s[76:79], 0 offen\n"
            "v_and_b32 %[t0], 31, %[tid]\n"
            "v_lshlrev_b32 %[t0], 2, %[t0]\n"
            "v_add_u32 %[t0], %[lfl], %[t0]\n"
            "v_mov_b32 %[t1], 0\n"
            "ds_write_b32 %[t0], %[t1]\n"
            "s_mulk_i32 s75, 896\n"
            "v_lshl_add_u32 %[t3], %[t2], 4, s75\n"
            "v_cndmask_b32 %[t3], 2.0, %[t3], vcc\n"
            "s_waitcnt lgkmcnt(0)\n"
            "s_barrier\n"
            "buffer_load_dwordx4 %[ca], %[t3], s[56:59], 0 offen\n"
            "buffer_load_dwordx4 %[cb], %[t3], s[60:63], 0 offen\n"
            "buffer_load_dwordx4 %[cc], %[t3], s[64:67], 0 offen\n"
            "buffer_load_dword %[tag], off, s[52:55], 0\n"
            "buffer_load_ushort %[b2r], off, s[68:71], 0\n"
            "9:\n"
            : [wv] "=&s"(wave2), [ts] "=&s"(tstart), [tag] "=&v"(tag0), [b2r] "=&v"(b2r), [ca] "=&v"(ca), [cb] "=&v"(cb), [cc] "=&v"(cc),
              [t0] "=&v"(t0), [t1] "=&v"(t1), [t2] "=&v"(t2), [t3] "=&v"(t3)
            : [ka] "s"(__builtin_amdgcn_kernarg_segment_ptr()), [pid] "s"(pid), [tid] "v"(threadIdx.x), "s"((u32)(size_t)(LAS void*)smem),
              [lx] "i"(L_X), [lw] "i"(L_W), [lk] "i"(L_K), [fl] "i"(F_L), [lfl] "i"(L_FL), [wso] "i"(WS_O), [code] "i"(CODE_BYTES), [win] "i"(WIN - 2), [rota] "i"(ROTA), [rotb] "i"(ROTB), [t1f] "i"(T1F), [t2f] "i"(T2F - T1F), [rem] "i"(15 - (WIN - 2) / 2)
            : "memory", "scc", "vcc", "s36", "s37", "s38", "s39", "s40", "s41", "s42", "s43", "s44", "s45", "s46", "s47", "s48", "s49", "s50", "s51", "s52", "s53", "s54", "s55", "s56", "s57", "s58", "s59", "s60", "s61", "s62", "s63", "s64", "s65", "s66", "s67", "s68", "s69", "s70", "s71", "s72", "s73", "s74", "s75", "s76", "s77", "s78", "s79");
        static_assert(L_V == 0 && OOB == 0x40000000 && F_ARR == F_L + 8, "");
    }
    SB();
    const int l = threadIdx.x & 63;
    const int s = pid & 7, g = (pid >> 3) & 7, b = pid >> 6;
    const int n16 = l & 15, kg = l >> 4, lr = l >> 3;
    const int swz = ((l & 7) ^ (lr & 6)) * 16;
    const __amdgpu_buffer_rsrc_t r_ws = mkrsrc(ws, WS_END);
    const u32 fra = (kg * 4 + (n16 >> 2)) * 128 + (((kg & 1) * 2 + (n16 >> 3)) * 32) + (n16 & 3) * 8;
    const bool fin = (pid & 63) == FINM * b + FINC;

    if (wave2 >= 4 && wave2 < 6) {
        STAMP(7);
        const int dh = wave2 - 4;
        const u32 lv = L_V + dh * 32768, fl = F_L + dh * 4;
        const u32 pa = pin(L_P + kg * 64 + (n16 >> 2) * 16);
        u32 va[4];
#pragma unroll
        for (int t = 0; t < 4; ++t) va[t] = pin((lv + fra) ^ (t * 32));
        const int ovo = pin(kg * 8192 + (n16 >> 3) * 512 + (n16 & 7) * 8);
        const int oso = WS_O + g * GO + b * 2048 + dh * 1024 + s * 64;
        wait_flag<4, 1>(F_P, 1);
        STAMP(16);
        v4u pf[8];
#pragma unroll
        for (int st = 0; st < 8; ++st) pf[st] = *lp<v4u>(pa + st * 256);
        v4f acc[4];
#pragma unroll
        for (int t = 0; t < 4; ++t) acc[t] = (v4f){0.f, 0.f, 0.f, 0.f};
        static_assert(WIN <= 16 && WIN % 4 == 0, "");
        constexpr int ST0 = 5;
#pragma unroll
        for (int st = 0; st < ST0; ++st) {
            WAITVM(28 - 4 * st);
            if (st == WOST) set_flag(fl, 32);
#pragma unroll
            for (int t = 0; t < 4; ++t) acc[t] = mfma16(pf[st], trfrag(va[t] + st * 4096), acc[t]);
        }
        SB();
        STAMP(17);
#pragma unroll
        for (int st = ST0; st < 8; ++st) {
            v2u lo[4], hi[4];
            WAITVM(30 - 4 * st);
#pragma unroll
            for (int t = 0; t < 4; ++t) lo[t] = __builtin_bit_cast(v2u, __builtin_amdgcn_ds_read_tr16_b64_v4i16(lp<v4s>(va[t] + st * 4096)));
            WAITVM(28 - 4 * st);
            if (st == WOST) set_flag(fl, 32);
            if (st == 7) STAMP(8);
#pragma unroll
            for (int t = 0; t < 4; ++t) hi[t] = __builtin_bit_cast(v2u, __builtin_amdgcn_ds_read_tr16_b64_v4i16(lp<v4s>(va[t] + st * 4096 + 2048)));
#pragma unroll
            for (int t = 0; t < 4; ++t) { const v4u bf = {lo[t].x, lo[t].y, hi[t].x, hi[t].y}; acc[t] = mfma16(pf[st], bf, acc[t]); }
            SB();
        }
        const v2u go = {umn(pkbf(acc[0].x, acc[1].x), ~1u), umn(pkbf(acc[2].x, acc[3].x), ~1u)};
        __builtin_amdgcn_raw_buffer_store_b64(go, r_ws, ovo, oso, AUX_SC1);
        set_flag(fl, 33);
        STAMP(9);
        if (dh == 0) { PUTST(7, 10); PUTST(16, 18); }
        __builtin_amdgcn_endpgm();
    }

    if (wave2 >= 6) {
        const int tw = wave2 - 6;
        const int tj = s * 32 + g * 4 + b;
        const v4u ones = {~0u, ~0u, ~0u, ~0u};
        ST128(ones, r_ws, l < 8 ? l * 16 : OOB, WS_A + tw * GA + (s * 32 + g * 4 + b) * 128, AUX_SC1);
        {
            const u16* p = tw == 0 ? vc + ((size_t)(b * 8 + g) * 8192 + 6144 + s * 256) * 128 : tw == 1 ? wo + (g * 4 + b) * 65536 : out;
            junk_load(mkrsrc(p, ((JUNK >> tw) & 1) && (tw != 2 || fin || JUNK >= 8) ? 2 : 0), l * 4, L_JUNK);
        }
        if (TWAIT) wait_flag<4, 4>(F_A, TWAIT);
        asm volatile("s_waitcnt vmcnt(2)" : "+v"(ca), "+v"(cb), "+v"(cc), "+v"(tag0), "+v"(b2r) :: "memory");
        SB();
        Coef cf;
        make_coef(cf, ca, cb, cc);
        tag = tag0 + TAG_STEP;
        {
            float t = u2f((u32)(((tj & 128) << 8) + T_LO + (tj & 127)) << 16);
            u32 ta = n16 == 0 ? L_TP + tw * 16 + kg * 4 : L_JUNK;
#pragma unroll 1
            for (int e = 0; e < TN; ++e) {
                *lp<float>(ta) = row_sum(mlp_part(cf, t));
                ta += n16 == 0 ? 64 : 0;
                t += t;
            }
        }
        WAITVM(0);
        {
            u32 arr = 0;
            if (l == 0) arr = __hip_atomic_fetch_add(lp<u32>(F_ARR), 9u, __ATOMIC_RELAXED, __HIP_MEMORY_SCOPE_WORKGROUP);
            asm volatile("" ::: "memory");
            if (__builtin_amdgcn_readfirstlane(arr) == 27) {
                const v4f p = *lp<v4f>(L_TP + l * 16);
                float v = (p.x + p.y) + (p.z + p.w);
                v += dppf<DPP_X1>(v); v += dppf<DPP_X2>(v);
                v = rbf(v) + bflo(b2r);
                const u32 p0 = pkbf(v, dppf<DPP_SHL4>(v));
                const v4u gt = {p0, tag, dppu<DPP_SHL8>(p0), tag};
                ST128(gt, r_ws, n16 == 0 ? kg * 16 : OOB, WS_T + tj * 64, AUX_SC1);
            }
        }
        STAMP(10);
        if (tw == 0) PUTST(10, 11);
        if (!fin) __builtin_amdgcn_endpgm();
        __builtin_amdgcn_s_setprio(1);
        if (TG == 0) __builtin_amdgcn_s_sleep(GDELAY);
        if (TG == 1) wait_flag<2, 4>(F_L, 32);
        if (TG == 2) wait_flag<2, 4>(F_L, 33);
        if (TG == 3) wait_flag<4, 4>(F_W, 1);
        {
            v4u t[4];
            bool bad = true;
#pragma unroll 1
            for (int spin = 0; spin < SPIN_MAX; ++spin) {
                asm volatile("" ::: "memory");
                if (bad) {
#pragma unroll
                    for (int q = 0; q < 4; ++q) t[q] = LD128(r_ws, l * 16 + q * 1024, WS_T + tw * 4096, AUX_SC1);
                }
                u32 e = 0;
#pragma unroll
                for (int q = 0; q < 4; ++q) e |= (t[q].y ^ tag) | (t[q].w ^ tag);
                bad = e != 0;
                if (all_lanes(!bad)) break;
            }
            if (TG == 0) wait_flag<4, 2>(F_P, 1);
            const u32 a = pin(L_TAB + 2 * T_LO + (tw >> 1) * 65536 + ((tw & 1) * 64 + (l >> 2)) * 2 + (l & 3) * 1024);
#pragma unroll
            for (int q = 0; q < 4; ++q) {
                *lp<u16>(a + q * 32) = (u16)t[q].x; *lp<u16>(a + q * 32 + 256) = (u16)(t[q].x >> 16);
                *lp<u16>(a + q * 32 + 512) = (u16)t[q].z; *lp<u16>(a + q * 32 + 768) = (u16)(t[q].z >> 16);
            }
        }
        const int D = tw * 64 + (n16 >> 3) * 32 + (kg >> 1) * 16 + (l & 7) * 2 + (kg & 1);
        v2f xr = bf2(*lp<u32>(L_X + b * 1024 + D * 4));
        v4u sel, a[8];
#pragma unroll
        for (int q = 0; q < 4; ++q) sel[q] = ((n16 & 2) == 0 && ((n16 >> 1) & 2) + (n16 & 1) == q) ? (n16 >= 8 ? 0x3F800000u : 0x3F80u) : 0u;
#pragma unroll
        for (int i = 0; i < 8; ++i) a[i] = ones;
        const int avo = pin((n16 >> 3) * 4096 + kg * 128 + (l & 7) * 16);
        const int aso = WS_A + b * GA + tw * 8192;
        const u32 sta = pin(l == 0 ? L_ST + tw * 8 : L_JUNK);
        const int ooff = pin(b * 1024 + D * 4);
        const __amdgpu_buffer_rsrc_t r_out = mkrsrc(out, 4096);
#define AREG(c) c "{v[128:131]}"(a[0]), c "{v[132:135]}"(a[1]), c "{v[136:139]}"(a[2]), c "{v[140:143]}"(a[3]), c "{v[144:147]}"(a[4]), c "{v[148:151]}"(a[5]), c "{v[152:155]}"(a[6]), c "{v[156:159]}"(a[7])
        asm volatile("" : "+v"(xr), "+v"(sel), AREG("+") : "s"(aso), "s"(r_out));
        wait_flag<4, 2>(F_A, 3);
        __builtin_amdgcn_s_setprio(3);
        if (ADELAY) __builtin_amdgcn_s_sleep(ADELAY);
        {
            u32 t0, t1, n = SPIN_MAX;
#define ACHK(r0, r1, r2, r3, r4, r5, r6, r7, m0, m1) \
            "v_max3_u32 %[t0], v" #r0 ", v" #r1 ", v" #r2 "\n v_max3_u32 %[t1], v" #r4 ", v" #r5 ", v" #r6 "\n" \
            "v_max_u32 %[t0], %[t0], v" #r3 "\n v_max_u32 %[t1], %[t1], v" #r7 "\n" \
            "v_cmp_eq_u32 s[" m0 "], -1, %[t0]\n v_cmp_eq_u32 s[" m1 "], -1, %[t1]\n"
#define ALD(r, m, o) "s_mov_b64 exec, s[" m "]\n buffer_load_dwordx4 v[" r "], %[vo], %[rs], %[so] offen offset:" #o " sc1\n"
            asm volatile("1:\n"
                ACHK(128, 129, 130, 131, 132, 133, 134, 135, "36:37", "38:39") ACHK(136, 137, 138, 139, 140, 141, 142, 143, "40:41", "42:43")
                ACHK(144, 145, 146, 147, 148, 149, 150, 151, "44:45", "46:47") ACHK(152, 153, 154, 155, 156, 157, 158, 159, "48:49", "50:51")
                "s_or_b64 s[52:53], s[36:37], s[38:39]\n s_or_b64 s[54:55], s[40:41], s[42:43]\n s_or_b64 s[56:57], s[44:45], s[46:47]\n"
                "s_or_b64 s[52:53], s[52:53], s[54:55]\n s_or_b64 s[54:55], s[48:49], s[50:51]\n s_or_b64 s[52:53], s[52:53], s[56:57]\n"
                "s_or_b64 s[52:53], s[52:53], s[54:55]\n"
                "s_cbranch_scc0 2f\n"
                ALD("128:131", "36:37", 0) ALD("132:135", "38:39", 512) ALD("136:139", "40:41", 1024) ALD("140:143", "42:43", 1536)
                ALD("144:147", "44:45", 2048) ALD("148:151", "46:47", 2560) ALD("152:155", "48:49", 3072) ALD("156:159", "50:51", 3584)
                "s_mov_b64 exec, -1\n"
                "s_waitcnt vmcnt(%[cap])\n"
                "s_sub_u32 %[n], %[n], 1\n"
                "s_cbranch_scc0 1b\n"
                "2:\n"
                : AREG("+"), [t0] "=&v"(t0), [t1] "=&v"(t1), [n] "+s"(n)
                : [vo] "v"(avo), [rs] "s"(r_ws), [so] "s"(aso), [cap] "i"(ACAP), [slp] "i"(ASLP)
                : "memory", "scc", "s36", "s37", "s38", "s39", "s40", "s41", "s42", "s43", "s44", "s45", "s46", "s47", "s48", "s49", "s50", "s51", "s52", "s53", "s54", "s55", "s56", "s57");
        }
        STAMP(14);
        v4f c0 = {0.f, 0.f, 0.f, 0.f}, c1 = c0;
#pragma unroll
        for (int i = 0; i < 8; i += 2) { c0 = mfma16(sel, a[i], c0); c1 = mfma16(sel, a[i + 1], c1); }
        const v2f h = bf2(pk2(xr + bf2(pkbf(c0.x + c1.x, c0.y + c1.y))));
        const float h0 = u2f(__builtin_amdgcn_readfirstlane(f2u(h.x)));
        const float d0 = h.x - h0, d1 = h.y - h0;
        const float sd = wave_sum(d0 + d1), sdd = wave_sum(__builtin_fmaf(d0, d0, d1 * d1));
        { const v2u t = {f2u(128.f * h0 + sd), f2u(sdd - sd * sd * (1.f / 128.f)) | 1u}; *lp<v2u>(sta) = t; }
        v4f s0, s1;
#pragma unroll 1
        for (int spin = 0; spin < SPIN_MAX; ++spin) {
            s0 = *lp<volatile v4f>(L_ST); s1 = *lp<volatile v4f>(L_ST + 16);
            if (uni_ge(umn(umn(f2u(s0.y), f2u(s0.w)), umn(f2u(s1.y), f2u(s1.w))), 1)) break;
        }
        asm volatile("" ::: "memory");
        const float mean = ((s0.x + s0.z) + (s1.x + s1.z)) * (1.f / 512.f);
        const float e0 = s0.x * (1.f / 128.f) - mean, e1 = s0.z * (1.f / 128.f) - mean, e2 = s1.x * (1.f / 128.f) - mean, e3 = s1.z * (1.f / 128.f) - mean;
        const float var = fmx(__builtin_fmaf(128.f, (e0 * e0 + e1 * e1) + (e2 * e2 + e3 * e3), (s0.y + s0.w) + (s1.y + s1.w)) * (1.f / 512.f), 0.f);
        const float mb = rbf(mean);
        const float rden = __builtin_amdgcn_rcpf(rbf(__builtin_amdgcn_sqrtf(rbf(rbf(var) + 1e-5f))));
        const float mean2 = rbf((mean - mb) * rden), var2 = var * rden * rden;
        const float rden2 = __builtin_amdgcn_rcpf(rbf(__builtin_amdgcn_sqrtf(rbf(rbf(var2) + 1e-5f))));
        const v2f mb2 = {mb, mb}, rd2 = {rden, rden}, mc2 = {mean2, mean2}, re2 = {rden2, rden2};
        const v2f n1 = bf2(pk2(bf2(pk2(h - mb2)) * rd2));
        const u32 tt = pk2(bf2(pk2(n1 - mc2)) * re2);
        const u32 ta = tt & 0xffffu, tb = tt >> 16;
        const v2f y = {bflo(*lp<u16>(L_TAB + 2 * ta)), bflo(*lp<u16>(L_TAB + 2 * tb))};
        __builtin_amdgcn_raw_buffer_store_b32(pk2(h + y), r_out, ooff, 0, 0);
        if (b == 0 && tw == 0 && l == 0) __builtin_amdgcn_raw_buffer_store_b32(tag, r_ws, 0, WS_EPOCH, AUX_SC1);
        STAMP(15);
        if (tw == 0) PUTST(14, 16);
#pragma unroll
        for (int q = 0; q < 2; ++q) {
            const u32 t = q ? tb : ta;
            if ((t & 0x7fffu) - T_LO >= 2048u) {
                const u32 i = __hip_atomic_fetch_add(lp<u32>(F_FB), 1u, __ATOMIC_RELAXED, __HIP_MEMORY_SCOPE_WORKGROUP);
                const v2u it = {(t << 16) | (u32)(2 * D + q), f2u(q ? h.y : h.x)};
                *lp<v2u>(L_FB + i * 8) = it;
            }
        }
        lds_barrier();
        const int n = __builtin_amdgcn_readfirstlane(*lp<u32>(F_FB));
        if (n > 0) {
#pragma unroll 1
            for (int i = 0; i < n; ++i) {
                const float p = wave_sum(mlp_part(cf, bfhi(*lp<u32>(L_FB + i * 8))));
                *lp<float>(l == 0 ? L_FP + i * 16 + tw * 4 : L_JUNK) = p;
            }
            __builtin_amdgcn_s_waitcnt(0x0F70);
            lds_barrier();
#pragma unroll 1
            for (int i = tw * 64 + l; i < n; i += 256) {
                const v2u it = *lp<v2u>(L_FB + i * 8);
                const v4f p = *lp<v4f>(L_FP + i * 16);
                const float yv = rbf(rbf((p.x + p.y) + (p.z + p.w)) + bflo(b2r));
                out[b * 512 + (it.x & 0xffffu)] = (u16)pkbf(u2f(it.y) + yv, 0.f);
            }
        }
        asm volatile("" :: AREG(""));
        __builtin_amdgcn_endpgm();
    }

    const int w = wave2;
    v4u tb;
    u32 wa[4];
#pragma unroll
    for (int ct = 0; ct < 4; ++ct) wa[ct] = pin((L_W + fra) ^ (ct * 32));
    {
        const u32 xb = pin(L_X + w * 1024 + l * 16);
        const u32 xa = pin(kg == b ? L_XN + w * 320 + (n16 >> 2) * 64 + (n16 & 1) * 32 + ((n16 >> 1) & 1) * 8 : L_JUNK);
        const u32 xf = pin(L_XN + (n16 >> 2) * 320 + kg * 16);
        const u32 wq0 = pin((L_W + fra) ^ (w * 32));
        const int qvo = pin((n16 & 3) == 0 ? (n16 >> 2) * 8192 + kg * 16 : OOB);
        WAITVM(6);
        STAMP(1);
        const v4u xr = *lp<v4u>(xb);
        v2f xv[4];
#pragma unroll
        for (int q = 0; q < 4; ++q) xv[q] = bf2(xr[q]);
        const float c = u2f(__builtin_amdgcn_readfirstlane(f2u(xv[0].x)));
        const v2f c2 = {c, c};
        v2f d[4];
#pragma unroll
        for (int q = 0; q < 4; ++q) d[q] = xv[q] - c2;
        const v2f s2 = (d[0] + d[1]) + (d[2] + d[3]);
        const v2f q2 = __builtin_elementwise_fma(d[0], d[0], d[1] * d[1]) + __builtin_elementwise_fma(d[2], d[2], d[3] * d[3]);
        const float sd = wave_sum(s2.x + s2.y) * (1.f / 512.f), sq = wave_sum(q2.x + q2.y) * (1.f / 512.f);
        const float var = fmx(__builtin_fmaf(-sd, sd, sq), 0.f);
        const float mb = rbf(c + sd);
        const float rden = __builtin_amdgcn_rcpf(rbf(__builtin_amdgcn_sqrtf(rbf(rbf(var) + 1e-5f))));
        const v2f mb2 = {mb, mb}, rd2 = {rden, rden};
        v4u o;
#pragma unroll
        for (int q = 0; q < 4; ++q) o[q] = pk2(bf2(pk2(xv[q] - mb2)) * rd2);
        const v2u o0 = {o.x, o.y}, o1 = {o.z, o.w};
        *lp<v2u>(xa) = o0; *lp<v2u>(xa + 16) = o1;
        asm volatile("s_waitcnt vmcnt(0)\n\tv_add_u32 %0, %2, %1" : "=v"(tag) : "v"(tag0), "i"(TAG_STEP) : "memory");
        set_flag(F_A + w * 4, 1);
#pragma unroll
        for (int i = 0; i < 4; ++i) tb[i] = (tag >> (8 * i)) & 255u;
        v4u af[4], bf[4];
        if (PRE) wait_flag<4, PRE>(F_A, 1);
#pragma unroll 1
        for (int spin = 0; spin < SPIN_MAX; ++spin) {
            const v4u f = *lp<volatile v4u>(F_A);
            asm volatile("" ::: "memory");
#pragma unroll
            for (int t = 0; t < 4; ++t) { af[t] = *lp<v4u>(xf + t * 64); bf[t] = trfrag(wq0 + t * 4096); }
            SB();
            if (uni_ge(umn4(f), 1)) break;
        }
        v4f acc = {0.f, 0.f, 0.f, 0.f};
#pragma unroll
        for (int t = 0; t < 4; ++t) acc = mfma16(bf[t], af[t], acc);
        v4u gq;
#pragma unroll
        for (int i = 0; i < 4; ++i) gq[i] = (f2u(acc[i]) & ~255u) | tb[i];
        ST128(gq, r_ws, qvo, WS_Q + g * GQ + (s >> 1) * 2048 + b * 512 + (s & 1) * 256 + w * 64, AUX_SC1);
        set_flag(F_A + w * 4, 2);
    }
    STAMP(2);
    {
        const int qso = WS_Q + g * GQ + b * 8192 + w * 2048;
        const u32 qw = pin(L_QI + w * 320 + (l & 31) * 8 + (l >> 5) * 4);
        wait_flag<1, 2>(F_L, QP);
        STAMP(3);
        v4u p[2], c[2];
        bool bad = true;
#pragma unroll 1
        for (int spin = 0; spin < SPIN_MAX; ++spin) {
            asm volatile("" ::: "memory");
            if (bad) {
#pragma unroll
                for (int k = 0; k < 2; ++k) p[k] = LD128(r_ws, l * 16 + k * 1024, qso, AUX_SC1);
            }
            c[0] = p[0] ^ tb; c[1] = p[1] ^ tb;
            const v4u o = c[0] | c[1];
            bad = (((o.x | o.y) | (o.z | o.w)) & 255u) != 0;
            if (all_lanes(!bad)) break;
        }
        const v4f qs = __builtin_bit_cast(v4f, c[0]) + __builtin_bit_cast(v4f, c[1]);
        const v2f q0 = swap32(qs.x, qs.z), q1 = swap32(qs.y, qs.w);
        *lp<u32>(qw) = pkbf(q0.x + q0.y, q1.x + q1.y);
        set_flag(F_Q + w * 4, 1);
    }
    STAMP(4);
    {
        const u32 qa = pin(L_QI + (n16 >> 2) * 320 + kg * 16);
        const u32 ma = pin(n16 == 0 ? L_M + kg * 16 + w * 4 : L_JUNK), mr = pin(L_M + kg * 16);
        const u32 pw = pin(L_P + w * 512 + (n16 >> 2) * 64 + kg * 16 + (n16 & 3) * 2);
        const u32 k0 = pin(L_K + w * 8192 + n16 * 128 + ((kg ^ (n16 & 6)) * 16)), k1 = pin(k0 ^ 64);
        wait_flag<2, 1>(F_L, 4 * w + 4 + (WIN - 2) / 2);
        STAMP(24);
        v4u kf[4][4];
#pragma unroll
        for (int t = 0; t < 4; ++t)
#pragma unroll
            for (int u = 0; u < 4; ++u) kf[t][u] = *lp<v4u>(((t & 1) ? k1 : k0) + (t >> 1) * 32768 + u * 2048);
        SB();
        v4u qf[4];
        if (PRE) wait_flag<4, PRE>(F_Q, 1);
#pragma unroll 1
        for (int spin = 0; spin < SPIN_MAX; ++spin) {
            const v4u f = *lp<volatile v4u>(F_Q);
            asm volatile("" ::: "memory");
#pragma unroll
            for (int t = 0; t < 4; ++t) qf[t] = *lp<v4u>(qa + t * 64);
            SB();
            if (uni_ge(umn4(f), 1)) break;
        }
        STAMP(18);
        v4f acc[4];
#pragma unroll
        for (int u = 0; u < 4; ++u) acc[u] = (v4f){0.f, 0.f, 0.f, 0.f};
#pragma unroll
        for (int t = 0; t < 4; ++t)
#pragma unroll
            for (int u = 0; u < 4; ++u) acc[u] = mfma16(qf[t], kf[t][u], acc[u]);
        const v2f scale = {0.08838834764831845f, 0.08838834764831845f};
        const v2f s0 = bf2(pk2(bf2(pkbf(acc[0].x, acc[1].x)) * scale)), s1 = bf2(pk2(bf2(pkbf(acc[2].x, acc[3].x)) * scale));
        float m = fmx(fmx(s0.x, s0.y), fmx(s1.x, s1.y));
        m = fmx(m, dppf<DPP_X1>(m)); m = fmx(m, dppf<DPP_X2>(m)); m = fmx(m, dppf<DPP_HMIR>(m)); m = fmx(m, dppf<DPP_ROR8>(m));
        *lp<float>(ma) = m;
        set_flag(F_M + w * 4, 1);
        v4f mv;
        if (PRE) wait_flag<4, PRE>(F_M, 1);
#pragma unroll 1
        for (int spin = 0; spin < SPIN_MAX; ++spin) {
            const v4u f = *lp<volatile v4u>(F_M);
            asm volatile("" ::: "memory");
            mv = *lp<v4f>(mr);
            SB();
            if (uni_ge(umn4(f), 1)) break;
        }
        STAMP(19);
        const float mo = -fmx(fmx(mv.x, mv.y), fmx(mv.z, mv.w)) * LOG2E;
        const v2f e0 = {__builtin_amdgcn_exp2f(__builtin_fmaf(s0.x, LOG2E, mo)), __builtin_amdgcn_exp2f(__builtin_fmaf(s0.y, LOG2E, mo))};
        const v2f e1 = {__builtin_amdgcn_exp2f(__builtin_fmaf(s1.x, LOG2E, mo)), __builtin_amdgcn_exp2f(__builtin_fmaf(s1.y, LOG2E, mo))};
        const u32 p0 = pk2(e0), p1 = pk2(e1);
        *lp<u16>(pw) = (u16)p0; *lp<u16>(pw + 8) = (u16)(p0 >> 16); *lp<u16>(pw + 256) = (u16)p1; *lp<u16>(pw + 264) = (u16)(p1 >> 16);
        const v2f es = e0 + e1;
        *lp<float>(ma + (L_LS - L_M)) = row_sum(es.x + es.y);
        set_flag(F_P + w * 4, 1);
    }
    STAMP(5);
    __builtin_amdgcn_s_setprio(3);
    const int aline = WS_A + w * GA + (s * 32 + g * 4 + b) * 128;
    const int mvo = pin((4 * (kg & 1) + (l & 3)) * 16);
    const int mso = WS_ML + g * GML + b * 512 + w * 128;
    const int ovo = pin((n16 & 7) * 64 + kg * 16);
    const int oso = WS_O + g * GO + b * 8192 + w * 2048;
    const int avs = pin((AST16 ? (l & 35) == 0 : l < 32) ? l * 4 : OOB);
    if (w == 0) {
        wait_flag<4, 0>(F_P, 1);
        const v4f m = *lp<v4f>(L_M + (l & 3) * 16), q = *lp<v4f>(L_LS + (l & 3) * 16);
        const v4u gm = {f2u(fmx(fmx(m.x, m.y), fmx(m.z, m.w))), tag, f2u((q.x + q.y) + (q.z + q.w)), tag};
        ST128(gm, r_ws, l < 4 ? l * 512 : OOB, WS_ML + g * GML + b * 128 + s * 16, AUX_SC1);
    }
    v4u gm, av[4], wf[4][4];
    v2f W01, W23;
    auto weights = [&]() {
        const float ms = u2f(gm.x), lv = u2f(gm.z);
        float mm = fmx(ms, dppf<DPP_X1>(ms)); mm = fmx(mm, dppf<DPP_X2>(mm));
        { v2f a = swap16(mm, mm); mm = fmx(a.x, a.y); }
        const float wg = __builtin_amdgcn_exp2f((ms - mm) * LOG2E);
        float L = wg * lv; L += dppf<DPP_X1>(L); L += dppf<DPP_X2>(L);
        { v2f a = swap16(L, L); L = a.x + a.y; }
        const float W = wg * __builtin_amdgcn_rcpf(L);
        W01.x = dppf<0x00>(W); W01.y = dppf<0x55>(W); W23.x = dppf<0xAA>(W); W23.y = dppf<0xFF>(W);
        asm volatile("" : "+v"(W01), "+v"(W23));
    };
    {
        const __amdgpu_buffer_rsrc_t r_wo = mkrsrc(wo, 4u << 20);
        const int wvo = pin((16 * (lr & 3) + 2 * (lr >> 2)) * 1024 + swz);
        u32 so = (g * 4 + b) * (128 * 1024) + (w >> 1) * 65536 + (w & 1) * 8192 + s * 128;
        u32 lw = L_W + w * 4096, ms2 = mso;
        asm volatile("" : "+s"(so), "+s"(lw), "+s"(ms2) : "s"(r_wo));
#define OREG(c) c "{v[128:131]}"(av[0]), c "{v[132:135]}"(av[1]), c "{v[136:139]}"(av[2]), c "{v[140:143]}"(av[3])
#define OIN OREG("")
#define OCHK "v_max3_u32 %[t0], v128, v129, v130\n v_max3_u32 %[t1], v136, v137, v138\n v_max3_u32 %[t0], %[t0], v131, v132\n v_max3_u32 %[t1], %[t1], v139, v140\n" \
             "v_max3_u32 %[t0], %[t0], v133, v134\n v_max3_u32 %[t1], %[t1], v141, v142\n v_max_u32 %[t0], %[t0], v135\n v_max_u32 %[t1], %[t1], v143\n" \
             "v_cmp_eq_u32 vcc, -1, %[t0]\n v_cmp_eq_u32 %[m1], -1, %[t1]\n"
#define OLD  "s_mov_b64 exec, vcc\n" \
             "buffer_load_dwordx4 v[128:131], %[vo], %[rs], %[so] offen sc1\n buffer_load_dwordx4 v[132:135], %[vo], %[rs], %[so] offen offset:512 sc1\n" \
             "s_mov_b64 exec, %[m1]\n" \
             "buffer_load_dwordx4 v[136:139], %[vo], %[rs], %[so] offen offset:1024 sc1\n buffer_load_dwordx4 v[140:143], %[vo], %[rs], %[so] offen offset:1536 sc1\n" \
             "s_mov_b64 exec, -1\n"
        {
            const u32 iv = n16 < 8 ? ~0u : 0u;
#pragma unroll
            for (int k = 0; k < 4; ++k) av[k] = (v4u){iv, iv, iv, iv};
            asm volatile("" : OREG("+") : "s"(oso));
        }
        const u32 t3 = __builtin_amdgcn_readfirstlane((u32)tstart) + T3F;
        wait_flag<2, 2>(F_L, WOP);
#define WOL "buffer_load_dwordx4 %2, %3, %1 offen nt lds\n\t"
#define WON(d) "s_add_u32 m0, m0, 0x400\n\ts_add_i32 %1, %1, " #d "\n\t"
        asm volatile("s_mov_b32 m0, %4\n\t"
                     "buffer_load_dwordx4 %0, %5, %6, %7 offen sc1\n\t"
                     WOL WON(4096) WOL WON(-3072) WOL WON(4096) WOL
                     : "=&v"(gm), "+s"(so) : "v"(wvo), "s"(r_wo), "s"(lw), "v"(mvo), "s"(r_ws), "s"(ms2) : "memory", "scc");
        wait_flag<3, 1>(F_L, 33);
        if (ODELAY) __builtin_amdgcn_s_sleep(ODELAY);
        asm volatile("s_memrealtime s[36:37]\n s_waitcnt lgkmcnt(0)\n s_sub_i32 s36, s36, %[t3]\n s_cmp_lt_i32 s36, 0\n"
                     "s_cselect_b64 exec, 0, %[m]\n"
                     "buffer_load_dwordx4 v[128:131], %[vo], %[rs], %[so] offen sc1\n buffer_load_dwordx4 v[132:135], %[vo], %[rs], %[so] offen offset:512 sc1\n"
                     "buffer_load_dwordx4 v[136:139], %[vo], %[rs], %[so] offen offset:1024 sc1\n buffer_load_dwordx4 v[140:143], %[vo], %[rs], %[so] offen offset:1536 sc1\n"
                     "s_mov_b64 exec, -1\n"
                     "s_waitcnt vmcnt(8)"
                     : OREG("+"), "+v"(gm) : [vo] "v"(ovo), [rs] "s"(r_ws), [so] "s"(oso), [m] "s"(0x00ff00ff00ff00ffull), [t3] "s"(t3) : "memory", "s36", "s37", "scc");
        STAMP(6);
        const bool early = all_lanes((gm.y == tag) & (gm.w == tag));
        weights();
        WAITVM(4);
        set_flag(F_W + w * 4, 1);
        STAMP(25);
        u32 t0, t1;
        unsigned long long m1, m2;
        if (OSTEP2) asm volatile(OCHK OLD : OREG("+"), [t0] "=&v"(t0), [t1] "=&v"(t1), [m1] "=&s"(m1) : [vo] "v"(ovo), [rs] "s"(r_ws), [so] "s"(oso) : "memory", "vcc");
        wait_flag<4, 0>(F_W, 1);
#pragma unroll
        for (int k = 0; k < 4; ++k)
#pragma unroll
            for (int ct = 0; ct < 4; ++ct) wf[k][ct] = trfrag(wa[ct] + k * 4096);
        SB();
        if (!early) {
#pragma unroll 1
            for (int spin = 0; spin < SPIN_MAX; ++spin) {
                asm volatile("buffer_load_dwordx4 %0, %1, %2, %3 offen sc1\n\ts_waitcnt vmcnt(0)" : "=v"(gm) : "v"(mvo), "s"(r_ws), "s"(ms2) : "memory");
                if (all_lanes((gm.y == tag) & (gm.w == tag))) break;
            }
            weights();
        }
        STAMP(11);
#pragma unroll 1
        for (int i = 0; i < 4096; ++i) { if ((int)(now32() - t3) >= 0) break; __builtin_amdgcn_s_sleep(1); }
        u32 n = SPIN_MAX;
        asm volatile("s_waitcnt vmcnt(0)\n1:\n" OCHK
                     "s_or_b64 %[m2], vcc, %[m1]\n"
                     "s_cbranch_scc0 2f\n"
                     OLD
                     "s_waitcnt vmcnt(%[cap])\n"
                     "s_sub_u32 %[n], %[n], 1\n"
                     "s_cbranch_scc0 1b\n"
                     "2:\n"
                     : OREG("+"), [t0] "=&v"(t0), [t1] "=&v"(t1), [m1] "=&s"(m1), [m2] "=&s"(m2), [n] "+s"(n)
                     : [vo] "v"(ovo), [rs] "s"(r_ws), [so] "s"(oso), [cap] "i"(OCAP), [slp] "i"(OSLP) : "memory", "vcc", "scc");
    }
    {
        STAMP(12);
        v4f c[4];
#pragma unroll
        for (int ct = 0; ct < 4; ++ct) c[ct] = (v4f){0.f, 0.f, 0.f, 0.f};
#pragma unroll
        for (int k = 0; k < 4; ++k) {
#pragma unroll
            for (int ct = 0; ct < 4; ++ct) c[ct] = mfma16(av[k], wf[k][ct], c[ct]);
            SB();
        }
        float z[4];
#pragma unroll
        for (int ct = 0; ct < 4; ++ct) {
            const v2f c01 = {c[ct].x, c[ct].y}, c23 = {c[ct].z, c[ct].w};
            const v2f t = __builtin_elementwise_fma(c23, W23, c01 * W01);
            z[ct] = t.x + t.y;
        }
        const v2f r0 = swap16(z[0], z[1]), r1 = swap16(z[2], z[3]);
        const u32 y = umn(pkbf(r0.x + r0.y, r1.x + r1.y), ~1u);
        const v4u ga = {y, dppu<0x55>(y), dppu<0xAA>(y), dppu<0xFF>(y)};
        ST128(ga, r_ws, avs, aline, AUX_SC1);
    }
    asm volatile("" :: OIN);
    set_flag(F_A + w * 4, 3);
    STAMP(13);
}

extern "C" void kernel_launch(void* const* d_in, const int* in_sizes, int n_in, void* d_out, int out_size, void* d_ws, size_t ws_size, hipStream_t stream) {
    constexpr u32 F = 0x00020000;
    fused_decode_kernel<<<256, 640, 0, stream>>>((const u16*)d_in[0], 4096u, F, (const u16*)d_in[3], 4u << 20, F, (const u16*)d_in[1], 64u << 20, F,
                                                 (const u16*)d_in[2], 64u << 20, F, (unsigned char*)d_ws, WS_END, F, (const u16*)d_in[6], 3584u, F,
                                                 (const u16*)d_in[7], 3584u, F, (const u16*)d_in[8], 3584u, F, (const u16*)d_in[9], 2u, F,
                                                 (const u16*)d_in[4], (u16*)d_out);
}
```

```cpp
#define TQ 210
#define PD 256
#define PC 600
#define T3F 660
#define HN 0
#include <hip/hip_runtime.h>
#include <stdint.h>

typedef unsigned u32;
typedef unsigned short u16;
typedef u32 v4u __attribute__((ext_vector_type(4)));
typedef u32 v2u __attribute__((ext_vector_type(2)));
typedef float v4f __attribute__((ext_vector_type(4)));
typedef float v2f __attribute__((ext_vector_type(2)));
typedef __bf16 v8bf __attribute__((ext_vector_type(8)));
typedef __bf16 v2bf __attribute__((ext_vector_type(2)));
typedef short v4s __attribute__((ext_vector_type(4)));
#define LAS __attribute__((address_space(3)))

#define STAMP(i) do { } while (0)
#define PUTST(a, b) do { } while (0)

#define MAPG 0
#define WIN 20
#define QP 13
#define WOP 32
#define XP 0
#define PRE 0
#define TN 16
#define TWAIT 0
#define WOE 4
#define GDELAY 10
#define KFIRST 0
#define OST16 0
#define AST16 1
#define ODELAY 0
#define ADELAY 0
#define ROTA 6
#define ROTB 3
#define ROTC 0
#define SPREAD 1
#define WSOFF 0
#define ILV 0
#define FINM 0
#define FINC 0
#define JUNK 15
#define T1F 480
#define WOST 5
#define TG 2
#define OSLP 0
#define OCAP 0
#define OSTEP2 0
#define ACAP 8
#define ASLP 0
#define T2F 0
#define LP5 1
#define ORST 0
#define ARST 0
#define DRY 0
#define CSW 1
#define OSEG 128
#define WOT 63
#define NUMA 0
#define SENSE 0
#define EPE 0
#define OAUX 16
#define NOFB 0
#define V5FMT 4

constexpr int AUX_SC1 = 16;
constexpr u32 WS_EPOCH = 0;
constexpr u32 GQ = 262144, GO = 262144, GML = 262144, GA = 524288;
constexpr u32 WS_Q = 4096;
constexpr u32 WS_O = 65536;
constexpr u32 WS_ML = 131072;
constexpr u32 WS_RD = 131072 + 2048;
constexpr u32 WS_A = 163840;
constexpr u32 WS_T = 262144 + 163840;
constexpr u32 WS_END = 2097152;
constexpr u32 L_V = 0, L_K = 0x10000, L_W = 0x20000, L_TN = 0x24000, L_X = 0x25000, L_XN = 0x26000, L_QI = 0x26500, L_P = 0x26A00,
              L_TP = 0x27200, L_JUNK = 0x27600, L_FL = 0x27A00, L_M = 0x27A80, L_LS = 0x27AC0, L_END = 0x27B00;
constexpr u32 F_A = L_FL, F_Q = L_FL + 16, F_M = L_FL + 32, F_P = L_FL + 48, F_W = L_FL + 64, F_L = L_FL + 80, F_ARR = L_FL + 88, F_FB = L_FL + 92, L_ST = L_FL + 96;
constexpr u32 TAG_STEP = 0x01010101u;
constexpr u32 L_TAB = 0xCE00;
constexpr u32 L_OS = L_K, L_FB = L_K + 0x2000, L_FP = L_K + 0x6000;
constexpr u32 T_LO = 114 * 128;
constexpr int SPIN_MAX = 1 << 18;
#define CODEB 11776
constexpr u32 CODE_BYTES = CODEB;
constexpr int OOB = 0x40000000;
constexpr float LOG2E = 1.4426950408889634f;

__device__ __forceinline__ float u2f(u32 v) { return __builtin_bit_cast(float, v); }
__device__ __forceinline__ u32 f2u(float v) { return __builtin_bit_cast(u32, v); }
__device__ __forceinline__ float bflo(u32 v) { return u2f(v << 16); }
__device__ __forceinline__ float bfhi(u32 v) { return u2f(v & 0xffff0000u); }
__device__ __forceinline__ v2f bf2(u32 v) { v2f r = {bflo(v), bfhi(v)}; return r; }
__device__ __forceinline__ u32 pk2(v2f f) { return __builtin_bit_cast(u32, __builtin_convertvector(f, v2bf)); }
__device__ __forceinline__ u32 pkbf(float a, float b) { v2f f = {a, b}; return pk2(f); }
__device__ __forceinline__ float rbf(float a) { return u2f(pkbf(0.f, a)); }
template <int CTRL> __device__ __forceinline__ float dppf(float v) { return u2f((u32)__builtin_amdgcn_update_dpp(0, (int)f2u(v), CTRL, 0xf, 0xf, true)); }
template <int CTRL> __device__ __forceinline__ u32 dppu(u32 v) { return (u32)__builtin_amdgcn_update_dpp(0, (int)v, CTRL, 0xf, 0xf, true); }
constexpr int DPP_X1 = 0xB1, DPP_X2 = 0x4E, DPP_HMIR = 0x141, DPP_ROR4 = 0x124, DPP_ROR8 = 0x128, DPP_SHL4 = 0x104, DPP_SHL8 = 0x108;
__device__ __forceinline__ float fmx(float a, float b) { return __builtin_fmaxf(a, b); }
__device__ __forceinline__ u32 umn(u32 a, u32 b) { return a < b ? a : b; }
__device__ __forceinline__ u32 umx(u32 a, u32 b) { return a > b ? a : b; }
__device__ __forceinline__ u32 umx4(v4u a) { return umx(umx(a.x, a.y), umx(a.z, a.w)); }
__device__ __forceinline__ v2f swap32(float a, float b) {
    auto r = __builtin_amdgcn_permlane32_swap(f2u(a), f2u(b), false, false);
    v2f o = {u2f(r[0]), u2f(r[1])}; return o;
}
__device__ __forceinline__ v2f swap16(float a, float b) {
    auto r = __builtin_amdgcn_permlane16_swap(f2u(a), f2u(b), false, false);
    v2f o = {u2f(r[0]), u2f(r[1])}; return o;
}
__device__ __forceinline__ float sum_x16_x32(float v) { v2f a = swap16(v, v); v = a.x + a.y; a = swap32(v, v); return a.x + a.y; }
__device__ __forceinline__ float max_x16_x32(float v) { v2f a = swap16(v, v); v = fmx(a.x, a.y); a = swap32(v, v); return fmx(a.x, a.y); }
__device__ __forceinline__ float row_sum(float v) { v += dppf<DPP_X1>(v); v += dppf<DPP_X2>(v); v += dppf<DPP_HMIR>(v); v += dppf<DPP_ROR8>(v); return v; }
__device__ __forceinline__ float wave_sum(float v) { return sum_x16_x32(row_sum(v)); }
__device__ __forceinline__ v4f mfma16(v4u a, v4u b, v4f c) {
    return __builtin_amdgcn_mfma_f32_16x16x32_bf16(__builtin_bit_cast(v8bf, a), __builtin_bit_cast(v8bf, b), c, 0, 0, 0);
}
__device__ __forceinline__ void lds_barrier() { asm volatile("s_waitcnt lgkmcnt(0)\n\ts_barrier" ::: "memory"); }
#define SB() __builtin_amdgcn_sched_barrier(0)
#define WAITVM(n) asm volatile("s_waitcnt vmcnt(%0)" :: "i"(n) : "memory")
__device__ __forceinline__ __amdgpu_buffer_rsrc_t mkrsrc(const void* p, u32 bytes) { return __builtin_amdgcn_make_buffer_rsrc((void*)p, 0, bytes, 0x00020000); }
__device__ __forceinline__ bool all_lanes(bool p) { return __builtin_amdgcn_ballot_w64(!p) == 0ull; }
__device__ __forceinline__ void junk_load(__amdgpu_buffer_rsrc_t rs, int vo, u32 lds) {
    u32 keep;
    asm volatile("s_mov_b32 %0, m0\n\ts_mov_b32 m0, %3\n\ts_nop 0\n\tbuffer_load_ushort %1, %2, 0 offen lds\n\ts_mov_b32 m0, %0" : "=&s"(keep) : "v"(vo), "s"(rs), "s"(lds) : "memory");
}
#define LD128(r, vo, so, aux) __builtin_amdgcn_raw_buffer_load_b128(r, vo, so, aux)
#define ST128(d, r, vo, so, aux) do { v4u d_ = (d); asm volatile("" : "+v"(d_)); __builtin_amdgcn_raw_buffer_store_b128(d_, r, vo, so, aux); asm volatile("s_nop 1" :: "v"(d_)); } while (0)
template <typename T> __device__ __forceinline__ LAS T* lp(u32 a) { return (LAS T*)(size_t)a; }
__device__ __forceinline__ u32 pin(u32 v) { asm volatile("" : "+v"(v)); return v; }
__device__ __forceinline__ v4u trfrag(u32 a) {
    const v2u lo = __builtin_bit_cast(v2u, __builtin_amdgcn_ds_read_tr16_b64_v4i16(lp<v4s>(a)));
    const v2u hi = __builtin_bit_cast(v2u, __builtin_amdgcn_ds_read_tr16_b64_v4i16(lp<v4s>(a + 2048)));
    v4u r = {lo.x, lo.y, hi.x, hi.y}; return r;
}
__device__ __forceinline__ void set_flag(u32 a, u32 v) { asm volatile("" ::: "memory"); *lp<volatile u32>(a) = v; }
__device__ __forceinline__ u32 umn4(v4u a) { return umn(umn(a.x, a.y), umn(a.z, a.w)); }
__device__ __forceinline__ bool uni_ge(u32 x, u32 v) { return (u32)__builtin_amdgcn_readfirstlane(x) >= v; }
template <int N, int SLP> __device__ __forceinline__ void wait_flag(u32 a, u32 v) {
#pragma unroll 1
    for (int i = 0; i < SPIN_MAX; ++i) {
        u32 m;
        if (N == 1) m = *lp<volatile u32>(a);
        else if (N == 2) { const v2u f = *lp<volatile v2u>(a); m = umn(f.x, f.y); }
        else if (N == 3) { const v4u f = *lp<volatile v4u>(a); m = umn(umn(f.x, f.y), f.z); }
        else m = umn4(*lp<volatile v4u>(a));
        if (uni_ge(m, v)) break;
        if (SLP) __builtin_amdgcn_s_sleep(SLP);
    }
    asm volatile("" ::: "memory");
}
__device__ __forceinline__ u32 now32() { unsigned long long t; asm volatile("s_memrealtime %0\n\ts_waitcnt lgkmcnt(0)" : "=s"(t) :: "memory"); return (u32)t; }
struct Coef { v2f w[4], b[4], g1[4], g0[4]; };
__device__ __forceinline__ float mlp_part(const Coef& c, float t) {
    const v2f t2 = {t, t};
    const float K0 = -2.0f * LOG2E * 0.7978845608028654f, K1 = K0 * 0.044715f;
    const v2f k0 = {K0, K0}, k1 = {K1, K1}, one = {1.f, 1.f};
    v2f acc = {0.f, 0.f};
#pragma unroll
    for (int i = 0; i < 4; ++i) {
        const v2f u = __builtin_elementwise_fma(t2, c.w[i], c.b[i]);
        const v2f a = u * __builtin_elementwise_fma(u * u, k1, k0);
        v2f e = {__builtin_amdgcn_exp2f(a.x), __builtin_amdgcn_exp2f(a.y)};
        e += one;
        const v2f rc = {__builtin_amdgcn_rcpf(e.x), __builtin_amdgcn_rcpf(e.y)};
        acc = __builtin_elementwise_fma(__builtin_elementwise_fma(t2, c.g1[i], c.g0[i]), rc, acc);
    }
    return acc.x + acc.y;
}

__device__ __forceinline__ v4u load_part(const u16* p, int part, int l) { return LD128(mkrsrc(p + part * 448, 896), l * 16, 0, 0); }
__device__ __forceinline__ void make_coef(Coef& cf, v4u ta, v4u tb, v4u tc) {
#pragma unroll
    for (int i = 0; i < 4; ++i) { const v2f c = bf2(tc[i]); cf.w[i] = bf2(ta[i]); cf.b[i] = bf2(tb[i]); cf.g1[i] = cf.w[i] * c; cf.g0[i] = cf.b[i] * c; }
}

#define DESC(T, p) T p, u32, u32
__global__ void __launch_bounds__(640)
fused_decode_kernel(DESC(const u16*, x), DESC(const u16*, wq), DESC(const u16*, kc), DESC(const u16*, vc), DESC(unsigned char*, ws),
                    DESC(const u16*, w1), DESC(const u16*, b1), DESC(const u16*, w2), DESC(const u16*, b2), const u16* wo, u16* out) {
    __shared__ __attribute__((aligned(16))) unsigned char smem[L_END];
    STAMP(0);
    u32 tag0, tag, b2r;
    v4u ca, cb, cc;
    int wave2;
    unsigned long long tstart;
    const int bid = blockIdx.x;
    const int pid = MAPG ? (bid & 192) | ((bid & 7) << 3) | ((bid >> 3) & 7) : bid;
    {
        u32 t0, t1, t2, t3;
#define WQL "s_add_u32 m0, m0, 0x400\n s_add_u32 s73, s73, 0x10000\n buffer_load_dwordx4 %[t3], s[40:43], s73 offen nt lds\n"
#define KVL "buffer_load_dwordx4 %[t3], s[44:47], s73 offen nt lds\n s_add_u32 m0, m0, 0x400\n s_add_u32 s73, s73, 0x800\n"
        asm volatile(
            "s_load_dwordx16 s[36:51], %[ka], 0x0\n"
            "s_load_dwordx16 s[52:67], %[ka], 0x40\n"
            "s_getpc_b64 s[76:77]\n"
            "v_readfirstlane_b32 %[wv], %[tid]\n"
            "v_bfe_u32 %[t0], %[tid], 3, 3\n"
            "v_and_b32 %[t1], 6, %[t0]\n"
            "v_xor_b32 %[t1], %[t1], %[tid]\n"
            "v_and_b32 %[t1], 7, %[t1]\n"
            "v_lshlrev_b32 %[t1], 4, %[t1]\n"
            "s_lshr_b32 %[wv], %[wv], 6\n"
            "s_and_b32 s72, %[pid], 7\n"
            "s_cmp_gt_u32 %[wv], 3\n"
            "s_cbranch_scc1 2f\n"
            "s_setprio 2\n"
            "v_lshlrev_b32 %[t2], 4, %[tid]\n"
            "v_lshl_or_b32 %[t3], %[t0], 13, %[t1]\n"
            "s_lshr_b32 s75, %[pid], 6\n"
            "s_lshl_b32 s73, s75, 2\n"
            "s_add_u32 s73, s73, %[wv]\n"
            "s_lshl_b32 s73, s73, 11\n"
            "s_and_b32 s74, %[pid], 63\n"
            "s_or_b32 s73, s73, s74\n"
            "s_lshl_b32 s73, s73, 7\n"
            "s_lshl_b32 s74, %[wv], 10\n"
            "s_add_u32 m0, s74, %[lx]\n"
            "s_lshl_b32 s74, s74, 2\n"
            "s_add_u32 s74, s74, %[lw]\n"
            "v_bfe_u32 %[t0], %[tid], 5, 1\n"
            "v_and_b32 %[t1], 31, %[tid]\n"
            "v_lshlrev_b32 %[t1], 2, %[t1]\n"
            "v_lshl_or_b32 %[t0], %[t0], 10, %[t1]\n"
            "s_and_b32 s78, %[pid], 56\n"
            "s_lshl_b32 s78, s78, 3\n"
            "s_lshl1_add_u32 s78, %[wv], s78\n"
            "s_lshl1_add_u32 s78, s78, s75\n"
            "s_lshl_b32 s78, s78, 4\n"
            "s_or_b32 s78, s78, s72\n"
            "s_lshl_b32 s78, s78, 7\n"
            "s_add_u32 s78, s78, %[wso]\n"
            "v_mov_b32 %[t1], -1\n"
            "s_waitcnt lgkmcnt(0)\n"
            "buffer_load_dwordx4 %[t2], s[36:39], 0 offen lds\n"
            "s_mov_b32 m0, s74\n"
            "s_nop 0\n"
            "buffer_load_dwordx4 %[t3], s[40:43], s73 offen nt lds\n"
            WQL WQL WQL
            "buffer_load_dword %[tag], off, s[52:55], 0\n"
#if !(XP & 72) && ORST < 2
            "buffer_store_dword %[t1], %[t0], s[52:55], s78 offen sc1\n"
#else
            "buffer_load_dword %[tag], off, s[52:55], 0\n"
#endif
            "s_barrier\n"
            "s_memrealtime %[ts]\n"
            "s_waitcnt lgkmcnt(0)\n"
            "s_branch 9f\n"
            "2:\n"
            "s_cmp_gt_u32 %[wv], 5\n"
            "s_cbranch_scc1 5f\n"
            "s_setprio 3\n"
            "s_sub_u32 s75, %[wv], 4\n"
            "v_lshl_or_b32 %[t3], %[t0], 8, %[t1]\n"
            "s_lshr_b32 s74, %[pid], 3\n"
            "s_lshr_b32 s73, %[pid], 6\n"
            "s_mul_i32 s73, s73, %[rotc]\n"
            "s_mul_i32 s78, s74, %[rotb]\n"
            "s_add_u32 s73, s73, s78\n"
            "s_mul_i32 s78, s72, %[rota]\n"
            "s_add_u32 s73, s73, s78\n"
            "s_lshl_b32 s73, s73, 5\n"
            "s_or_b32 s73, s73, s75\n"
            "s_lshl_b32 s73, s73, 7\n"
            "s_lshl_b32 s74, s74, 5\n"
            "s_or_b32 s74, s74, s72\n"
            "s_or_b32 s74, s74, 0x18\n"
            "s_pack_ll_b32_b16 s73, s73, s74\n"
            "s_lshl_b32 s78, s75, 15\n"
            "s_add_u32 m0, s78, %[lk]\n"
            "s_lshl2_add_u32 s75, s75, %[fl]\n"
            "v_mov_b32 %[t0], s75\n"
            "s_mov_b32 s75, 0\n"
            "s_waitcnt lgkmcnt(0)\n"
            "s_barrier\n"
            "s_memrealtime %[ts]\n"
            KVL KVL
            "s_waitcnt lgkmcnt(0)\n"
            "s_mov_b64 s[70:71], %[ts]\n"
            "s_lshl_b32 s79, s70, 4\n"
            "s_add_u32 s79, s79, %[pc]\n"
            "3:\n"
            "s_pack_ll_b32_b16 s73, s73, s74\n"
            "s_add_u32 s75, s75, 1\n"
            "s_cmp_eq_u32 s75, 16\n"
            "s_cselect_b64 s[44:45], s[48:49], s[44:45]\n"
            "s_cselect_b32 m0, s78, m0\n"
            "v_mov_b32 %[t1], s75\n"
            "s_waitcnt vmcnt(%[win])\n"
            "ds_write_b32 %[t0], %[t1]\n"
            "4:\n"
            "s_memrealtime s[70:71]\n"
            "s_waitcnt lgkmcnt(0)\n"
            "s_lshl_b32 s72, s70, 4\n"
            "s_sub_i32 s72, s72, s79\n"
            "s_cmp_lt_i32 s72, 0\n"
            "s_cbranch_scc1 4b\n"
            KVL KVL
            "s_add_u32 s79, s79, %[pd]\n"
            "s_cmp_lg_u32 s75, 31\n"
            "s_cbranch_scc1 3b\n"
            "s_branch 9f\n"
            "5:\n"
            "s_load_dwordx4 s[68:71], %[ka], 0x80\n"
            "s_sub_u32 s75, %[wv], 6\n"
            "s_and_b32 s74, %[bid], 56\n"
            "s_lshl1_add_u32 s74, s75, s74\n"
            "s_lshl_b32 s74, s74, 8\n"
            "s_add_u32 s78, s74, 512\n"
            "s_min_u32 s78, s78, %[code]\n"
            "s_mov_b32 s79, 0x20000\n"
            "s_and_b32 s76, s76, 0xffffff00\n"
            "v_and_b32 %[t2], 63, %[tid]\n"
            "v_cmp_gt_u32 vcc, 56, %[t2]\n"
            "v_lshl_add_u32 %[t3], %[t2], 7, s74\n"
#if !(XP & 32)
            "buffer_load_dword %[b2r], %[t3], s[76:79], 0 offen\n"
#endif
            "v_and_b32 %[t0], 31, %[tid]\n"
            "v_lshlrev_b32 %[t0], 2, %[t0]\n"
            "v_add_u32 %[t0], %[lfl], %[t0]\n"
            "v_mov_b32 %[t1], 0\n"
            "ds_write_b32 %[t0], %[t1]\n"
            "s_mulk_i32 s75, 896\n"
            "v_lshl_add_u32 %[t3], %[t2], 4, s75\n"
            "v_cndmask_b32 %[t3], 2.0, %[t3], vcc\n"
            "s_waitcnt lgkmcnt(0)\n"
            "s_barrier\n"
            "buffer_load_dwordx4 %[ca], %[t3], s[56:59], 0 offen\n"
            "buffer_load_dwordx4 %[cb], %[t3], s[60:63], 0 offen\n"
            "buffer_load_dwordx4 %[cc], %[t3], s[64:67], 0 offen\n"
            "buffer_load_dword %[tag], off, s[52:55], 0\n"
            "buffer_load_ushort %[b2r], off, s[68:71], 0\n"
            "9:\n"
            : [wv] "=&s"(wave2), [ts] "=&s"(tstart), [tag] "=&v"(tag0), [b2r] "=&v"(b2r), [ca] "=&v"(ca), [cb] "=&v"(cb), [cc] "=&v"(cc),
              [t0] "=&v"(t0), [t1] "=&v"(t1), [t2] "=&v"(t2), [t3] "=&v"(t3)
            : [ka] "s"(__builtin_amdgcn_kernarg_segment_ptr()), [pid] "s"(pid), [bid] "s"(bid), [tid] "v"(threadIdx.x), "s"((u32)(size_t)(LAS void*)smem),
              [lx] "i"(L_X), [lw] "i"(L_W), [lk] "i"(L_K), [fl] "i"(F_L), [lfl] "i"(L_FL), [wso] "i"(WS_O), [code] "i"(CODE_BYTES), [win] "i"(WIN - 2), [rota] "i"(ROTA), [rotb] "i"(ROTB), [rotc] "i"(ROTC), [pd] "i"(PD), [pc] "i"(PC), [sense] "i"(SENSE)
            : "memory", "scc", "vcc", "s36", "s37", "s38", "s39", "s40", "s41", "s42", "s43", "s44", "s45", "s46", "s47", "s48", "s49", "s50", "s51", "s52", "s53", "s54", "s55", "s56", "s57", "s58", "s59", "s60", "s61", "s62", "s63", "s64", "s65", "s66", "s67", "s68", "s69", "s70", "s71", "s72", "s73", "s74", "s75", "s76", "s77", "s78", "s79", "s80");
        static_assert(L_V == 0 && OOB == 0x40000000 && F_ARR == F_L + 8, "");
    }
    SB();
    const int l = threadIdx.x & 63;
    const int s = pid & 7, g = (pid >> 3) & 7, b = pid >> 6;
    const int gc = CSW ? s : g, sc = CSW ? g : s;
    const int n16 = l & 15, kg = l >> 4, lr = l >> 3;
    const int swz = ((l & 7) ^ (lr & 6)) * 16;
    const __amdgpu_buffer_rsrc_t r_ws = mkrsrc(ws, WS_END);
    const u32 fra = (kg * 4 + (n16 >> 2)) * 128 + (((kg & 1) * 2 + (n16 >> 3)) * 32) + (n16 & 3) * 8;
    const bool fin = (pid & 63) == FINM * b + FINC;
    constexpr int OH = (HN & 1) ? 16384 : 8192;
    const int opg = (HN & 1) ? (((g >> 2) ^ SENSE) & 1) * 8192 : 0, opc = (HN & 1) ? (((gc >> 2) ^ SENSE) & 1) * 8192 : 0;
    const int aoff = (HN & 2) ? (sc >> 1) * 16384 + SENSE * 8192 + (sc & 1) * 4096 + (gc * 4 + b) * 128 : (sc * 32 + gc * 4 + b) * 128;

    if (wave2 >= 4 && wave2 < 6) {
        STAMP(7);
        const int dh = wave2 - 4;
        const u32 lv = L_V + dh * 32768, fl = F_L + dh * 4;
        const u32 pa = pin(L_P + kg * 64 + (n16 >> 2) * 16);
        u32 va[4];
#pragma unroll
        for (int t = 0; t < 4; ++t) va[t] = pin((lv + fra) ^ (t * 32));
        const int ovo = pin(kg * OH + n16 * 8);
        int oso = WS_O + g * GO + opg + b * 2048 + dh * 1024 + s * OSEG;
        asm volatile("" : "+s"(oso));
        static_assert(16 + (WIN - 2) / 2 <= 31 && WIN % 2 == 0, "");
        const __amdgpu_buffer_rsrc_t r_wo = mkrsrc(wo, 4u << 20);
        const int wvo = pin((16 * (lr & 3) + 4 * (lr >> 2)) * 1024 + swz);
        u32 wso = (gc * 4 + b) * (128 * 1024) + dh * 65536 + sc * 128;
#define WOL "buffer_load_dwordx4 %1, %2, %0 offen nt lds\n\t"
#define WON(d) "s_add_u32 m0, m0, 0x400\n\ts_add_i32 %0, %0, " #d "\n\t"
#define WOISSUE() asm volatile("s_mov_b32 m0, %3\n\ts_nop 0\n\t" \
                         WOL WON(8192) WOL WON(-7168) WOL WON(8192) WOL WON(-7168) WOL WON(8192) WOL WON(-7168) WOL WON(8192) WOL \
                         : "+s"(wso) : "v"(wvo), "s"(r_wo), "s"(L_W + dh * 8192) : "memory", "scc")
        v4u vf[8][4], pf[8];
        v4f acc[4];
#pragma unroll
        for (int t = 0; t < 4; ++t) acc[t] = (v4f){0.f, 0.f, 0.f, 0.f};
#define rd(st) do { SB(); _Pragma("unroll") for (int t = 0; t < 4; ++t) vf[st][t] = trfrag(va[t] + st * 4096); SB(); } while (0)
#define mm(st) do { _Pragma("unroll") for (int t = 0; t < 4; ++t) acc[t] = mfma16(pf[st], vf[st][t], acc[t]); } while (0)
        static_assert(WIN == 20, "");
        {
            const v4u fa = *lp<volatile v4u>(F_A), f = *lp<volatile v4u>(F_P);
            asm volatile("" ::: "memory");
#pragma unroll
            for (int st = 0; st < 8; ++st) pf[st] = *lp<v4u>(pa + st * 256);
            rd(0); rd(1); rd(2);
            if (!uni_ge(umn4(fa), 2)) wait_flag<4, 1>(F_A, 2);
            WOISSUE();
            STAMP(16);
            if (!uni_ge(umn4(f), 1)) {
                wait_flag<4, 1>(F_P, 1);
#pragma unroll
                for (int st = 0; st < 8; ++st) pf[st] = *lp<v4u>(pa + st * 256);
            }
            SB();
        }
        STAMP(17);
        mm(0); mm(1);
        SB(); WAITVM(24); rd(3); mm(2);
        SB(); WAITVM(20); rd(4); mm(3);
        SB(); WAITVM(16); rd(5); mm(4);
        SB(); WAITVM(12); rd(6); mm(5);
        SB();
        WAITVM(10);
        v2u lo[4];
#pragma unroll
        for (int t = 0; t < 4; ++t) lo[t] = __builtin_bit_cast(v2u, __builtin_amdgcn_ds_read_tr16_b64_v4i16(lp<v4s>(va[t] + 7 * 4096)));
        SB();
        mm(6);
        SB();
        WAITVM(8);
        STAMP(8);
#pragma unroll
        for (int t = 0; t < 4; ++t) {
            const v2u hi = __builtin_bit_cast(v2u, __builtin_amdgcn_ds_read_tr16_b64_v4i16(lp<v4s>(va[t] + 7 * 4096 + 2048)));
            vf[7][t] = (v4u){lo[t].x, lo[t].y, hi.x, hi.y};
        }
        SB();
        mm(7);
#undef rd
#undef mm
        const v2u go = {umn(pkbf(acc[0].x, acc[1].x), ~1u), umn(pkbf(acc[2].x, acc[3].x), ~1u)};
        __builtin_amdgcn_raw_buffer_store_b64(go, r_ws, ovo, oso, AUX_SC1);
        set_flag(fl, 33);
        STAMP(9);
        WAITVM(1);
        set_flag(fl, 34);
        STAMP(25);
        if (dh == 0) { PUTST(7, 10); PUTST(16, 18); PUTST(25, 26); }
        __builtin_amdgcn_endpgm();
    }

    if (wave2 >= 6) {
        const int tw = wave2 - 6;
        const int tj = sc * 32 + gc * 4 + b;
        const v4u ones = {~0u, ~0u, ~0u, ~0u};
        ST128(ones, r_ws, l < 8 && !(XP & 136) && !ARST ? l * 16 : OOB, WS_A + tw * GA + aoff, AUX_SC1);
        {
            const u16* p = tw == 0 ? vc + ((size_t)(b * 8 + g) * 8192 + 6144 + s * 256) * 128 : tw == 1 ? wo + (gc * 4 + b) * 65536 : out;
            junk_load(mkrsrc(p, ((JUNK >> tw) & 1) && (tw != 2 || fin || JUNK >= 8) ? 2 : 0), l * 4, L_JUNK);
        }
        if (TWAIT) wait_flag<4, 4>(F_A, TWAIT);
        asm volatile("s_waitcnt vmcnt(2)" : "+v"(ca), "+v"(cb), "+v"(cc), "+v"(tag0), "+v"(b2r) :: "memory");
        SB();
        Coef cf;
        make_coef(cf, ca, cb, cc);
        tag = tag0 + TAG_STEP;
        {
            float t = u2f((u32)(((tj & 128) << 8) + T_LO + (tj & 127)) << 16);
            u32 ta = n16 == 0 ? L_TP + tw * 16 + kg * 4 : L_JUNK;
#pragma unroll 1
            for (int e = 0; e < TN; ++e) {
                *lp<float>(ta) = row_sum(mlp_part(cf, t));
                ta += n16 == 0 ? 64 : 0;
                t += t;
            }
        }
        if (ARST) ST128(ones, r_ws, l < 8 ? l * 16 : OOB, WS_A + tw * GA + tj * 128, AUX_SC1);
        if (ORST == 4) __builtin_amdgcn_raw_buffer_store_b32(~0u, r_ws, kg * 512 + n16 * 4, WS_O + g * GO + tw * 8192 + b * 2048 + s * 64, AUX_SC1);
        WAITVM(0);
        {
            u32 arr = 0;
            if (l == 0) arr = __hip_atomic_fetch_add(lp<u32>(F_ARR), 9u, __ATOMIC_RELAXED, __HIP_MEMORY_SCOPE_WORKGROUP);
            asm volatile("" ::: "memory");
            if (__builtin_amdgcn_readfirstlane(arr) == 27) {
                const v4f p = *lp<v4f>(L_TP + l * 16);
                float v = (p.x + p.y) + (p.z + p.w);
                v += dppf<DPP_X1>(v); v += dppf<DPP_X2>(v);
                v = rbf(v) + bflo(b2r);
                const u32 p0 = pkbf(v, dppf<DPP_SHL4>(v));
                const v4u gt = {p0, tag, dppu<DPP_SHL8>(p0), tag};
                ST128(gt, r_ws, n16 == 0 ? kg * 16 : OOB, WS_T + tj * 64, AUX_SC1);
            }
        }
        STAMP(10);
        if (tw == 0) PUTST(10, 11);
        if (!fin) __builtin_amdgcn_endpgm();
        __builtin_amdgcn_s_setprio(1);
        if (TG == 0) __builtin_amdgcn_s_sleep(GDELAY);
        if (TG == 1) wait_flag<2, 4>(F_L, 32);
        if (TG == 2) wait_flag<2, 4>(F_L, 33);
        if (TG == 3) wait_flag<4, 4>(F_W, 1);
        {
            v4u t[4];
            bool bad = true;
#pragma unroll 1
            for (int spin = 0; spin < SPIN_MAX; ++spin) {
                asm volatile("" ::: "memory");
                if (bad) {
#pragma unroll
                    for (int q = 0; q < 4; ++q) t[q] = LD128(r_ws, l * 16 + q * 1024, WS_T + tw * 4096, AUX_SC1);
                }
                u32 e = 0;
#pragma unroll
                for (int q = 0; q < 4; ++q) e |= (t[q].y ^ tag) | (t[q].w ^ tag);
                bad = e != 0;
                if (all_lanes(!bad)) break;
            }
            if (TG == 0) wait_flag<4, 2>(F_P, 1);
            const u32 a = pin(L_TAB + 2 * T_LO + (tw >> 1) * 65536 + ((tw & 1) * 64 + (l >> 2)) * 2 + (l & 3) * 1024);
#pragma unroll
            for (int q = 0; q < 4; ++q) {
                *lp<u16>(a + q * 32) = (u16)t[q].x; *lp<u16>(a + q * 32 + 256) = (u16)(t[q].x >> 16);
                *lp<u16>(a + q * 32 + 512) = (u16)t[q].z; *lp<u16>(a + q * 32 + 768) = (u16)(t[q].z >> 16);
            }
        }
        const int D = tw * 64 + (n16 >> 3) * 32 + (l & 7) * 4 + kg;
        v2f xr = bf2(*lp<u32>(L_X + b * 1024 + D * 4));
        v4u sel, a[8];
#pragma unroll
        for (int q = 0; q < 4; ++q) sel[q] = ((n16 & 2) == 0 && (n16 >> 2) == q) ? ((n16 & 1) ? 0x3F800000u : 0x3F80u) : 0u;
#pragma unroll
        for (int i = 0; i < 8; ++i) a[i] = ones;
        const int avo = pin((n16 >> 3) * 4096 + kg * 128 + (l & 7) * 16);
        const int aso = WS_A + b * GA + ((HN & 2) ? tw * 16384 + SENSE * 8192 : tw * 8192);
        u32 sta = DRY ? pin(L_JUNK) : pin(l == 0 ? L_ST + tw * 8 : L_JUNK);
        int ooff = DRY ? pin(OOB) : pin(b * 1024 + D * 4);
        int eoff = DRY ? pin(OOB) : pin(b == 0 && tw == 0 && l == 0 ? 0 : OOB);
        v2f h;
        u32 ta, tb, pass = DRY ? 0 : 1;
        const __amdgpu_buffer_rsrc_t r_out = mkrsrc(out, 4096);
#define AREG(c) c "{v[128:131]}"(a[0]), c "{v[132:135]}"(a[1]), c "{v[136:139]}"(a[2]), c "{v[140:143]}"(a[3]), c "{v[144:147]}"(a[4]), c "{v[148:151]}"(a[5]), c "{v[152:155]}"(a[6]), c "{v[156:159]}"(a[7])
        asm volatile("" : "+v"(xr), "+v"(sel), AREG("+") : "s"(aso), "s"(r_out));
#pragma unroll 1
        for (;;) {
        asm volatile("" : "+s"(pass));
        if (pass) {
            if (EPE) __builtin_amdgcn_raw_buffer_store_b32(tag, r_ws, eoff, WS_EPOCH, AUX_SC1);
            wait_flag<4, 2>(F_A, 3);
            __builtin_amdgcn_s_setprio(3);
            u32 t0, t1, n = SPIN_MAX;
#define ACHK(r0, r1, r2, r3, r4, r5, r6, r7, m0, m1) \
            "v_max3_u32 %[t0], v" #r0 ", v" #r1 ", v" #r2 "\n v_max3_u32 %[t1], v" #r4 ", v" #r5 ", v" #r6 "\n" \
            "v_max_u32 %[t0], %[t0], v" #r3 "\n v_max_u32 %[t1], %[t1], v" #r7 "\n" \
            "v_cmp_eq_u32 s[" m0 "], -1, %[t0]\n v_cmp_eq_u32 s[" m1 "], -1, %[t1]\n"
#define ALD(r, m, o) "s_mov_b64 exec, s[" m "]\n buffer_load_dwordx4 v[" r "], %[vo], %[rs], %[so] offen offset:" #o " sc1\n"
            asm volatile("1:\n"
                ACHK(128, 129, 130, 131, 132, 133, 134, 135, "36:37", "38:39") ACHK(136, 137, 138, 139, 140, 141, 142, 143, "40:41", "42:43")
                ACHK(144, 145, 146, 147, 148, 149, 150, 151, "44:45", "46:47") ACHK(152, 153, 154, 155, 156, 157, 158, 159, "48:49", "50:51")
                "s_or_b64 s[52:53], s[36:37], s[38:39]\n s_or_b64 s[54:55], s[40:41], s[42:43]\n s_or_b64 s[56:57], s[44:45], s[46:47]\n"
                "s_or_b64 s[52:53], s[52:53], s[54:55]\n s_or_b64 s[54:55], s[48:49], s[50:51]\n s_or_b64 s[52:53], s[52:53], s[56:57]\n"
                "s_or_b64 s[52:53], s[52:53], s[54:55]\n"
                "s_cbranch_scc0 2f\n"
                ALD("128:131", "36:37", 0) ALD("132:135", "38:39", 512) ALD("136:139", "40:41", 1024) ALD("140:143", "42:43", 1536)
                ALD("144:147", "44:45", 2048) ALD("148:151", "46:47", 2560) ALD("152:155", "48:49", 3072) ALD("156:159", "50:51", 3584)
                "s_mov_b64 exec, -1\n"
                "s_waitcnt vmcnt(%[cap])\n"
                "s_sub_u32 %[n], %[n], 1\n"
                "s_cbranch_scc0 1b\n"
                "2:\n"
                : AREG("+"), [t0] "=&v"(t0), [t1] "=&v"(t1), [n] "+s"(n)
                : [vo] "v"(avo), [rs] "s"(r_ws), [so] "s"(aso), [cap] "i"(ACAP), [slp] "i"(ASLP)
                : "memory", "scc", "s36", "s37", "s38", "s39", "s40", "s41", "s42", "s43", "s44", "s45", "s46", "s47", "s48", "s49", "s50", "s51", "s52", "s53", "s54", "s55", "s56", "s57");
        }
        STAMP(14);
        v4f c0 = {0.f, 0.f, 0.f, 0.f}, c1 = c0;
#pragma unroll
        for (int i = 0; i < 8; i += 2) { c0 = mfma16(sel, a[i], c0); c1 = mfma16(sel, a[i + 1], c1); }
        h = bf2(pk2(xr + bf2(pkbf(c0.x + c1.x, c0.y + c1.y))));
        const float h0 = u2f(__builtin_amdgcn_readfirstlane(f2u(h.x)));
        const float d0 = h.x - h0, d1 = h.y - h0;
        const float sd = wave_sum(d0 + d1), sdd = wave_sum(__builtin_fmaf(d0, d0, d1 * d1));
        { const v2u t = {f2u(128.f * h0 + sd), f2u(sdd - sd * sd * (1.f / 128.f)) | 1u}; *lp<v2u>(sta) = t; }
        v4f s0, s1;
#pragma unroll 1
        for (int spin = 0; spin < SPIN_MAX; ++spin) {
            s0 = *lp<volatile v4f>(L_ST); s1 = *lp<volatile v4f>(L_ST + 16);
            if (uni_ge(umn(umn(f2u(s0.y), f2u(s0.w)), umn(f2u(s1.y), f2u(s1.w))), pass)) break;
        }
        asm volatile("" ::: "memory");
        const float mean = ((s0.x + s0.z) + (s1.x + s1.z)) * (1.f / 512.f);
        const float e0 = s0.x * (1.f / 128.f) - mean, e1 = s0.z * (1.f / 128.f) - mean, e2 = s1.x * (1.f / 128.f) - mean, e3 = s1.z * (1.f / 128.f) - mean;
        const float var = fmx(__builtin_fmaf(128.f, (e0 * e0 + e1 * e1) + (e2 * e2 + e3 * e3), (s0.y + s0.w) + (s1.y + s1.w)) * (1.f / 512.f), 0.f);
        const float mb = rbf(mean);
        const float rden = __builtin_amdgcn_rcpf(rbf(__builtin_amdgcn_sqrtf(rbf(rbf(var) + 1e-5f))));
        const float mean2 = rbf((mean - mb) * rden), var2 = var * rden * rden;
        const float rden2 = __builtin_amdgcn_rcpf(rbf(__builtin_amdgcn_sqrtf(rbf(rbf(var2) + 1e-5f))));
        const v2f mb2 = {mb, mb}, rd2 = {rden, rden}, mc2 = {mean2, mean2}, re2 = {rden2, rden2};
        const v2f n1 = bf2(pk2(bf2(pk2(h - mb2)) * rd2));
        const u32 tt = pk2(bf2(pk2(n1 - mc2)) * re2);
        ta = tt & 0xffffu; tb = tt >> 16;
        const v2f y = {bflo(*lp<u16>(L_TAB + 2 * ta)), bflo(*lp<u16>(L_TAB + 2 * tb))};
        __builtin_amdgcn_raw_buffer_store_b32(pk2(h + y), r_out, ooff, 0, OAUX);
        if (!EPE) __builtin_amdgcn_raw_buffer_store_b32(tag, r_ws, eoff, WS_EPOCH, AUX_SC1);
        if (pass) break;
        pass = 1;
        sta = pin(l == 0 ? L_ST + tw * 8 : L_JUNK);
        ooff = pin(b * 1024 + D * 4);
        eoff = pin(b == 0 && tw == 0 && l == 0 ? 0 : OOB);
        }
        STAMP(15);
        if (tw == 0) PUTST(14, 16);
        if (NOFB) { asm volatile("" :: AREG("")); __builtin_amdgcn_endpgm(); }
#pragma unroll
        for (int q = 0; q < 2; ++q) {
            const u32 t = q ? tb : ta;
            if ((t & 0x7fffu) - T_LO >= 2048u) {
                const u32 i = __hip_atomic_fetch_add(lp<u32>(F_FB), 1u, __ATOMIC_RELAXED, __HIP_MEMORY_SCOPE_WORKGROUP);
                const v2u it = {(t << 16) | (u32)(2 * D + q), f2u(q ? h.y : h.x)};
                *lp<v2u>(L_FB + i * 8) = it;
            }
        }
        lds_barrier();
        const int n = __builtin_amdgcn_readfirstlane(*lp<u32>(F_FB));
        if (n > 0) {
#pragma unroll 1
            for (int i = 0; i < n; ++i) {
                const float p = wave_sum(mlp_part(cf, bfhi(*lp<u32>(L_FB + i * 8))));
                *lp<float>(l == 0 ? L_FP + i * 16 + tw * 4 : L_JUNK) = p;
            }
            __builtin_amdgcn_s_waitcnt(0x0F70);
            lds_barrier();
#pragma unroll 1
            for (int i = tw * 64 + l; i < n; i += 256) {
                const v2u it = *lp<v2u>(L_FB + i * 8);
                const v4f p = *lp<v4f>(L_FP + i * 16);
                const float yv = rbf(rbf((p.x + p.y) + (p.z + p.w)) + bflo(b2r));
                __builtin_amdgcn_raw_buffer_store_b16((u16)pkbf(u2f(it.y) + yv, 0.f), r_out, (it.x & 0xffffu) * 2, b * 1024, OAUX);
            }
        }
        asm volatile("" :: AREG(""));
        __builtin_amdgcn_endpgm();
    }

    const int w = wave2;
    v4u tb;
    u32 wa[4], rdy;
    const int rvo = (l & 7) * 4, rso = WS_RD + gc * GML + w * 32;
#pragma unroll
    for (int ct = 0; ct < 4; ++ct) wa[ct] = pin((L_W + fra) ^ (ct * 32));
    {
        const u32 xb = pin(L_X + w * 1024 + l * 16);
        const u32 xa = pin(kg == b ? L_XN + w * 320 + (n16 >> 2) * 64 + (n16 & 1) * 32 + ((n16 >> 1) & 1) * 8 : L_JUNK);
        const u32 xf = pin(L_XN + (n16 >> 2) * 320 + kg * 16);
        const u32 wq0 = pin((L_W + fra) ^ (w * 32));
        const int qvo = pin((n16 & 3) == 0 && !(XP & 2) ? (n16 >> 2) * 8192 + kg * 16 : OOB);
        WAITVM(6);
        STAMP(1);
        const v4u xr = *lp<v4u>(xb);
        v2f xv[4];
#pragma unroll
        for (int q = 0; q < 4; ++q) xv[q] = bf2(xr[q]);
        const float c = u2f(__builtin_amdgcn_readfirstlane(f2u(xv[0].x)));
        const v2f c2 = {c, c};
        v2f d[4];
#pragma unroll
        for (int q = 0; q < 4; ++q) d[q] = xv[q] - c2;
        const v2f s2 = (d[0] + d[1]) + (d[2] + d[3]);
        const v2f q2 = __builtin_elementwise_fma(d[0], d[0], d[1] * d[1]) + __builtin_elementwise_fma(d[2], d[2], d[3] * d[3]);
        const float sd = wave_sum(s2.x + s2.y) * (1.f / 512.f), sq = wave_sum(q2.x + q2.y) * (1.f / 512.f);
        const float var = fmx(__builtin_fmaf(-sd, sd, sq), 0.f);
        const float mb = rbf(c + sd);
        const float rden = __builtin_amdgcn_rcpf(rbf(__builtin_amdgcn_sqrtf(rbf(rbf(var) + 1e-5f))));
        const v2f mb2 = {mb, mb}, rd2 = {rden, rden};
        v4u o;
#pragma unroll
        for (int q = 0; q < 4; ++q) o[q] = pk2(bf2(pk2(xv[q] - mb2)) * rd2);
        const v2u o0 = {o.x, o.y}, o1 = {o.z, o.w};
        *lp<v2u>(xa) = o0; *lp<v2u>(xa + 16) = o1;
        asm volatile("s_waitcnt vmcnt(0)\n\tv_add_u32 %0, %2, %1" : "=v"(tag) : "v"(tag0), "i"(TAG_STEP) : "memory");
        set_flag(F_A + w * 4, 1);
#pragma unroll
        for (int i = 0; i < 4; ++i) tb[i] = (tag >> (8 * i)) & 255u;
        v4u af[4], bf[4];
        if (PRE) wait_flag<4, PRE>(F_A, 1);
#pragma unroll 1
        for (int spin = 0; spin < SPIN_MAX; ++spin) {
            const v4u f = *lp<volatile v4u>(F_A);
            asm volatile("" ::: "memory");
#pragma unroll
            for (int t = 0; t < 4; ++t) { af[t] = *lp<v4u>(xf + t * 64); bf[t] = trfrag(wq0 + t * 4096); }
            SB();
            if (uni_ge(umn4(f), 1)) break;
        }
        v4f acc = {0.f, 0.f, 0.f, 0.f};
#pragma unroll
        for (int t = 0; t < 4; ++t) acc = mfma16(bf[t], af[t], acc);
        v4u gq;
#pragma unroll
        for (int i = 0; i < 4; ++i) gq[i] = (f2u(acc[i]) & ~255u) | tb[i];
        ST128(gq, r_ws, qvo, WS_Q + g * GQ + (s >> 1) * 2048 + b * 512 + (s & 1) * 256 + w * 64, AUX_SC1);
        set_flag(F_A + w * 4, 2);
        if (CSW && w == 3) __builtin_amdgcn_raw_buffer_store_b32(tag, r_ws, l == 0 ? 0 : OOB, WS_RD + g * GML + (b * 8 + s) * 4, AUX_SC1);
        if (ORST == 2) __builtin_amdgcn_raw_buffer_store_b32(~0u, r_ws, kg * 512 + n16 * 4, WS_O + g * GO + w * 8192 + b * 2048 + s * 64, AUX_SC1);
    }
    STAMP(2);
    {
        const int qso = WS_Q + g * GQ + b * 8192 + w * 2048;
        const u32 qw = pin(L_QI + w * 320 + (l & 31) * 8 + (l >> 5) * 4);
        {
            const u32 tq = __builtin_amdgcn_readfirstlane((u32)tstart) + TQ;
#pragma unroll 1
            for (int i = 0; i < 4096; ++i) { if ((int)(now32() - tq) >= 0) break; __builtin_amdgcn_s_sleep(1); }
        }
        STAMP(3);
        v4u p[2], c[2];
        bool bad = true;
#pragma unroll 1
        for (int spin = 0; spin < SPIN_MAX; ++spin) {
            asm volatile("" ::: "memory");
            if (bad) {
#pragma unroll
                for (int k = 0; k < 2; ++k) p[k] = LD128(r_ws, (XP & 1) ? OOB : l * 16 + k * 1024, qso, AUX_SC1);
            }
            c[0] = p[0] ^ tb; c[1] = p[1] ^ tb;
            const v4u o = c[0] | c[1];
            bad = (((o.x | o.y) | (o.z | o.w)) & 255u) != 0 && !(XP & 17);
            if (all_lanes(!bad)) break;
        }
        if (CSW) asm volatile("buffer_load_dword %0, %1, %2, %3 offen sc1" : "={v144}"(rdy) : "v"(rvo), "s"(r_ws), "s"(rso) : "memory");
        const v4f qs = __builtin_bit_cast(v4f, c[0]) + __builtin_bit_cast(v4f, c[1]);
        const v2f q0 = swap32(qs.x, qs.z), q1 = swap32(qs.y, qs.w);
        *lp<u32>(qw) = pkbf(q0.x + q0.y, q1.x + q1.y);
        set_flag(F_Q + w * 4, 1);
        if (ORST == 3) __builtin_amdgcn_raw_buffer_store_b32(~0u, r_ws, kg * 512 + n16 * 4, WS_O + g * GO + w * 8192 + b * 2048 + s * 64, AUX_SC1);
    }
    STAMP(4);
    {
        const u32 qa = pin(L_QI + (n16 >> 2) * 320 + kg * 16);
        const u32 ma = pin(n16 == 0 ? L_M + kg * 16 + w * 4 : L_JUNK), mr = pin(L_M + kg * 16);
        const u32 pw = pin(L_P + w * 512 + (n16 >> 2) * 64 + kg * 16 + (n16 & 3) * 2);
        const u32 k0 = pin(L_K + w * 8192 + n16 * 128 + ((kg ^ (n16 & 6)) * 16)), k1 = pin(k0 ^ 64);
        wait_flag<2, 1>(F_L, 4 * w + 4 + (WIN - 2) / 2);
        STAMP(24);
        v4u kf[4][4];
#pragma unroll
        for (int t = 0; t < 4; ++t)
#pragma unroll
            for (int u = 0; u < 4; ++u) kf[t][u] = *lp<v4u>(((t & 1) ? k1 : k0) + (t >> 1) * 32768 + u * 2048);
        SB();
        v4u qf[4];
        if (PRE) wait_flag<4, PRE>(F_Q, 1);
#pragma unroll 1
        for (int spin = 0; spin < SPIN_MAX; ++spin) {
            const v4u f = *lp<volatile v4u>(F_Q);
            asm volatile("" ::: "memory");
#pragma unroll
            for (int t = 0; t < 4; ++t) qf[t] = *lp<v4u>(qa + t * 64);
            SB();
            if (uni_ge(umn4(f), 1)) break;
        }
        STAMP(18);
        v4f acc[4];
#pragma unroll
        for (int u = 0; u < 4; ++u) acc[u] = (v4f){0.f, 0.f, 0.f, 0.f};
#pragma unroll
        for (int t = 0; t < 4; ++t)
#pragma unroll
            for (int u = 0; u < 4; ++u) acc[u] = mfma16(qf[t], kf[t][u], acc[u]);
        const v2f scale = {0.08838834764831845f, 0.08838834764831845f};
        const v2f s0 = bf2(pk2(bf2(pkbf(acc[0].x, acc[1].x)) * scale)), s1 = bf2(pk2(bf2(pkbf(acc[2].x, acc[3].x)) * scale));
        float m = fmx(fmx(s0.x, s0.y), fmx(s1.x, s1.y));
        m = fmx(m, dppf<DPP_X1>(m)); m = fmx(m, dppf<DPP_X2>(m)); m = fmx(m, dppf<DPP_HMIR>(m)); m = fmx(m, dppf<DPP_ROR8>(m));
        *lp<float>(ma) = m;
        set_flag(F_M + w * 4, 1);
        v4f mv;
        if (PRE) wait_flag<4, PRE>(F_M, 1);
#pragma unroll 1
        for (int spin = 0; spin < SPIN_MAX; ++spin) {
            const v4u f = *lp<volatile v4u>(F_M);
            asm volatile("" ::: "memory");
            mv = *lp<v4f>(mr);
            SB();
            if (uni_ge(umn4(f), 1)) break;
        }
        STAMP(19);
        const float mo = -fmx(fmx(mv.x, mv.y), fmx(mv.z, mv.w)) * LOG2E;
        const v2f e0 = {__builtin_amdgcn_exp2f(__builtin_fmaf(s0.x, LOG2E, mo)), __builtin_amdgcn_exp2f(__builtin_fmaf(s0.y, LOG2E, mo))};
        const v2f e1 = {__builtin_amdgcn_exp2f(__builtin_fmaf(s1.x, LOG2E, mo)), __builtin_amdgcn_exp2f(__builtin_fmaf(s1.y, LOG2E, mo))};
        const u32 p0 = pk2(e0), p1 = pk2(e1);
        *lp<u16>(pw) = (u16)p0; *lp<u16>(pw + 8) = (u16)(p0 >> 16); *lp<u16>(pw + 256) = (u16)p1; *lp<u16>(pw + 264) = (u16)(p1 >> 16);
        const v2f es = e0 + e1;
        *lp<float>(ma + (L_LS - L_M)) = row_sum(es.x + es.y);
        set_flag(F_P + w * 4, 1);
    }
    STAMP(5);
    __builtin_amdgcn_s_setprio(3);
    int aline = WS_A + w * GA + aoff;
    asm volatile("" : "+s"(aline));
    const int mvo = pin((2 * kg + (l & 1)) * 16);
    const int mso = WS_ML + gc * GML + opc + b * 512 + w * 128;
    const int ovo = pin((n16 >> 1) * 128 + kg * 32 + (n16 & 1) * 16);
    const int oso = WS_O + gc * GO + opc + b * OH + w * 2048;
    const int avs = pin((l & (AST16 ? 7 : 1)) == 0 ? l * 2 : OOB);
    u32 rd2;
    if (w == 0) {
        wait_flag<4, 0>(F_P, 1);
        const v4f m = *lp<v4f>(L_M + (l & 3) * 16), q = *lp<v4f>(L_LS + (l & 3) * 16);
        const v4u gm = {f2u(fmx(fmx(m.x, m.y), fmx(m.z, m.w))), tag, f2u((q.x + q.y) + (q.z + q.w)), tag};
        ST128(gm, r_ws, l < 4 ? l * 512 : OOB, WS_ML + g * GML + opg + b * 128 + s * 16, AUX_SC1);
        if (CSW) asm volatile("s_waitcnt vmcnt(1)\n v_xor_b32 %0, %1, %2" : "=&v"(rd2) : "{v144}"(rdy), "v"(tag));
    } else if (CSW) asm volatile("s_waitcnt vmcnt(0)\n v_xor_b32 %0, %1, %2" : "=&v"(rd2) : "{v144}"(rdy), "v"(tag));
    v4u gm, av[2], wf[4][4];
    float Wa, Wb;
    auto weights = [&]() {
        const float ms = u2f(gm.x), lv = u2f(gm.z);
        const float mm = max_x16_x32(fmx(ms, dppf<DPP_X1>(ms)));
        const float wg = __builtin_amdgcn_exp2f((ms - mm) * LOG2E);
        float L = wg * lv; L = sum_x16_x32(L + dppf<DPP_X1>(L));
        const float W = wg * __builtin_amdgcn_rcpf(L);
        Wa = dppf<0x00>(W); Wb = dppf<0x55>(W);
        asm volatile("" : "+v"(Wa), "+v"(Wb));
    };
    {
        u32 ms2 = mso;
        asm volatile("" : "+s"(ms2));
#define OREG(c) c "{v[128:131]}"(av[0]), c "{v[132:135]}"(av[1])
#define OIN OREG("")
#define OCHK "v_max3_u32 %[t0], v128, v129, v130\n v_max3_u32 %[t1], v132, v133, v134\n v_max_u32 %[t0], %[t0], v131\n v_max_u32 %[t1], %[t1], v135\n" \
             "v_cmp_eq_u32 vcc, -1, %[t0]\n v_cmp_eq_u32 %[m1], -1, %[t1]\n"
#define OL0 "buffer_load_dwordx4 v[128:131], %[vo], %[rs], %[so] offen sc1\n"
#define OL1 "buffer_load_dwordx4 v[132:135], %[vo], %[rs], %[so] offen offset:1024 sc1\n"
#define OLD  "s_mov_b64 exec, vcc\n" OL0 "s_mov_b64 exec, %[m1]\n" OL1 "s_mov_b64 exec, -1\n"
        av[0] = av[1] = (v4u){~0u, ~0u, ~0u, ~0u};
        asm volatile("" : OREG("+") : "s"(oso));
        const u32 t3 = __builtin_amdgcn_readfirstlane((u32)tstart) + T3F;
        if (CSW) {
#pragma unroll 1
            for (int spin = 0; spin < SPIN_MAX; ++spin) {
                if (all_lanes(rd2 == 0)) break;
                asm volatile("buffer_load_dword %0, %1, %2, %3 offen sc1\n s_waitcnt vmcnt(0)\n v_xor_b32 %0, %0, %4" : "=&v"(rd2) : "v"(rvo), "s"(r_ws), "s"(rso), "v"(tag) : "memory");
            }
        }
#pragma unroll 1
        for (int spin = 0; spin < SPIN_MAX; ++spin) {
            const v2u f = *lp<volatile v2u>(F_L);
            if (uni_ge(umn(f.x, f.y), 34) || (int)(now32() - t3) >= 0) break;
            __builtin_amdgcn_s_sleep(1);
        }
        unsigned long long om = ~0ull;
        asm volatile("s_memrealtime s[36:37]\n buffer_load_dwordx4 %[gm], %[mvo], %[rs], %[mso] offen sc1\n s_waitcnt lgkmcnt(0)\n s_sub_i32 s36, s36, %[t3]\n s_cmp_lt_i32 s36, 0\n"
                     "s_cselect_b64 exec, 0, %[m]\n s_cselect_b64 %[m], %[m], 0\n"
                     OL0 OL1
                     "s_mov_b64 exec, -1\n"
                     : OREG("+"), [gm] "=&v"(gm), [m] "+s"(om) : [vo] "v"(ovo), [rs] "s"(r_ws), [so] "s"(oso), [t3] "s"(t3), [mvo] "v"(mvo), [mso] "s"(ms2) : "memory", "s36", "s37", "scc");
        STAMP(6);
        u32 t0, t1;
        unsigned long long m1, m2;
        wait_flag<3, 1>(F_L, 34);
#pragma unroll
        for (int k = 0; k < 4; ++k)
#pragma unroll
            for (int ct = 0; ct < 4; ++ct) wf[k][ct] = trfrag(wa[ct] + k * 4096);
        SB();
        STAMP(11);
#pragma unroll 1
        for (int i = 0; i < 4096; ++i) { if ((int)(now32() - t3) >= 0) break; __builtin_amdgcn_s_sleep(1); }
        asm volatile("s_mov_b64 exec, %[m]\n"
                     OL0 OL1
                     "s_mov_b64 exec, -1\n s_waitcnt vmcnt(4)"
                     : OREG("+"), "+v"(gm) : [vo] "v"(ovo), [rs] "s"(r_ws), [so] "s"(oso), [m] "s"(om) : "memory");
#pragma unroll 1
        for (int spin = 0; spin < SPIN_MAX; ++spin) {
            if (all_lanes((gm.y == tag) & (gm.w == tag))) break;
            asm volatile("buffer_load_dwordx4 %0, %1, %2, %3 offen sc1\n\ts_waitcnt vmcnt(0)" : "=v"(gm) : "v"(mvo), "s"(r_ws), "s"(ms2) : "memory");
        }
        weights();
        u32 n = SPIN_MAX;
        asm volatile("s_waitcnt vmcnt(0)\n1:\n" OCHK
                     "s_or_b64 %[m2], vcc, %[m1]\n"
                     "s_cbranch_scc0 2f\n"
                     OLD
                     "s_waitcnt vmcnt(%[cap])\n"
                     "s_sub_u32 %[n], %[n], 1\n"
                     "s_cbranch_scc0 1b\n"
                     "2:\n"
                     : OREG("+"), [t0] "=&v"(t0), [t1] "=&v"(t1), [m1] "=&s"(m1), [m2] "=&s"(m2), [n] "+s"(n)
                     : [vo] "v"(ovo), [rs] "s"(r_ws), [so] "s"(oso), [cap] "i"(OCAP), [slp] "i"(OSLP) : "memory", "vcc", "scc");
    }
    {
        STAMP(12);
#define MF(d, a, f, c) "v_mfma_f32_16x16x32_bf16 v[" d "], v[" a "], %[" f "], " c "\n"
#define ME(d, ct) MF(d, "128:131", "a" #ct, "0") MF(d, "132:135", "c" #ct, "v[" d "]")
        asm volatile(
            ME("136:139", 0) MF("152:155", "128:131", "b0", "0") MF("152:155", "132:135", "d0", "v[152:155]")
            ME("140:143", 1) MF("156:159", "128:131", "b1", "0") MF("156:159", "132:135", "d1", "v[156:159]")
            ME("144:147", 2)
            "v_mul_f32 v137, %[wa], v136\n v_fmac_f32 v137, %[wb], v138\n"
            MF("160:163", "128:131", "b2", "0")
            "v_fmac_f32 v137, %[wa], v153\n v_fmac_f32 v137, %[wb], v155\n"
            MF("160:163", "132:135", "d2", "v[160:163]")
            "v_mul_f32 v141, %[wa], v140\n v_fmac_f32 v141, %[wb], v142\n"
            MF("148:151", "128:131", "a3", "0")
            "v_fmac_f32 v141, %[wa], v157\n v_fmac_f32 v141, %[wb], v159\n"
            MF("148:151", "132:135", "c3", "v[148:151]")
            "v_mul_f32 v145, %[wa], v144\n v_fmac_f32 v145, %[wb], v146\n"
            MF("164:167", "128:131", "b3", "0")
            "v_permlane16_swap_b32 v137, v141\n v_add_f32 v137, v137, v141\n"
            MF("164:167", "132:135", "d3", "v[164:167]")
            "v_fmac_f32 v145, %[wa], v161\n v_fmac_f32 v145, %[wb], v163\n"
            "v_mul_f32 v149, %[wa], v148\n v_fmac_f32 v149, %[wb], v150\n"
            "v_mov_b32 v136, -2\n"
            "s_nop 2\n"
            "v_fmac_f32 v149, %[wa], v165\n v_fmac_f32 v149, %[wb], v167\n"
            "s_nop 1\n"
            "v_permlane16_swap_b32 v145, v149\n v_add_f32 v145, v145, v149\n"
            "s_nop 1\n"
            "v_permlane32_swap_b32 v137, v145\n v_add_f32 v137, v137, v145\n"
            "s_nop 1\n"
            "v_mov_b32_dpp v138, v137 quad_perm:[1,0,3,2] row_mask:0xf bank_mask:0xf bound_ctrl:1\n"
            "v_cvt_pk_bf16_f32 v137, v137, v138\n"
            "v_min_u32 v140, v137, v136\n"
            "s_nop 0\n"
            "v_min_u32_dpp v141, v137, v136 row_shl:2 row_mask:0xf bank_mask:0xf bound_ctrl:1\n"
            "v_min_u32_dpp v142, v137, v136 row_shl:4 row_mask:0xf bank_mask:0xf bound_ctrl:1\n"
            "v_min_u32_dpp v143, v137, v136 row_shl:6 row_mask:0xf bank_mask:0xf bound_ctrl:1\n"
            "buffer_store_dwordx4 v[140:143], %[vo], %[rs], %[so] offen sc1\n"
            "s_nop 1\n"
            :: OIN, [a0] "v"(wf[0][0]), [a1] "v"(wf[0][1]), [a2] "v"(wf[0][2]), [a3] "v"(wf[0][3]), [b0] "v"(wf[1][0]), [b1] "v"(wf[1][1]), [b2] "v"(wf[1][2]), [b3] "v"(wf[1][3]),
               [c0] "v"(wf[2][0]), [c1] "v"(wf[2][1]), [c2] "v"(wf[2][2]), [c3] "v"(wf[2][3]), [d0] "v"(wf[3][0]), [d1] "v"(wf[3][1]), [d2] "v"(wf[3][2]), [d3] "v"(wf[3][3]),
               [wa] "v"(Wa), [wb] "v"(Wb), [vo] "v"(avs), [rs] "s"(r_ws), [so] "s"(aline)
            : "memory", "v136", "v137", "v138", "v139", "v140", "v141", "v142", "v143", "v144", "v145", "v146", "v147", "v148", "v149", "v150", "v151",
              "v152", "v153", "v154", "v155", "v156", "v157", "v158", "v159", "v160", "v161", "v162", "v163", "v164", "v165", "v166", "v167");
    }
    asm volatile("" :: OIN);
    set_flag(F_A + w * 4, 3);
    STAMP(13);
}

extern "C" void kernel_launch(void* const* d_in, const int* in_sizes, int n_in, void* d_out, int out_size, void* d_ws, size_t ws_size, hipStream_t stream) {
    constexpr u32 F = 0x00020000;
    fused_decode_kernel<<<256, 640, 0, stream>>>((const u16*)d_in[0], 4096u, F, (const u16*)d_in[3], 4u << 20, F, (const u16*)d_in[1], 64u << 20, F,
                                                 (const u16*)d_in[2], 64u << 20, F, (unsigned char*)d_ws, WS_END, F, (const u16*)d_in[6], 3584u, F,
                                                 (const u16*)d_in[7], 3584u, F, (const u16*)d_in[8], 3584u, F, (const u16*)d_in[9], 2u, F,
                                                 (const u16*)d_in[4], (u16*)d_out);
}
```

```cpp
#define TQ 210
#define PD 260
#define PC 600
#define WV 2
#define TG 2
#define FSL 0
#define ACAP 8
#define FPM 7
#define HN 0
#define TMX 1
#define T3F 650
#define CODEB 13312
#define T4F 723
#define WT 563
#define WD 14
#include <hip/hip_runtime.h>
#include <stdint.h>

typedef unsigned u32;
typedef unsigned short u16;
typedef u32 v4u __attribute__((ext_vector_type(4)));
typedef u32 v2u __attribute__((ext_vector_type(2)));
typedef u32 v8u __attribute__((ext_vector_type(8)));
typedef float v4f __attribute__((ext_vector_type(4)));
typedef float v2f __attribute__((ext_vector_type(2)));
typedef __bf16 v8bf __attribute__((ext_vector_type(8)));
typedef __bf16 v2bf __attribute__((ext_vector_type(2)));
typedef short v4s __attribute__((ext_vector_type(4)));
#define LAS __attribute__((address_space(3)))

#define STAMP(i) do { } while (0)
#define PUTST(a, b) do { } while (0)

#define WST(i)
#define MAPG 0
#define WIN 20
#define QP 13
#define WOP 32
#define XP 0
#define PRE 0
#define TN 18
#define TWAIT 0
#define WOE 4
#define GDELAY 10
#define KFIRST 0
#define OST16 0
#define AST16 1
#define ODELAY 0
#define ADELAY 0
#define ROTA 6
#define ROTB 3
#define ROTC 0
#define SPREAD 1
#define WSOFF 0
#define ILV 0
#define FINM 0
#define FINC 0
#define JUNK 15
#define T1F 480
#define WOST 5
#define OSLP 0
#define OCAP 0
#define OSTEP2 0
#define ASLP 0
#define T2F 0
#define LP5 1
#define ORST 0
#define ARST 0
#define DRY 0
#define CSW 1
#define OSEG 128
#define WOT 63
#define NUMA 0
#define SENSE 0
#define EPE 0
#define OAUX 16
#define NOFB 0
#define PDF 224
#define DMAX 80
#define TD 140
#define TM 30
#define WOF 0
#define W34 1
#define PCS 1
#define PSL 1
#define PRG 0
#define EG 0
#define ALN 0
#define FM 0
#define FBT 0
#define PD2 0
#define PS 15
#define WSB 0
#define TSW 0
#define GSL 0
#define TGL 31
#define NBW 0
#define F6W 8
#define WE 0
#define W0T 300
#define W0D 60
#define WPR 3
#define WS 6
#define FAW 0
#define FDEP 15
#define FST 0
#define TWL 1
#define FDL 0
#define TDL 4
#define H2 0
#define V5FMT 4

constexpr int AUX_SC1 = 16;
constexpr u32 WS_EPOCH = 0;
constexpr u32 GQ = 262144, GO = 262144, GML = 262144, GA = 524288;
constexpr u32 WS_Q = 4096;
constexpr u32 WS_O = 65536;
constexpr u32 WS_ML = 131072;
constexpr u32 WS_RD = 131072 + 2048;
constexpr u32 WS_A = 163840;
constexpr u32 WS_T = 262144 + 163840;
constexpr u32 WS_T2 = WS_T + 16384;
constexpr u32 WS_TS = 262144 + 196608;
constexpr u32 WS_END = 2097152;
constexpr u32 L_V = 0, L_K = 0x8000, L_H = 0x10000, L_W = 0x20000, L_TP = 0x24080, L_X = 0x25000, L_XN = 0x26000, L_QI = 0x26500, L_P = 0x26A00,
              L_JUNK = 0x27600, L_FL = 0x27A00, L_M = 0x27A80, L_LS = 0x27AC0, L_SP = 0x27B00, L_END = 0x27E00;
constexpr u32 F_A = L_FL, F_Q = L_FL + 16, F_M = L_FL + 32, F_P = L_FL + 48, F_L = L_FL + 64, F_ARR = L_FL + 72, F_FB = L_FL + 76, F_DL = L_FL + 80, F_W = L_FL + 88, L_ST = L_FL + 96;
constexpr u32 TAG_STEP = 0x01010101u;
constexpr u32 L_TAB = 0x2000;
constexpr u32 L_FB = L_K + 0x3000, L_FP = L_K + 0x4000;
constexpr u32 T_LO = (130 - TN) * 128;
constexpr int SPIN_MAX = 1 << 18;
constexpr u32 CODE_BYTES = CODEB;
constexpr int OOB = 0x40000000;
constexpr float LOG2E = 1.4426950408889634f;

__device__ __forceinline__ float u2f(u32 v) { return __builtin_bit_cast(float, v); }
__device__ __forceinline__ u32 f2u(float v) { return __builtin_bit_cast(u32, v); }
__device__ __forceinline__ float bflo(u32 v) { return u2f(v << 16); }
__device__ __forceinline__ float bfhi(u32 v) { return u2f(v & 0xffff0000u); }
__device__ __forceinline__ v2f bf2(u32 v) { v2f r = {bflo(v), bfhi(v)}; return r; }
__device__ __forceinline__ u32 pk2(v2f f) { return __builtin_bit_cast(u32, __builtin_convertvector(f, v2bf)); }
__device__ __forceinline__ u32 pkbf(float a, float b) { v2f f = {a, b}; return pk2(f); }
__device__ __forceinline__ float rbf(float a) { return u2f(pkbf(0.f, a)); }
template <int CTRL> __device__ __forceinline__ float dppf(float v) { return u2f((u32)__builtin_amdgcn_update_dpp(0, (int)f2u(v), CTRL, 0xf, 0xf, true)); }
template <int CTRL> __device__ __forceinline__ u32 dppu(u32 v) { return (u32)__builtin_amdgcn_update_dpp(0, (int)v, CTRL, 0xf, 0xf, true); }
constexpr int DPP_X1 = 0xB1, DPP_X2 = 0x4E, DPP_HMIR = 0x141, DPP_ROR4 = 0x124, DPP_ROR8 = 0x128, DPP_SHL4 = 0x104, DPP_SHL8 = 0x108;
__device__ __forceinline__ float fmx(float a, float b) { return __builtin_fmaxf(a, b); }
__device__ __forceinline__ u32 umn(u32 a, u32 b) { return a < b ? a : b; }
__device__ __forceinline__ u32 umx(u32 a, u32 b) { return a > b ? a : b; }
__device__ __forceinline__ u32 umx4(v4u a) { return umx(umx(a.x, a.y), umx(a.z, a.w)); }
__device__ __forceinline__ v2f swap32(float a, float b) {
    auto r = __builtin_amdgcn_permlane32_swap(f2u(a), f2u(b), false, false);
    v2f o = {u2f(r[0]), u2f(r[1])}; return o;
}
__device__ __forceinline__ v2f swap16(float a, float b) {
    auto r = __builtin_amdgcn_permlane16_swap(f2u(a), f2u(b), false, false);
    v2f o = {u2f(r[0]), u2f(r[1])}; return o;
}
__device__ __forceinline__ float sum_x16_x32(float v) { v2f a = swap16(v, v); v = a.x + a.y; a = swap32(v, v); return a.x + a.y; }
__device__ __forceinline__ float max_x16_x32(float v) { v2f a = swap16(v, v); v = fmx(a.x, a.y); a = swap32(v, v); return fmx(a.x, a.y); }
__device__ __forceinline__ float row_sum(float v) { v += dppf<DPP_X1>(v); v += dppf<DPP_X2>(v); v += dppf<DPP_HMIR>(v); v += dppf<DPP_ROR8>(v); return v; }
__device__ __forceinline__ float wave_sum(float v) { return sum_x16_x32(row_sum(v)); }
__device__ __forceinline__ v4f mfma16(v4u a, v4u b, v4f c) {
    return __builtin_amdgcn_mfma_f32_16x16x32_bf16(__builtin_bit_cast(v8bf, a), __builtin_bit_cast(v8bf, b), c, 0, 0, 0);
}
__device__ __forceinline__ void lds_barrier() { asm volatile("s_waitcnt lgkmcnt(0)\n\ts_barrier" ::: "memory"); }
#define SB() __builtin_amdgcn_sched_barrier(0)
#define WAITVM(n) asm volatile("s_waitcnt vmcnt(%0)" :: "i"(n) : "memory")
__device__ __forceinline__ __amdgpu_buffer_rsrc_t mkrsrc(const void* p, u32 bytes) { return __builtin_amdgcn_make_buffer_rsrc((void*)p, 0, bytes, 0x00020000); }
__device__ __forceinline__ bool all_lanes(bool p) { return __builtin_amdgcn_ballot_w64(!p) == 0ull; }
__device__ __forceinline__ void junk_load(__amdgpu_buffer_rsrc_t rs, int vo, u32 lds) {
    u32 keep;
    asm volatile("s_mov_b32 %0, m0\n\ts_mov_b32 m0, %3\n\ts_nop 0\n\tbuffer_load_ushort %1, %2, 0 offen lds\n\ts_mov_b32 m0, %0" : "=&s"(keep) : "v"(vo), "s"(rs), "s"(lds) : "memory");
}
#define LD128(r, vo, so, aux) __builtin_amdgcn_raw_buffer_load_b128(r, vo, so, aux)
#define ST128(d, r, vo, so, aux) do { v4u d_ = (d); asm volatile("" : "+v"(d_)); __builtin_amdgcn_raw_buffer_store_b128(d_, r, vo, so, aux); asm volatile("s_nop 1" :: "v"(d_)); } while (0)
template <typename T> __device__ __forceinline__ LAS T* lp(u32 a) { return (LAS T*)(size_t)a; }
__device__ __forceinline__ u32 pin(u32 v) { asm volatile("" : "+v"(v)); return v; }
__device__ __forceinline__ v4u trfrag(u32 a) {
    const v2u lo = __builtin_bit_cast(v2u, __builtin_amdgcn_ds_read_tr16_b64_v4i16(lp<v4s>(a)));
    const v2u hi = __builtin_bit_cast(v2u, __builtin_amdgcn_ds_read_tr16_b64_v4i16(lp<v4s>(a + 2048)));
    v4u r = {lo.x, lo.y, hi.x, hi.y}; return r;
}
__device__ __forceinline__ void set_flag(u32 a, u32 v) { asm volatile("" ::: "memory"); *lp<volatile u32>(a) = v; }
__device__ __forceinline__ u32 umn4(v4u a) { return umn(umn(a.x, a.y), umn(a.z, a.w)); }
__device__ __forceinline__ bool uni_ge(u32 x, u32 v) { return (u32)__builtin_amdgcn_readfirstlane(x) >= v; }
template <int N, int SLP> __device__ __forceinline__ void wait_flag(u32 a, u32 v) {
#pragma unroll 1
    for (int i = 0; i < SPIN_MAX; ++i) {
        u32 m;
        if (N == 1) m = *lp<volatile u32>(a);
        else if (N == 2) { const v2u f = *lp<volatile v2u>(a); m = umn(f.x, f.y); }
        else if (N == 3) { const v4u f = *lp<volatile v4u>(a); m = umn(umn(f.x, f.y), f.z); }
        else m = umn4(*lp<volatile v4u>(a));
        if (uni_ge(m, v)) break;
        if (SLP) __builtin_amdgcn_s_sleep(SLP);
    }
    asm volatile("" ::: "memory");
}
__device__ __forceinline__ u32 now32() { unsigned long long t; asm volatile("s_memrealtime %0\n\ts_waitcnt lgkmcnt(0)" : "=s"(t) :: "memory"); return (u32)t; }
struct Coef { v2f a3[3], a2[3], a1[3], a0[3], g1[3], g0[3], c32, c10, cg; };
__device__ __forceinline__ v2f fma2(v2f a, v2f b, v2f c) { return __builtin_elementwise_fma(a, b, c); }
__device__ __forceinline__ float mlp_part(const Coef& c, float t) {
    const v2f t2 = {t, t}, one = {1.f, 1.f};
    const float a6 = __builtin_fmaf(__builtin_fmaf(__builtin_fmaf(c.c32.x, t, c.c32.y), t, c.c10.x), t, c.c10.y);
    v2f acc = {__builtin_fmaf(c.cg.x, t, c.cg.y) * __builtin_amdgcn_rcpf(__builtin_amdgcn_exp2f(a6) + 1.f), 0.f};
#pragma unroll
    for (int i = 0; i < 3; ++i) {
        const v2f a = fma2(fma2(fma2(c.a3[i], t2, c.a2[i]), t2, c.a1[i]), t2, c.a0[i]);
        v2f e = {__builtin_amdgcn_exp2f(a.x), __builtin_amdgcn_exp2f(a.y)};
        e += one;
        const v2f rc = {__builtin_amdgcn_rcpf(e.x), __builtin_amdgcn_rcpf(e.y)};
        acc = fma2(fma2(c.g1[i], t2, c.g0[i]), rc, acc);
    }
    return acc.x + acc.y;
}
__device__ __forceinline__ void make_coef(Coef& cf, v4u ta, v4u tb, v4u tc) {
    const float K0 = -2.0f * LOG2E * 0.7978845608028654f, K1 = K0 * 0.044715f;
    const v2f k0 = {K0, K0}, k1 = {K1, K1}, k3 = {3.f * K1, 3.f * K1}, k31 = {3.f * K1, K1}, k13 = {K1, 3.f * K1};
#pragma unroll
    for (int i = 0; i < 3; ++i) {
        const v2f w = bf2(ta[i]), b = bf2(tb[i]), c = bf2(tc[i]), ww = w * w, bb = b * b;
        cf.a3[i] = ww * (w * k1); cf.a2[i] = ww * (b * k3); cf.a1[i] = w * fma2(bb, k3, k0); cf.a0[i] = b * fma2(bb, k1, k0);
        cf.g1[i] = w * c; cf.g0[i] = b * c;
    }
    const v2f wb = {bflo(ta[3]), bflo(tb[3])}, sq = wb * wb, ww = {sq.x, sq.x}, bb = {sq.y, sq.y}, c = {bflo(tc[3]), bflo(tc[3])};
    cf.c32 = ww * (wb * k13); cf.c10 = wb * fma2(bb, k31, k0); cf.cg = wb * c;
}

#define DESC(T, p) T p, u32, u32
__global__ void __launch_bounds__(640)
fused_decode_kernel(DESC(const u16*, x), DESC(const u16*, wq), DESC(const u16*, kc), DESC(const u16*, vc), DESC(unsigned char*, ws),
                    DESC(const u16*, w1), DESC(const u16*, b1), DESC(const u16*, w2), DESC(const u16*, b2), const u16* wo, u16* out) {
    __shared__ __attribute__((aligned(16))) unsigned char smem[L_END];
    STAMP(0);
    u32 tag0, tag, b2r;
    v4u ca, cb, cc;
    int wave2;
    unsigned long long tstart, dbgk;
    const int bid = blockIdx.x;
    const int pid = MAPG ? (bid & 192) | ((bid & 7) << 3) | ((bid >> 3) & 7) : bid;
    {
        u32 t0, t1, t2, t3;
#define WQL "s_add_u32 m0, m0, 0x400\n s_add_u32 s73, s73, 0x10000\n buffer_load_dwordx4 %[t3], s[40:43], s73 offen nt lds\n"
#define KVL "buffer_load_dwordx4 %[t3], s[44:47], s73 offen nt lds\n s_add_u32 m0, m0, 0x400\n s_add_u32 s73, s73, 0x800\n"
        asm volatile(
            "s_load_dwordx16 s[36:51], %[ka], 0x0\n"
            "s_load_dwordx16 s[52:67], %[ka], 0x40\n"
            "s_getpc_b64 s[76:77]\n"
            "8:\n"
            "v_readfirstlane_b32 %[wv], %[tid]\n"
            "v_bfe_u32 %[t0], %[tid], 3, 3\n"
            "v_and_b32 %[t1], 6, %[t0]\n"
            "v_xor_b32 %[t1], %[t1], %[tid]\n"
            "v_and_b32 %[t1], 7, %[t1]\n"
            "v_lshlrev_b32 %[t1], 4, %[t1]\n"
            "s_lshr_b32 %[wv], %[wv], 6\n"
            "s_and_b32 s72, %[pid], 7\n"
            "s_cmp_gt_u32 %[wv], 3\n"
            "s_cbranch_scc1 2f\n"
            "s_setprio 2\n"
            "v_lshlrev_b32 %[t2], 4, %[tid]\n"
            "v_lshl_or_b32 %[t3], %[t0], 13, %[t1]\n"
            "s_lshr_b32 s75, %[pid], 6\n"
            "s_lshl_b32 s73, s75, 2\n"
            "s_add_u32 s73, s73, %[wv]\n"
            "s_lshl_b32 s73, s73, 11\n"
            "s_and_b32 s74, %[pid], 63\n"
            "s_or_b32 s73, s73, s74\n"
            "s_lshl_b32 s73, s73, 7\n"
            "s_lshl_b32 s74, %[wv], 10\n"
            "s_add_u32 m0, s74, %[lx]\n"
            "s_lshl_b32 s74, s74, 2\n"
            "s_add_u32 s74, s74, %[lw]\n"
            "v_bfe_u32 %[t0], %[tid], 5, 1\n"
            "v_and_b32 %[t1], 31, %[tid]\n"
            "v_lshlrev_b32 %[t1], 2, %[t1]\n"
            "v_lshl_or_b32 %[t0], %[t0], 10, %[t1]\n"
            "s_and_b32 s78, %[pid], 56\n"
            "s_lshl_b32 s78, s78, 3\n"
            "s_lshl1_add_u32 s78, %[wv], s78\n"
            "s_lshl1_add_u32 s78, s78, s75\n"
            "s_lshl_b32 s78, s78, 4\n"
            "s_or_b32 s78, s78, s72\n"
            "s_lshl_b32 s78, s78, 7\n"
            "s_add_u32 s78, s78, %[wso]\n"
            "v_mov_b32 %[t1], -1\n"
            "s_waitcnt lgkmcnt(0)\n"
            "buffer_load_dwordx4 %[t2], s[36:39], 0 offen lds\n"
            "s_mov_b32 m0, s74\n"
            "s_nop 0\n"
            "buffer_load_dwordx4 %[t3], s[40:43], s73 offen nt lds\n"
            WQL WQL WQL
            "buffer_load_dword %[tag], off, s[52:55], 0\n"
#if !(XP & 72) && ORST < 2
            "buffer_store_dword %[t1], %[t0], s[52:55], s78 offen sc1\n"
#else
            "buffer_load_dword %[tag], off, s[52:55], 0\n"
#endif
            "s_barrier\n"
            "s_memrealtime %[ts]\n"
            "s_waitcnt lgkmcnt(0)\n"
            "s_branch 9f\n"
            "2:\n"
            "s_cmp_gt_u32 %[wv], 5\n"
            "s_cbranch_scc1 5f\n"
            "s_setprio 3\n"
            "s_sub_u32 s75, %[wv], 4\n"
            "v_lshl_or_b32 %[t3], %[t0], 8, %[t1]\n"
            "s_lshr_b32 s74, %[pid], 3\n"
            "s_lshr_b32 s73, %[pid], 6\n"
            "s_mul_i32 s73, s73, %[rotc]\n"
            "s_mul_i32 s78, s74, %[rotb]\n"
            "s_add_u32 s73, s73, s78\n"
            "s_mul_i32 s78, s72, %[rota]\n"
            "s_add_u32 s73, s73, s78\n"
            "s_lshl_b32 s73, s73, 5\n"
            "s_or_b32 s73, s73, s75\n"
            "s_lshl_b32 s73, s73, 7\n"
            "s_lshl_b32 s74, s74, 5\n"
            "s_or_b32 s74, s74, s72\n"
            "s_or_b32 s74, s74, 0x18\n"
            "s_pack_ll_b32_b16 s73, s73, s74\n"
            "s_lshl_b32 s78, s75, 16\n"
            "s_add_u32 m0, s78, %[lk]\n"
            "s_lshl2_add_u32 s75, s75, %[fl]\n"
            "v_mov_b32 %[t0], s75\n"
            "s_mov_b32 s75, 0\n"
            "s_waitcnt lgkmcnt(0)\n"
            "s_barrier\n"
            "s_memrealtime %[ts]\n"
            KVL KVL
            "s_waitcnt lgkmcnt(0)\n"
            "s_mov_b64 s[70:71], %[ts]\n"
            "s_lshl_b32 s79, s70, 4\n"
            "s_add_u32 s79, s79, %[pc]\n"
            "3:\n"
            "s_pack_ll_b32_b16 s73, s73, s74\n"
            "s_add_u32 s75, s75, 1\n"
            "s_cmp_eq_u32 s75, 16\n"
            "s_cselect_b64 s[44:45], s[48:49], s[44:45]\n"
            "s_cselect_b32 m0, s78, m0\n"
            "v_mov_b32 %[t1], s75\n"
            "s_waitcnt vmcnt(%[win])\n"
            "ds_write_b32 %[t0], %[t1]\n"
            "4:\n"
            "s_memrealtime s[70:71]\n"
            "s_waitcnt lgkmcnt(0)\n"
            "s_lshl_b32 s72, s70, 4\n"
            "s_sub_i32 s72, s72, s79\n"
            "s_cmp_lt_i32 s72, 0\n"
            "s_cbranch_scc1 4b\n"
            KVL KVL
            "s_add_u32 s79, s79, %[pd]\n"
            "s_cmp_lg_u32 s75, 31\n"
            "s_cbranch_scc1 3b\n"
            "s_branch 9f\n"
            "5:\n"
            "s_load_dwordx4 s[68:71], %[ka], 0x80\n"
            "s_sub_u32 s75, %[wv], 6\n"
            "s_and_b32 s74, %[bid], 56\n"
            "s_lshl1_add_u32 s74, s75, s74\n"
            "s_lshl_b32 s74, s74, 8\n"
            "s_add_u32 s78, s74, 512\n"
            "s_min_u32 s78, s78, %[code]\n"
            "s_mov_b32 s79, 0x20000\n"
            "s_and_b32 s76, s76, 0xffffff00\n"
            "v_and_b32 %[t2], 63, %[tid]\n"
            "v_lshl_add_u32 %[t3], %[t2], 7, s74\n"
#if !(XP & 32)
            "buffer_load_dword %[b2r], %[t3], s[76:79], 0 offen\n"
#endif
            "v_lshlrev_b32 %[t0], 2, %[tid]\n"
            "v_add_u32 %[t0], %[lfl], %[t0]\n"
            "v_mov_b32 %[t1], 0\n"
            "ds_write_b32 %[t0], %[t1]\n"
            "s_mulk_i32 s75, 896\n"
            "v_mul_u32_u24 %[t3], 12, %[t2]\n"
            "v_lshlrev_b32 %[t2], 1, %[t2]\n"
            "s_waitcnt lgkmcnt(0)\n"
            "s_barrier\n"
            "s_memrealtime %[ts]\n"
            "buffer_load_dwordx3 v[100:102], %[t3], s[56:59], s75 offen\n"
            "buffer_load_dwordx3 v[104:106], %[t3], s[60:63], s75 offen\n"
            "buffer_load_dwordx3 v[108:110], %[t3], s[64:67], s75 offen\n"
            "buffer_load_ushort v103, %[t2], s[56:59], s75 offen offset:768\n"
            "buffer_load_ushort v107, %[t2], s[60:63], s75 offen offset:768\n"
            "buffer_load_ushort v111, %[t2], s[64:67], s75 offen offset:768\n"
            "buffer_load_dword %[tag], off, s[52:55], 0\n"
            "buffer_load_ushort %[b2r], off, s[68:71], 0\n"
            "s_waitcnt lgkmcnt(0)\n"
            "9:\n"
            : [wv] "=&s"(wave2), [ts] "=&s"(tstart), [tag] "=&v"(tag0), [b2r] "=&v"(b2r), "=&{v[100:103]}"(ca), "=&{v[104:107]}"(cb), "=&{v[108:111]}"(cc),
              [t0] "=&v"(t0), [t1] "=&v"(t1), [t2] "=&v"(t2), [t3] "=&v"(t3)
            : [ka] "s"(__builtin_amdgcn_kernarg_segment_ptr()), [pid] "s"(pid), [bid] "s"(bid), [tid] "v"(threadIdx.x), "s"((u32)(size_t)(LAS void*)smem),
              [lx] "i"(L_X), [lw] "i"(L_W), [lk] "i"(L_K), [fl] "i"(F_L), [lfl] "i"(L_FL - 384 * 4), [wso] "i"(WS_O), [code] "i"(CODE_BYTES), [win] "i"(WIN - 2), [nbw] "i"(NBW), [rota] "i"(ROTA), [rotb] "i"(ROTB), [rotc] "i"(ROTC), [pd] "i"(PD), [pdf] "i"(PDF), [pd2] "i"(PD2), [ps] "i"(PS), [pc] "i"(PC), [sense] "i"(SENSE)
            : "memory", "scc", "vcc", "s36", "s37", "s38", "s39", "s40", "s41", "s42", "s43", "s44", "s45", "s46", "s47", "s48", "s49", "s50", "s51", "s52", "s53", "s54", "s55", "s56", "s57", "s58", "s59", "s60", "s61", "s62", "s63", "s64", "s65", "s66", "s67", "s68", "s69", "s70", "s71", "s72", "s73", "s74", "s75", "s76", "s77", "s78", "s79", "s80", "s81", "s82", "s83", "s84", "s85");
        static_assert(!NUMA && F_DL == F_L + 16 && L_V == 0 && OOB == 0x40000000 && F_ARR == F_L + 8, "");
    }
    SB();
    const int l = threadIdx.x & 63;
    const int s = pid & 7, g = (pid >> 3) & 7, b = pid >> 6;
    const int gc = CSW ? s : g, sc = CSW ? g : s;
    const int n16 = l & 15, kg = l >> 4, lr = l >> 3;
    const int swz = ((l & 7) ^ (lr & 6)) * 16;
    const __amdgpu_buffer_rsrc_t r_ws = mkrsrc(ws, WS_END);
    const u32 fra = (kg * 4 + (n16 >> 2)) * 128 + (((kg & 1) * 2 + (n16 >> 3)) * 32) + (n16 & 3) * 8;
    const bool fin = (pid & 63) == FINM * b + FINC;
    constexpr int OH = (HN & 1) ? 16384 : 8192;
    const int opg = (HN & 1) ? (((g >> 2) ^ SENSE) & 1) * 8192 : 0, opc = (HN & 1) ? (((gc >> 2) ^ SENSE) & 1) * 8192 : 0;
    const int aoff = (HN & 2) ? (sc >> 1) * 16384 + SENSE * 8192 + (sc & 1) * 4096 + (gc * 4 + b) * 128 : (sc * 32 + gc * 4 + b) * 128;

    if (wave2 >= 4 && wave2 < 6) {
        STAMP(7);
        const int dh = wave2 - 4;
        const u32 lv = L_V + dh * L_H, fl = F_L + dh * 4;
        const u32 pa = pin(L_P + kg * 64 + (n16 >> 2) * 16);
        u32 va[4];
#pragma unroll
        for (int t = 0; t < 4; ++t) va[t] = pin((lv + fra) ^ (t * 32));
        const int ovo = pin(kg * OH + ((WV & 2) ? (n16 & 3) * 32 + ((n16 >> 2) & 1) * 16 + (n16 >> 3) * 8 : n16 * 8));
        int oso = WS_O + g * GO + opg + b * 2048 + dh * 1024 + s * OSEG;
        asm volatile("" : "+s"(oso));
        static_assert(16 + (WIN - 2) / 2 <= 31 && WIN % 2 == 0, "");
        const __amdgpu_buffer_rsrc_t r_wo = mkrsrc(wo, 4u << 20);
        const int wvo = pin(((WV & 2) ? 16 * (lr & 1) + 2 * (lr & 6) : 16 * (lr & 1) + (lr & 2) + (lr >> 2)) * 1024 + (H2 ? (swz & 64) * 2048 + (swz & 48) : swz));
        u32 wso = (H2 == 2 ? (gc >> 1) * 8 + b * 2 : H2 == 3 ? (gc & 3) * 8 + b * 2 : gc * 4 + (H2 ? b & 2 : b)) * (128 * 1024) + dh * ((WV & 1) ? 65536 : (WV & 2) ? 2048 : 8192) + sc * 128 + (H2 == 2 ? (gc & 1) * 64 : H2 == 3 ? (gc >> 2) * 64 : H2 ? (b & 1) * 64 : 0);
#define WNT " nt"
#define WOL "buffer_load_dwordx4 %1, %2, %0 offen" WNT " lds\n\t"
#define WON(m, d) "s_add_u32 m0, m0, " #m "\n\ts_add_i32 %0, %0, " #d "\n\t"
#define WOISSUE() asm volatile("s_mov_b32 m0, %3\n\ts_nop 0\n\t" \
                         WOSEQ \
                         : "+s"(wso) : "v"(wvo), "s"(r_wo), "s"(L_W + dh * ((WV & 1) ? 8192 : 4096)) : "memory", "scc")
#define WOSEQ WOL WON(1024, 1024) WOL WON(1024, 31744) WOL WON(1024, 1024) WOL WON(5120, 31744) WOL WON(1024, 1024) WOL WON(1024, 31744) WOL WON(1024, 1024) WOL
        const u32 flv = pin(fl), c31 = pin(31);
        v4u vf[8][4], pf[8];
        v4f acc[4];
#pragma unroll
        for (int t = 0; t < 4; ++t) acc[t] = (v4f){0.f, 0.f, 0.f, 0.f};
#define rd(st) do { SB(); _Pragma("unroll") for (int t = 0; t < 4; ++t) vf[st][t] = trfrag(va[t] + st * 4096); SB(); } while (0)
#define mm(st) do { _Pragma("unroll") for (int t = 0; t < 4; ++t) acc[t] = mfma16(pf[st], vf[st][t], acc[t]); } while (0)
        static_assert(WIN == 20, "");
        {
            const v4u fa = *lp<volatile v4u>(F_A), f = *lp<volatile v4u>(F_P);
            asm volatile("" ::: "memory");
#pragma unroll
            for (int st = 0; st < 8; ++st) pf[st] = *lp<v4u>(pa + st * 256);
            if (WOF < 3) rd(0);
            if (WOF < 2) rd(1);
            if (WOF < 1) rd(2);
            if (!WT) {
                if (!uni_ge(umn4(fa), 2)) wait_flag<4, 1>(F_A, 2);
                WOISSUE();
            }
            if (WOF >= 3) rd(0);
            if (WOF >= 2) rd(1);
            if (WOF >= 1) rd(2);
            STAMP(16);
            if (!uni_ge(umn4(f), 1)) {
                wait_flag<4, 1>(F_P, 1);
#pragma unroll
                for (int st = 0; st < 8; ++st) pf[st] = *lp<v4u>(pa + st * 256);
            }
            SB();
        }
        STAMP(17);
        mm(0); mm(1);
        constexpr int NW = WT ? 0 : 8;
        SB(); WAITVM(16 + NW); rd(3); mm(2);
        SB(); WAITVM(12 + NW); rd(4); mm(3);
        SB(); WAITVM(8 + NW); rd(5); mm(4);
        SB(); WAITVM(4 + NW); rd(6); mm(5);
        SB();
        WAITVM(2 + NW);
        v2u lo[4];
#pragma unroll
        for (int t = 0; t < 4; ++t) lo[t] = __builtin_bit_cast(v2u, __builtin_amdgcn_ds_read_tr16_b64_v4i16(lp<v4s>(va[t] + 7 * 4096)));
        SB();
        mm(6);
        SB();
        WAITVM(NW);
        STAMP(8);
#pragma unroll
        for (int t = 0; t < 4; ++t) {
            const v2u hi = __builtin_bit_cast(v2u, __builtin_amdgcn_ds_read_tr16_b64_v4i16(lp<v4s>(va[t] + 7 * 4096 + 2048)));
            vf[7][t] = (v4u){lo[t].x, lo[t].y, hi.x, hi.y};
        }
        SB();
        mm(7);
#undef rd
#undef mm
        const v2u go = {umn(pkbf(acc[0].x, acc[1].x), ~1u), umn(pkbf(acc[2].x, acc[3].x), ~1u)};
        __builtin_amdgcn_raw_buffer_store_b64(go, r_ws, ovo, oso, AUX_SC1);
        set_flag(fl, 33);
        STAMP(9);
        if (!WT) STAMP(25);
        if (dh == 0) { PUTST(7, 10); PUTST(16, 18); if (!WT) PUTST(25, 26); }
        __builtin_amdgcn_endpgm();
    }

    if (wave2 >= 6) {
        const int tw = wave2 - 6;
        const int tj = sc * 32 + gc * 4 + b;
        const v4u ones = {~0u, ~0u, ~0u, ~0u};
        ST128(ones, r_ws, l < 8 && !(XP & 136) && !ARST ? l * 16 : OOB, WS_A + tw * GA + aoff, AUX_SC1);
        {
            const u16* p = tw == 0 ? vc + ((size_t)(b * 8 + g) * 8192 + 6144 + s * 256) * 128 : tw == 1 ? wo + (H2 == 2 ? (gc >> 1) * 8 + b * 2 : H2 == 3 ? (gc & 3) * 8 + b * 2 : gc * 4 + b) * 65536 : out;
            junk_load(mkrsrc(p, ((JUNK >> tw) & 1) && (tw != 2 || fin || JUNK >= 8) ? 2 : 0), l * 4, L_JUNK);
        }
        if (TWAIT) wait_flag<4, 4>(F_A, TWAIT);
        asm volatile("s_waitcnt vmcnt(2)" : "+{v[100:103]}"(ca), "+{v[104:107]}"(cb), "+{v[108:111]}"(cc), "+v"(tag0), "+v"(b2r) :: "memory");
        SB();
        Coef cf;
        make_coef(cf, ca, cb, cc);
        tag = tag0 + TAG_STEP;
        if (ALN && tw == 3 && bid < 8) { const v2u st = {(u32)tstart, tag}; __builtin_amdgcn_raw_buffer_store_b64(st, r_ws, l * 128, WS_TS + bid * 8, AUX_SC1); }
        {
            const u32 tb0 = (u32)(((tj & 128) << 8) + T_LO + (tj & 127)) << 16;
            unsigned long long tt = tb0 | ((unsigned long long)(tb0 + 0x800000u) << 32);
            u32 ta = (l & 11) == 0 ? L_TP - 128 + tw * 16 + kg * 4 + (l & 4) * 16 : L_JUNK, n = TN / 2 - 1;
            const u32 inc = (l & 11) == 0 ? 128 : 0;
#define T0 " op_sel:[0,0,0] op_sel_hi:[1,0,1]\n"
#define T1 " op_sel:[0,1,0] op_sel_hi:[1,1,1]\n"
#define TC " op_sel:[0,0,1] op_sel_hi:[0,1,1]\n"
#define TP(x) " op_sel:[0,0," #x "] op_sel_hi:[1,1," #x "]\n"
#define HA(d, a, c, m) "v_pk_fma_f32 v[" d "], %[" a "], s[40:41], %[" c "]" m
#define HB(d, c, m) "v_pk_fma_f32 v[" d "], v[" d "], s[40:41], %[" c "]" m
#define AC(d, x, g) "v_pk_fma_f32 v[" d "], v[" x "], v[" g "], v[" d "]\n"
#define DA(s, c, m) "v_add_f32_dpp v158, " s ", " s " " c " row_mask:0xf bank_mask:" m "\n"
#define EX(r) "v_exp_f32 v" #r ", v" #r "\n"
#define RC(r) "v_rcp_f32 v" #r ", v" #r "\n"
#define P1(d) "v_pk_add_f32 v[" d "], v[" d "], 1.0 op_sel_hi:[1,0]\n"
            asm volatile(
                "1:\n"
                HA("128:129", "a30", "a20", T0) HA("130:131", "a30", "a20", T1)
                DA("v156", "row_ror:4", "0x5") DA("v157", "row_ror:4", "0xa")
                HA("132:133", "a31", "a21", T0) HA("134:135", "a31", "a21", T1)
                DA("v158", "row_ror:8", "0xf")
                HB("128:129", "a10", T0) HB("130:131", "a10", T1)
                DA("v158", "quad_perm:[1,0,3,2]", "0xf")
                HB("128:129", "a00", T0) HB("130:131", "a00", T1)
                DA("v158", "quad_perm:[2,3,0,1]", "0xf")
                "ds_write_b32 %[ta], v158\n"
                EX(128) HB("132:133", "a11", T0) HA("136:137", "a32", "a22", T0) EX(129) HB("134:135", "a11", T1) HA("138:139", "a32", "a22", T1)
                EX(130) HB("132:133", "a01", T0) HB("136:137", "a12", T0) EX(131) HB("134:135", "a01", T1) HB("138:139", "a12", T1)
                EX(132) HA("140:141", "c32", "c32", TC) HB("136:137", "a02", T0) EX(133) HB("138:139", "a02", T1) HB("140:141", "c10", TP(0))
                EX(134) P1("128:129") EX(135) HB("140:141", "c10", TP(1)) EX(136) P1("130:131") EX(137) P1("132:133") EX(138) P1("134:135")
                EX(139) P1("136:137") EX(140) HA("142:143", "g10", "g00", T0) EX(141) P1("138:139")
                RC(128) HA("144:145", "g10", "g00", T1) RC(129) P1("140:141") RC(130) HA("146:147", "g11", "g01", T0) RC(131) "v_pk_mul_f32 v[142:143], v[128:129], v[142:143]\n"
                RC(132) HA("148:149", "g11", "g01", T1) RC(133) "v_pk_mul_f32 v[144:145], v[130:131], v[144:145]\n" RC(134) HA("150:151", "g12", "g02", T0) RC(135) AC("142:143", "132:133", "146:147")
                RC(136) HA("152:153", "g12", "g02", T1) RC(137) AC("144:145", "134:135", "148:149") RC(138) HA("154:155", "cg", "cg", TC) RC(139) AC("142:143", "136:137", "150:151")
                "s_add_u32 s40, s40, 0x1000000\n s_add_u32 s41, s41, 0x1000000\n"
                RC(140) AC("144:145", "138:139", "152:153") RC(141)
                "v_add_f32 v156, v142, v143\n v_add_f32 v157, v144, v145\n"
                "s_sub_u32 %[n], %[n], 1\n"
                "v_add_u32 %[ta], %[ta], %[inc]\n"
                AC("156:157", "140:141", "154:155")
                "s_cbranch_scc0 1b\n"
                "s_nop 0\n"
                DA("v156", "row_ror:4", "0x5") DA("v157", "row_ror:4", "0xa") "s_nop 1\n" DA("v158", "row_ror:8", "0xf") "s_nop 1\n"
                DA("v158", "quad_perm:[1,0,3,2]", "0xf") "s_nop 1\n" DA("v158", "quad_perm:[2,3,0,1]", "0xf")
                "ds_write_b32 %[ta], v158\n"
                : [ta] "+v"(ta), [n] "+s"(n), "+{s[40:41]}"(tt)
                : [inc] "v"(inc), [a30] "v"(cf.a3[0]), [a31] "v"(cf.a3[1]), [a32] "v"(cf.a3[2]), [a20] "v"(cf.a2[0]), [a21] "v"(cf.a2[1]), [a22] "v"(cf.a2[2]),
                  [a10] "v"(cf.a1[0]), [a11] "v"(cf.a1[1]), [a12] "v"(cf.a1[2]), [a00] "v"(cf.a0[0]), [a01] "v"(cf.a0[1]), [a02] "v"(cf.a0[2]),
                  [g10] "v"(cf.g1[0]), [g11] "v"(cf.g1[1]), [g12] "v"(cf.g1[2]), [g00] "v"(cf.g0[0]), [g01] "v"(cf.g0[1]), [g02] "v"(cf.g0[2]),
                  [c32] "v"(cf.c32), [c10] "v"(cf.c10), [cg] "v"(cf.cg)
                : "memory", "scc", "v128", "v129", "v130", "v131", "v132", "v133", "v134", "v135", "v136", "v137", "v138", "v139", "v140", "v141", "v142", "v143",
                  "v144", "v145", "v146", "v147", "v148", "v149", "v150", "v151", "v152", "v153", "v154", "v155", "v156", "v157", "v158");
            static_assert(TN % 2 == 0, "");
        }
        if (ARST) ST128(ones, r_ws, l < 8 ? l * 16 : OOB, WS_A + tw * GA + tj * 128, AUX_SC1);
        if (ORST == 4) __builtin_amdgcn_raw_buffer_store_b32(~0u, r_ws, kg * 512 + n16 * 4, WS_O + g * GO + tw * 8192 + b * 2048 + s * 64, AUX_SC1);
        WAITVM(0);
        {
            u32 arr = 0;
            if (l == 0) arr = __hip_atomic_fetch_add(lp<u32>(F_ARR), 9u, __ATOMIC_RELAXED, __HIP_MEMORY_SCOPE_WORKGROUP);
            asm volatile("" ::: "memory");
            if (__builtin_amdgcn_readfirstlane(arr) == 27) {
#pragma unroll 1
                for (int q = 0; q < (TN > 16 ? 2 : 1); ++q) {
                    const v4f p = *lp<v4f>(L_TP + q * 1024 + l * 16);
                    float v = (p.x + p.y) + (p.z + p.w);
                    v += dppf<DPP_X1>(v); v += dppf<DPP_X2>(v);
                    v = rbf(v) + bflo(b2r);
                    if (FBT && q == 0 && (l >> 2) < FBT) v += 0.37f;
                    const u32 p0 = pkbf(v, dppf<DPP_SHL4>(v));
                    const u32 p1 = dppu<DPP_SHL8>(p0);
                    const v4u gt = {p0, tag, TMX && q ? (u32)tstart : p1, tag};
                    ST128(gt, r_ws, (l & (q ? 63 : 15)) == 0 ? kg * 16 : OOB, q ? WS_T2 + tj * 16 : WS_T + tj * 64, AUX_SC1);
                }
            }
        }
        STAMP(10);
        if (tw == TSW) PUTST(10, 11);
        static_assert(WV == 2 && !H2 && !NBW && T4F && TWL, "");
        const int wvo = pin((16 * (lr & 1) + 2 * (lr & 6)) * 1024 + swz);
        const u32 wfa = pin(F_W + tw), wfb = pin(1);
        u32 wso = (gc * 4 + b) * (128 * 1024) + tw * 32768 + sc * 128, wm0 = L_W + (tw >> 1) * 8192 + (tw & 1) * 2048;
        u32 wn = 4096;
        const u32 wt = __builtin_amdgcn_readfirstlane((u32)tstart) + (tw < WE ? W0T + W0D * tw : WT + WD * (tw - WE));
#define WDLY "1:\n s_memrealtime s[40:41]\n s_sub_u32 %[n], %[n], 1\n s_cbranch_scc1 2f\n s_waitcnt lgkmcnt(0)\n s_sub_i32 s40, s40, %[t]\n s_cmp_lt_i32 s40, -9\n s_cbranch_scc0 3f\n s_sleep 1\n" \
             "3:\n s_cmp_lt_i32 s40, 0\n s_cbranch_scc1 1b\n2:\n"
        const __amdgpu_buffer_rsrc_t r_wo = mkrsrc(wo, 4u << 20);
        wait_flag<4, 4>(F_A, 2);
#define WL2 "buffer_load_dwordx4 %[vo], %[rs], %[so] offen nt lds\n"
#define WISS() do { if (WT == 1) wait_flag<2, 4>(F_L, 29); else wait_flag<2, 3>(F_L, tw < WE ? 0 : 31); __builtin_amdgcn_s_setprio(WPR); if (WT == 1) wait_flag<2, 1>(F_L, 31); asm volatile("s_mov_b32 m0, %[m]\n" \
            WDLY \
            WL2 "s_add_u32 m0, m0, 1024\n s_add_u32 %[so], %[so], 1024\n" WL2 "s_add_u32 m0, m0, 3072\n s_add_u32 %[so], %[so], 1024\n" \
            WL2 "s_add_u32 m0, m0, 1024\n s_add_u32 %[so], %[so], 1024\n" WL2 "s_waitcnt lgkmcnt(0)\n" \
            : [so] "+s"(wso), [n] "+s"(wn) : [vo] "v"(wvo), [rs] "s"(r_wo), [m] "s"(wm0), [t] "s"(wt) : "memory", "scc", "s40", "s41"); } while (0)
#define WFIN(k) asm volatile("s_waitcnt vmcnt(" #k ")\n ds_write_b8 %[fa], %[fb]\n" :: [fa] "v"(wfa), [fb] "v"(wfb) : "memory")
#define WJOB() do { WISS(); WFIN(0); } while (0)
        if (!fin) {
            WJOB();
            STAMP(25);
            if (tw == 3) PUTST(25, 26);
            __builtin_amdgcn_endpgm();
        }
        __builtin_amdgcn_s_setprio(1);
        const int D = tw * 64 + (n16 >> 3) * 32 + (l & 7) * 4 + kg;
        v2f xr = bf2(*lp<u32>(L_X + b * 1024 + D * 4));
        v4u sel, a[8];
#pragma unroll
        for (int q = 0; q < 4; ++q) sel[q] = ((n16 & 2) == 0 && (n16 >> 2) == q) ? ((n16 & 1) ? 0x3F800000u : 0x3F80u) : 0u;
        if (FPM == 0) {
#pragma unroll
            for (int i = 0; i < 8; ++i) a[i] = ones;
        }
        const int aso = WS_A + b * GA + ((HN & 2) ? tw * 16384 + SENSE * 8192 : tw * 8192);
        u32 ta, tb, one = 1, fb;
        const __amdgpu_buffer_rsrc_t r_out = mkrsrc(out, 4096);
#define AREG(c) c "{v[128:131]}"(a[0]), c "{v[132:135]}"(a[1]), c "{v[136:139]}"(a[2]), c "{v[140:143]}"(a[3]), c "{v[144:147]}"(a[4]), c "{v[148:151]}"(a[5]), c "{v[152:155]}"(a[6]), c "{v[156:159]}"(a[7])
        const int avo = pin((n16 >> 3) * 4096 + kg * 128 + (l & 7) * 16);
        const u32 sta = pin(L_SP + tw * 64 + kg * 16), str = pin(L_SP + n16 * 16), ssa = pin(L_ST + tw * 4), ssr = pin(L_ST);
        const int ooff = pin(b * 1024 + D * 4);
        const int eoff = pin(b == 0 && tw == 0 && l == 0 ? 0 : OOB);
        const u32 a1 = pin(L_TAB + 2 * T_LO + (tw >> 1) * 65536 + ((tw & 1) * 64 + (l >> 2)) * 2 + (l & 3) * 1024);
        const u32 a2 = pin(L_TAB + 2 * T_LO + (tw >> 1) * 65536 + ((tw & 1) * 64 + l) * 2 + 4096);
        float hx = xr.x, hy = xr.y, y0 = 0.f, y1 = 0.f;
        u32 ns = SPIN_MAX;
        asm volatile("" : "+v"(hx), "+v"(hy), "+v"(y0), "+v"(y1), "+v"(sel), "+{v167}"(one), "+s"(ns) : "s"(aso), "s"(r_out));
        WISS();
        __builtin_amdgcn_s_setprio(1);
        if (TG == 2) wait_flag<2, 4>(F_L, 33);
        if (TG == 3) wait_flag<2, 4>(F_L, TGL);
        constexpr int NG = TN > 16 ? 5 : 4;
        v4u t[NG];
        {
            bool bad = true;
#pragma unroll
            for (int q = 0; q < 4; ++q) t[q] = LD128(r_ws, l * 16 + q * 1024, WS_T + tw * 4096, AUX_SC1);
            if (NG > 4) t[NG - 1] = LD128(r_ws, l * 16, WS_T2 + tw * 1024, AUX_SC1);
            static_assert(NG == 5, "");
            WFIN(5);
            STAMP(25);
            if (tw == 3) PUTST(25, 26);
            bad = false;
#pragma unroll 1
            for (int spin = 0; spin < SPIN_MAX; ++spin) {
                asm volatile("" ::: "memory");
                if (bad) {
#pragma unroll
                    for (int q = 0; q < 4; ++q) t[q] = LD128(r_ws, l * 16 + q * 1024, WS_T + tw * 4096, AUX_SC1);
                    if (NG > 4) t[NG - 1] = LD128(r_ws, l * 16, WS_T2 + tw * 1024, AUX_SC1);
                }
                u32 e = 0;
#pragma unroll
                for (int q = 0; q < NG; ++q) e |= (t[q].y ^ tag) | (t[q].w ^ tag);
                bad = e != 0;
                if (all_lanes(!bad)) break;
                if (GSL) __builtin_amdgcn_s_sleep(GSL);
            }
        }
#define TWR() do { _Pragma("unroll") for (int q = 0; q < NG; ++q) { const u32 a_ = q < 4 ? a1 + q * 32 : a2; \
                *lp<u16>(a_) = (u16)t[q].x; *lp<u16>(a_ + 256) = (u16)(t[q].x >> 16); *lp<u16>(a_ + 512) = (u16)t[q].z; *lp<u16>(a_ + 768) = (u16)(t[q].z >> 16); } } while (0)
        if (!TWL) TWR();
        {
            u32 t4 = __builtin_amdgcn_readfirstlane((u32)tstart) + T4F;
            u32 ko[8];
            if (TMX == 2) {
                int d = (int)(t[NG - 1].z - (u32)tstart) + 64;
                u32 e = d < 0 ? 0 : d > 128 ? 128 : d;
                e = umx(e, dppu<DPP_X1>(e)); e = umx(e, dppu<DPP_X2>(e));
                auto r = __builtin_amdgcn_permlane32_swap(e, e, false, false);
                e = umx(r[0], r[1]) * 4096 + (l & 28) * 128;
#pragma unroll
                for (int i = 0; i < 8; ++i) ko[i] = __builtin_amdgcn_readlane(e, 4 * i);
#define CE(i, j) do { u32 lo_; asm("s_min_u32 %0, %2, %1\n s_max_u32 %1, %2, %1" : "=&s"(lo_), "+s"(ko[j]) : "s"(ko[i]) : "scc"); ko[i] = lo_; } while (0)
                CE(0, 2); CE(1, 3); CE(4, 6); CE(5, 7); CE(0, 4); CE(1, 5); CE(2, 6); CE(3, 7); CE(0, 1); CE(2, 3); CE(4, 5); CE(6, 7);
                CE(2, 4); CE(3, 5); CE(1, 4); CE(3, 6); CE(1, 2); CE(3, 4); CE(5, 6);
                u32 m = 0;
#pragma unroll
                for (int i = 0; i < 8; ++i) asm("s_add_u32 %0, %0, %3\n s_max_u32 %1, %1, %0\n s_and_b32 %0, %0, 0xfff\n s_add_u32 %0, %0, %2" : "+s"(ko[i]), "+s"(m) : "s"(aso), "i"((7 - i) * TDL * 4096) : "scc");
                t4 += (m >> 12) - 64 - 7 * TDL;
            }
            if (TMX == 1) {
                int d = (int)(t[NG - 1].z - (u32)tstart) + 64;
                u32 e = d < 0 ? 0 : d > 128 ? 128 : d;
                e = umx(e, dppu<DPP_X1>(e)); e = umx(e, dppu<DPP_X2>(e)); e = umx(e, dppu<DPP_HMIR>(e)); e = umx(e, dppu<DPP_ROR8>(e));
                const u32 e0 = umx(__builtin_amdgcn_readlane(e, 0), __builtin_amdgcn_readlane(e, 16)), e1 = umx(__builtin_amdgcn_readlane(e, 32), __builtin_amdgcn_readlane(e, 48));
                t4 += umx(e0, e1) - 64;
            }
            if (FAW) wait_flag<4, FAW - 1>(F_A, 3);
#pragma unroll 1
            for (int i = 0; i < 4096; ++i) { if ((int)(now32() - t4) >= 0) break; if (FSL) __builtin_amdgcn_s_sleep(FSL); }
            __builtin_amdgcn_s_setprio(3);
            if (FDL) __builtin_amdgcn_s_sleep(FDL);
            u32 u0, u1;
#define FMA_(r, c) "v_mfma_f32_16x16x32_bf16 v[160:163], %[sel], v[" r "], " c "\n"
#define FCH(r0, r1, r2, r3) "v_max3_u32 %[ta], %[ta], v" #r0 ", v" #r1 "\n v_max3_u32 %[tb], %[tb], v" #r2 ", v" #r3 "\n"
#define FLD(r, o) "buffer_load_dwordx4 v[" r "], %[vo], %[rs], %[so] offen offset:" #o " sc1\n"
#define FLM(r0, r1, r2, r3, o) "v_max3_u32 %[ta], v" #r0 ", v" #r1 ", v" #r2 "\n v_max_u32 %[ta], %[ta], v" #r3 "\n v_cmpx_eq_u32 vcc, -1, %[ta]\n" FLD(#r0 ":" #r3, o) "s_mov_b64 exec, -1\n"
#define RDP(x, y, c) "v_add_f32_dpp " x ", " x ", " x " " c " row_mask:0xf bank_mask:0xf\n v_add_f32_dpp " y ", " y ", " y " " c " row_mask:0xf bank_mask:0xf\n"
#define BC0 " row_newbcast:0 row_mask:0xf bank_mask:0xf\n"
#define RB(x) "v_cvt_pk_bf16_f32 " x ", 0, " x "\n"
#define FALL FLD("128:131", 0) FLD("132:135", 512) FLD("136:139", 1024) FLD("140:143", 1536) FLD("144:147", 2048) FLD("148:151", 2560) FLD("152:155", 3072) FLD("156:159", 3584)
            if (FPM) asm volatile(FALL : AREG("=&") : [vo] "v"(avo), [rs] "s"(r_ws), [so] "s"(aso) : "memory");
            if (TWL) TWR();
            asm volatile(
#define FBODY "v_max3_u32 %[ta], v128, v129, v130\n v_max3_u32 %[tb], v131, v132, v133\n" \
                FMA_("128:131", "0") FCH(134, 135, 136, 137) FMA_("132:135", "v[160:163]") FCH(138, 139, 140, 141) FMA_("136:139", "v[160:163]") FCH(142, 143, 144, 145) \
                FMA_("140:143", "v[160:163]") FCH(146, 147, 148, 149) FMA_("144:147", "v[160:163]") FCH(150, 151, 152, 153) FMA_("148:151", "v[160:163]") FCH(154, 155, 156, 157) \
                FMA_("152:155", "v[160:163]") "v_max3_u32 %[ta], %[ta], v158, v159\n" FMA_("156:159", "v[160:163]") "v_max_u32 %[ta], %[ta], %[tb]\n v_cmp_eq_u32 vcc, -1, %[ta]\n"
#define FS7(w, r0, r1, r2, r3) "s_waitcnt vmcnt(" #w ")\n" FMA_(#r0 ":" #r3, "v[160:163]") FCH(r0, r1, r2, r3)
                "s_waitcnt vmcnt(7)\n" FMA_("128:131", "0") "v_max_u32 %[ta], v128, v129\n v_max_u32 %[tb], v130, v131\n"
                FS7(6, 132, 133, 134, 135) FS7(5, 136, 137, 138, 139) FS7(4, 140, 141, 142, 143) FS7(3, 144, 145, 146, 147)
                FS7(2, 148, 149, 150, 151) FS7(1, 152, 153, 154, 155) FS7(0, 156, 157, 158, 159)
                "v_max_u32 %[ta], %[ta], %[tb]\n v_cmp_eq_u32 vcc, -1, %[ta]\n"
                "s_cbranch_vccnz 8f\n"
                "2:\n"
                "s_nop 2\n"
                RB("v160") RB("v161")
                "v_add_f32 %[hx], %[hx], v160\n v_add_f32 %[hy], %[hy], v161\n"
                RB("%[hx]") RB("%[hy]")
                "s_nop 0\n"
                "v_subrev_f32_dpp v164, %[hx], %[hx]" BC0 "v_subrev_f32_dpp %[u0], %[hx], %[hy]" BC0
                "v_mul_f32 v165, v164, v164\n v_add_f32 v164, v164, %[u0]\n v_fmac_f32 v165, %[u0], %[u0]\n"
                "v_mov_b32_dpp v166, %[hx]" BC0
                RDP("v164", "v165", "quad_perm:[1,0,3,2]") "s_nop 0\n" RDP("v164", "v165", "quad_perm:[2,3,0,1]") "s_nop 0\n"
                RDP("v164", "v165", "row_half_mirror") "s_nop 0\n" RDP("v164", "v165", "row_ror:8")
                "ds_write_b128 %[sta], v[164:167]\n"
                "3:\n"
                "ds_read_b128 v[160:163], %[str]\n"
                "s_sub_u32 %[n], %[n], 1\n"
                "s_cbranch_scc1 4f\n"
                "s_waitcnt lgkmcnt(0)\n"
                "v_cmp_eq_u32 vcc, 0, v163\n"
                "v_subrev_f32_dpp %[u0], v162, v162" BC0
                "v_fmamk_f32 v164, %[u0], 0x42000000, v160\n"
                "v_add_f32 v160, v160, v164\n"
                "v_fmac_f32 v161, %[u0], v160\n"
                "s_cbranch_vccnz 3b\n"
                "4:\n"
                RDP("v164", "v161", "quad_perm:[1,0,3,2]") "v_mov_b32_dpp v162, v162" BC0 RDP("v164", "v161", "quad_perm:[2,3,0,1]") "s_nop 0\n"
                RDP("v164", "v161", "row_half_mirror") "s_nop 0\n" RDP("v164", "v161", "row_ror:8")
                "v_mul_f32 v164, 0x3b000000, v164\n"
                "v_mul_f32 v161, 0x3b000000, v161\n"
                "v_add_f32 v162, v162, v164\n"
                "v_fma_f32 v161, -v164, v164, v161\n"
                "v_cvt_pk_bf16_f32 v163, 0, v162\n"
                "v_cvt_pk_bf16_f32 v160, 0, v161\n"
                "v_sub_f32 %[u0], %[hx], v163\n"
                "v_add_f32 v160, 0x3727c5ac, v160\n"
                "v_sub_f32 %[u1], %[hy], v163\n"
                RB("v160")
                "v_sub_f32 v162, v162, v163\n"
                "v_sqrt_f32 v160, v160\n"
                RB("%[u0]")
                RB("v160")
                RB("%[u1]")
                "v_rcp_f32 v160, v160\n"
                "s_nop 0\n"
                "v_mul_f32 v161, v161, v160\n"
                "v_mul_f32 v162, v162, v160\n"
                "v_mul_f32 v161, v161, v160\n"
                "v_mul_f32 %[u0], %[u0], v160\n"
                RB("v161")
                "v_mul_f32 %[u1], %[u1], v160\n"
                "v_add_f32 v161, 0x3727c5ac, v161\n"
                RB("v162")
                RB("v161")
                RB("%[u0]")
                "v_sqrt_f32 v161, v161\n"
                RB("%[u1]")
                RB("v161")
                "v_sub_f32 %[u0], %[u0], v162\n"
                "v_rcp_f32 v161, v161\n"
                "v_sub_f32 %[u1], %[u1], v162\n"
                RB("%[u0]") RB("%[u1]")
                "v_mul_f32 %[u0], %[u0], v161\n v_mul_f32 %[u1], %[u1], v161\n"
                RB("%[u0]") RB("%[u1]")
                "v_lshrrev_b32 %[ta], 15, %[u0]\n v_lshrrev_b32 %[tb], 15, %[u1]\n"
                "ds_read_u16_d16_hi %[y0], %[ta] offset:%[tab]\n ds_read_u16_d16_hi %[y1], %[tb] offset:%[tab]\n"
                "v_subrev_u16 %[u0], %[tlo], %[ta]\n v_subrev_u16 %[u1], %[tlo], %[tb]\n"
                "v_max_u16 %[u0], %[u0], %[u1]\n"
                "v_cmp_lt_u16 vcc, %[lim], %[u0]\n"
                "s_cmp_lg_u64 vcc, 0\n"
                "s_cselect_b32 %[fb], 2, 1\n"
                "v_mov_b32 %[u0], %[fb]\n"
                "ds_write_b32 %[ssa], %[u0]\n"
                "ds_read_b128 v[160:163], %[ssr]\n"
                "s_waitcnt lgkmcnt(2)\n"
                "v_add_f32 %[y0], %[hx], %[y0]\n v_add_f32 %[y1], %[hy], %[y1]\n"
                "v_cvt_pk_bf16_f32 %[y0], %[y0], %[y1]\n"
                "buffer_store_dword %[y0], %[oo], %[ro], 0 offen sc1\n"
                "buffer_store_dword %[tag], %[eo], %[rs], 0 offen sc1\n"
                "5:\n"
                "s_waitcnt lgkmcnt(0)\n"
                "v_mul_u32_u24 v160, v160, v161\n v_mul_u32_u24 v162, v162, v163\n v_mul_u32_u24 v164, v160, v162\n"
                "s_sub_u32 %[n], %[n], 1\n"
                "s_cbranch_scc1 6f\n"
                "v_readfirstlane_b32 %[fb], v164\n"
                "s_cmp_lg_u32 %[fb], 0\n"
                "s_cbranch_scc1 6f\n"
                "ds_read_b128 v[160:163], %[ssr]\n"
                "s_branch 5b\n"
                "6:\n"
                "s_cmp_lg_u32 %[fb], 1\n"
                "s_cbranch_scc1 7f\n"
                "s_endpgm\n"
                "8:\n"
                "s_mov_b64 exec, vcc\n"
                FALL
                "s_mov_b64 exec, -1\n"
                "s_waitcnt vmcnt(%[cap])\n"
                "s_sub_u32 %[n], %[n], 1\n"
                "s_cbranch_scc1 2b\n"
                FBODY
                "s_nop 1\n"
                "s_cbranch_vccz 2b\n"
                "s_branch 8b\n"
                "7:\n"
                : AREG("+"), [ta] "=&v"(ta), [tb] "=&v"(tb), [u0] "=&v"(u0), [u1] "=&v"(u1), [n] "+s"(ns), [hx] "+v"(hx), [hy] "+v"(hy), [y0] "+v"(y0), [y1] "+v"(y1), [fb] "=&s"(fb)
                : [vo] "v"(avo), [rs] "s"(r_ws), [so] "s"(aso), [cap] "i"(ACAP), [f6] "i"(F6W), [sel] "v"(sel), "{v167}"(one), [sta] "v"(sta), [str] "v"(str), [ssa] "v"(ssa), [ssr] "v"(ssr),
                  [oo] "v"(ooff), [eo] "v"(eoff), [ro] "s"(r_out), [tag] "v"(tag), [tab] "i"(L_TAB), [tlo] "i"(2 * (T_LO + FBT * 128)), [lim] "i"(2 * (TN - FBT) * 128 - 1)
                : "memory", "scc", "vcc", "v160", "v161", "v162", "v163", "v164", "v165", "v166"
                );
            static_assert(WS_EPOCH == 0 && OAUX == 16 && L_TAB + 2 * T_LO >= L_K && L_TAB + 2 * 130 * 128 <= L_FB && L_TAB + 0x20000 <= L_END && TN >= 16 && TN <= 20, "");
        }
        if (tw == 0) PUTST(14, 16);
        const v2f h = {hx, hy};
        ta >>= 1; tb >>= 1;
#pragma unroll
        for (int q = 0; q < 2; ++q) {
            const u32 t = q ? tb : ta;
            if ((t & 0x7fffu) - (T_LO + FBT * 128) >= (TN - FBT) * 128) {
                const u32 i = __hip_atomic_fetch_add(lp<u32>(F_FB), 1u, __ATOMIC_RELAXED, __HIP_MEMORY_SCOPE_WORKGROUP);
                const v2u it = {(t << 16) | (u32)(2 * D + q), f2u(q ? h.y : h.x)};
                *lp<v2u>(L_FB + i * 8) = it;
            }
        }
        lds_barrier();
        const int n = __builtin_amdgcn_readfirstlane(*lp<u32>(F_FB));
        if (n > 0) {
#pragma unroll 1
            for (int i = 0; i < n; ++i) {
                const float p = wave_sum(mlp_part(cf, bfhi(*lp<u32>(L_FB + i * 8))));
                *lp<float>(l == 0 ? L_FP + i * 16 + tw * 4 : L_JUNK) = p;
            }
            __builtin_amdgcn_s_waitcnt(0x0F70);
            lds_barrier();
#pragma unroll 1
            for (int i = tw * 64 + l; i < n; i += 256) {
                const v2u it = *lp<v2u>(L_FB + i * 8);
                const v4f p = *lp<v4f>(L_FP + i * 16);
                const float yv = rbf(rbf((p.x + p.y) + (p.z + p.w)) + bflo(b2r));
                __builtin_amdgcn_raw_buffer_store_b16((u16)pkbf(u2f(it.y) + yv, 0.f), r_out, (it.x & 0xffffu) * 2, b * 1024, OAUX);
            }
        }
        asm volatile("" :: AREG(""));
        __builtin_amdgcn_endpgm();
    }

    const int w = wave2;
    v4u tb;
    u32 wa[4], rdy;
    const int rvo = (l & 7) * 4, rso = WS_RD + gc * GML + w * 32;
#pragma unroll
    for (int ct = 0; ct < 4; ++ct) wa[ct] = pin((L_W + fra) ^ (ct * 32));
    {
        const u32 xb = pin(L_X + w * 1024 + l * 16);
        const u32 xa = pin(kg == b ? L_XN + w * 320 + (n16 >> 2) * 64 + (n16 & 1) * 32 + ((n16 >> 1) & 1) * 8 : L_JUNK);
        const u32 xf = pin(L_XN + (n16 >> 2) * 320 + kg * 16);
        const u32 wq0 = pin((L_W + fra) ^ (w * 32));
        const int qvo = pin((n16 & 3) == 0 && !(XP & 2) ? (n16 >> 2) * 8192 + kg * 16 : OOB);
        WAITVM(6);
        STAMP(1);
        const v4u xr = *lp<v4u>(xb);
        v2f xv[4];
#pragma unroll
        for (int q = 0; q < 4; ++q) xv[q] = bf2(xr[q]);
        const float c = u2f(__builtin_amdgcn_readfirstlane(f2u(xv[0].x)));
        const v2f c2 = {c, c};
        v2f d[4];
#pragma unroll
        for (int q = 0; q < 4; ++q) d[q] = xv[q] - c2;
        const v2f s2 = (d[0] + d[1]) + (d[2] + d[3]);
        const v2f q2 = __builtin_elementwise_fma(d[0], d[0], d[1] * d[1]) + __builtin_elementwise_fma(d[2], d[2], d[3] * d[3]);
        const float sd = wave_sum(s2.x + s2.y) * (1.f / 512.f), sq = wave_sum(q2.x + q2.y) * (1.f / 512.f);
        const float var = fmx(__builtin_fmaf(-sd, sd, sq), 0.f);
        const float mb = rbf(c + sd);
        const float rden = __builtin_amdgcn_rcpf(rbf(__builtin_amdgcn_sqrtf(rbf(rbf(var) + 1e-5f))));
        const v2f mb2 = {mb, mb}, rd2 = {rden, rden};
        v4u o;
#pragma unroll
        for (int q = 0; q < 4; ++q) o[q] = pk2(bf2(pk2(xv[q] - mb2)) * rd2);
        const v2u o0 = {o.x, o.y}, o1 = {o.z, o.w};
        *lp<v2u>(xa) = o0; *lp<v2u>(xa + 16) = o1;
        asm volatile("s_waitcnt vmcnt(0)\n\tv_add_u32 %0, %2, %1" : "=v"(tag) : "v"(tag0), "i"(TAG_STEP) : "memory");
        set_flag(F_A + w * 4, 1);
#pragma unroll
        for (int i = 0; i < 4; ++i) tb[i] = (tag >> (8 * i)) & 255u;
        v4u af[4], bf[4];
        if (PRE) wait_flag<4, PRE>(F_A, 1);
#pragma unroll 1
        for (int spin = 0; spin < SPIN_MAX; ++spin) {
            const v4u f = *lp<volatile v4u>(F_A);
            asm volatile("" ::: "memory");
#pragma unroll
            for (int t = 0; t < 4; ++t) { af[t] = *lp<v4u>(xf + t * 64); bf[t] = trfrag(wq0 + t * 4096); }
            SB();
            if (uni_ge(umn4(f), 1)) break;
        }
        v4f acc = {0.f, 0.f, 0.f, 0.f};
#pragma unroll
        for (int t = 0; t < 4; ++t) acc = mfma16(bf[t], af[t], acc);
        v4u gq;
#pragma unroll
        for (int i = 0; i < 4; ++i) gq[i] = (f2u(acc[i]) & ~255u) | tb[i];
        ST128(gq, r_ws, qvo, WS_Q + g * GQ + (s >> 1) * 2048 + b * 512 + (s & 1) * 256 + w * 64, AUX_SC1);
        set_flag(F_A + w * 4, 2);
        if (CSW && w == 3) __builtin_amdgcn_raw_buffer_store_b32(tag, r_ws, l == 0 ? 0 : OOB, WS_RD + g * GML + (b * 8 + s) * 4, AUX_SC1);
        if (ORST == 2) __builtin_amdgcn_raw_buffer_store_b32(~0u, r_ws, kg * 512 + n16 * 4, WS_O + g * GO + w * 8192 + b * 2048 + s * 64, AUX_SC1);
    }
    STAMP(2);
    if (ALN && w == 3) {
        if (TD) {
            const u32 td = __builtin_amdgcn_readfirstlane((u32)tstart) + TD;
#pragma unroll 1
            for (int i = 0; i < 4096; ++i) { if ((int)(now32() - td) >= 0) break; __builtin_amdgcn_s_sleep(1); }
        }
        const v2u st = __builtin_amdgcn_raw_buffer_load_b64(r_ws, (l & 7) * 8, WS_TS + (bid >> 2) * 128, AUX_SC1);
        int d = st.y == tag ? (int)((u32)tstart - st.x) : 0;
        d = d < 0 ? 0 : d > DMAX ? DMAX : d;
        u32 e = d * 16;
        e = umx(e, dppu<DPP_X1>(e)); e = umx(e, dppu<DPP_X2>(e)); e = umx(e, dppu<DPP_HMIR>(e));
        *lp<u32>(F_DL + (l & 1) * 4) = e;
    }
    {
        const int qso = WS_Q + g * GQ + b * 8192 + w * 2048;
        const u32 qw = pin(L_QI + w * 320 + (l & 31) * 8 + (l >> 5) * 4);
        {
            const u32 tq = __builtin_amdgcn_readfirstlane((u32)tstart) + TQ;
#pragma unroll 1
            for (int i = 0; i < 4096; ++i) { if ((int)(now32() - tq) >= 0) break; __builtin_amdgcn_s_sleep(1); }
        }
        STAMP(3);
        v4u p[2], c[2];
        bool bad = true;
#pragma unroll 1
        for (int spin = 0; spin < SPIN_MAX; ++spin) {
            asm volatile("" ::: "memory");
            if (bad) {
#pragma unroll
                for (int k = 0; k < 2; ++k) p[k] = LD128(r_ws, (XP & 1) ? OOB : l * 16 + k * 1024, qso, AUX_SC1);
            }
            c[0] = p[0] ^ tb; c[1] = p[1] ^ tb;
            const v4u o = c[0] | c[1];
            bad = (((o.x | o.y) | (o.z | o.w)) & 255u) != 0 && !(XP & 17);
            if (all_lanes(!bad)) break;
        }
        if (CSW) asm volatile("buffer_load_dword %0, %1, %2, %3 offen sc1" : "={v144}"(rdy) : "v"(rvo), "s"(r_ws), "s"(rso) : "memory");
        const v4f qs = __builtin_bit_cast(v4f, c[0]) + __builtin_bit_cast(v4f, c[1]);
        const v2f q0 = swap32(qs.x, qs.z), q1 = swap32(qs.y, qs.w);
        *lp<u32>(qw) = pkbf(q0.x + q0.y, q1.x + q1.y);
        set_flag(F_Q + w * 4, 1);
        if (ORST == 3) __builtin_amdgcn_raw_buffer_store_b32(~0u, r_ws, kg * 512 + n16 * 4, WS_O + g * GO + w * 8192 + b * 2048 + s * 64, AUX_SC1);
    }
    STAMP(4);
    {
        const u32 qa = pin(L_QI + (n16 >> 2) * 320 + kg * 16);
        const u32 ma = pin(n16 == 0 ? L_M + kg * 16 + w * 4 : L_JUNK), mr = pin(L_M + kg * 16);
        const u32 pw = pin(L_P + w * 512 + (n16 >> 2) * 64 + kg * 16 + (n16 & 3) * 2);
        const u32 k0 = pin(L_K + w * 8192 + n16 * 128 + ((kg ^ (n16 & 6)) * 16)), k1 = pin(k0 ^ 64), k2 = pin(k0 + L_H), k3 = pin(k1 + L_H);
        wait_flag<2, 1>(F_L, 4 * w + 4 + (ALN || PRG ? 0 : (WIN - 2) / 2));
        STAMP(24);
        v4u kf[4][4];
#pragma unroll
        for (int t = 0; t < 4; ++t)
#pragma unroll
            for (int u = 0; u < 4; ++u) kf[t][u] = *lp<v4u>((t == 0 ? k0 : t == 1 ? k1 : t == 2 ? k2 : k3) + u * 2048);
        SB();
        v4u qf[4];
        if (PRE) wait_flag<4, PRE>(F_Q, 1);
#pragma unroll 1
        for (int spin = 0; spin < SPIN_MAX; ++spin) {
            const v4u f = *lp<volatile v4u>(F_Q);
            asm volatile("" ::: "memory");
#pragma unroll
            for (int t = 0; t < 4; ++t) qf[t] = *lp<v4u>(qa + t * 64);
            SB();
            if (uni_ge(umn4(f), 1)) break;
        }
        STAMP(18);
        v4f acc[4];
#pragma unroll
        for (int u = 0; u < 4; ++u) acc[u] = (v4f){0.f, 0.f, 0.f, 0.f};
#pragma unroll
        for (int t = 0; t < 4; ++t)
#pragma unroll
            for (int u = 0; u < 4; ++u) acc[u] = mfma16(qf[t], kf[t][u], acc[u]);
        const v2f scale = {0.08838834764831845f, 0.08838834764831845f};
        const v2f s0 = bf2(pk2(bf2(pkbf(acc[0].x, acc[1].x)) * scale)), s1 = bf2(pk2(bf2(pkbf(acc[2].x, acc[3].x)) * scale));
        float m = fmx(fmx(s0.x, s0.y), fmx(s1.x, s1.y));
        m = fmx(m, dppf<DPP_X1>(m)); m = fmx(m, dppf<DPP_X2>(m)); m = fmx(m, dppf<DPP_HMIR>(m)); m = fmx(m, dppf<DPP_ROR8>(m));
        *lp<float>(ma) = m;
        set_flag(F_M + w * 4, 1);
        v4f mv;
        if (PRE) wait_flag<4, PRE>(F_M, 1);
#pragma unroll 1
        for (int spin = 0; spin < SPIN_MAX; ++spin) {
            const v4u f = *lp<volatile v4u>(F_M);
            asm volatile("" ::: "memory");
            mv = *lp<v4f>(mr);
            SB();
            if (uni_ge(umn4(f), 1)) break;
        }
        STAMP(19);
        const float mo = -fmx(fmx(mv.x, mv.y), fmx(mv.z, mv.w)) * LOG2E;
        const v2f e0 = {__builtin_amdgcn_exp2f(__builtin_fmaf(s0.x, LOG2E, mo)), __builtin_amdgcn_exp2f(__builtin_fmaf(s0.y, LOG2E, mo))};
        const v2f e1 = {__builtin_amdgcn_exp2f(__builtin_fmaf(s1.x, LOG2E, mo)), __builtin_amdgcn_exp2f(__builtin_fmaf(s1.y, LOG2E, mo))};
        const u32 p0 = pk2(e0), p1 = pk2(e1);
        *lp<u16>(pw) = (u16)p0; *lp<u16>(pw + 8) = (u16)(p0 >> 16); *lp<u16>(pw + 256) = (u16)p1; *lp<u16>(pw + 264) = (u16)(p1 >> 16);
        const v2f es = e0 + e1;
        *lp<float>(ma + (L_LS - L_M)) = row_sum(es.x + es.y);
        set_flag(F_P + w * 4, 1);
    }
    STAMP(5);
    __builtin_amdgcn_s_setprio(3);
    int aline = WS_A + w * GA + aoff;
    asm volatile("" : "+s"(aline));
    const int mvo = pin(((2 * kg + l) & 7) * 16);
    const int mso = WS_ML + gc * GML + opc + b * 512 + w * 128;
    const int ovo = pin((n16 >> 1) * 128 + kg * 32 + (n16 & 1) * 16);
    const int oso = WS_O + gc * GO + opc + b * OH + w * 2048;
    const int avs = pin((l & (AST16 ? 7 : 1)) == 0 ? l * 2 : OOB);
    u32 rd2;
    if (w == 0) {
        wait_flag<4, 0>(F_P, 1);
        const v4f m = *lp<v4f>(L_M + (l & 3) * 16), q = *lp<v4f>(L_LS + (l & 3) * 16);
        const v4u gm = {f2u(fmx(fmx(m.x, m.y), fmx(m.z, m.w)) * LOG2E), tag, f2u((q.x + q.y) + (q.z + q.w)), tag};
        ST128(gm, r_ws, l < 4 ? l * 512 : OOB, WS_ML + g * GML + opg + b * 128 + s * 16, AUX_SC1);
        if (CSW) asm volatile("s_waitcnt vmcnt(1)\n v_xor_b32 %0, %1, %2" : "=&v"(rd2) : "{v144}"(rdy), "v"(tag));
    } else if (CSW) asm volatile("s_waitcnt vmcnt(0)\n v_xor_b32 %0, %1, %2" : "=&v"(rd2) : "{v144}"(rdy), "v"(tag));
    v4u gm, av[2];
    v8u wf[2][4];
    v4f ac[4];
    float Wa, Wb;
#define DP(c) " " c " row_mask:0xf bank_mask:0xf\n s_nop 1\n"
#define WGT "v_max_f32_dpp %[wa], v136, v136 quad_perm:[1,0,3,2] row_mask:0xf bank_mask:0xf\n v_cmp_ne_u32 vcc, v137, %[tag]\n v_cmp_ne_u32 %[bad], v139, %[tag]\n" \
            "v_max_f32_dpp %[wa], %[wa], %[wa]" DP("quad_perm:[2,3,0,1]") "v_max_f32_dpp %[wa], %[wa], %[wa] row_half_mirror row_mask:0xf bank_mask:0xf\n" \
            "v_sub_f32 %[wa], v136, %[wa]\n v_exp_f32 v137, %[wa]\n s_nop 0\n v_mul_f32 %[wa], v137, v138\n s_nop 1\n" \
            "v_add_f32_dpp %[wa], %[wa], %[wa]" DP("quad_perm:[1,0,3,2]") "v_add_f32_dpp %[wa], %[wa], %[wa]" DP("quad_perm:[2,3,0,1]") \
            "v_add_f32_dpp %[wa], %[wa], %[wa] row_half_mirror row_mask:0xf bank_mask:0xf\n" \
            "v_rcp_f32 %[wb], %[wa]\n s_or_b64 %[bad], %[bad], vcc\n" \
            "v_mul_f32_dpp %[wa], v137, %[wb] row_newbcast:0 row_mask:0xf bank_mask:0xf\n" \
            "v_mul_f32_dpp %[wb], v137, %[wb] row_newbcast:1 row_mask:0xf bank_mask:0xf\n"
#define GMLD "buffer_load_dwordx4 v[136:139], %[mvo], %[rs], %[mso] offen sc1\n"
    {
        u32 ms2 = mso;
        asm volatile("" : "+s"(ms2));
#define OREG(c) c "{v[128:131]}"(av[0]), c "{v[132:135]}"(av[1])
#define OIN OREG("")
#define OCHK "v_max3_u32 %[t0], v128, v129, v130\n v_max3_u32 %[t1], v132, v133, v134\n v_max_u32 %[t0], %[t0], v131\n v_max_u32 %[t1], %[t1], v135\n" \
             "v_cmp_eq_u32 vcc, -1, %[t0]\n v_cmp_eq_u32 %[m1], -1, %[t1]\n"
#define OL0 "buffer_load_dwordx4 v[128:131], %[vo], %[rs], %[so] offen sc1\n"
#define OL1 "buffer_load_dwordx4 v[132:135], %[vo], %[rs], %[so] offen offset:1024 sc1\n"
#define OLD  "s_mov_b64 exec, vcc\n" OL0 "s_mov_b64 exec, %[m1]\n" OL1 "s_mov_b64 exec, -1\n"
        av[0] = av[1] = (v4u){~0u, ~0u, ~0u, ~0u};
#define CREG(c) c "{v[152:155]}"(ac[0]), c "{v[156:159]}"(ac[1]), c "{v[160:163]}"(ac[2]), c "{v[164:167]}"(ac[3])
        ac[0] = ac[1] = ac[2] = ac[3] = (v4f){0.f, 0.f, 0.f, 0.f};
        u32 idx = (l & 1) ? 0xEEEEu : 0x4444u, top = ~1u;
        asm volatile("" : OREG("+"), CREG("+"), "+v"(idx), "+v"(top) : "s"(oso));
        const u32 t3 = __builtin_amdgcn_readfirstlane((u32)tstart - (ALN ? *lp<volatile u32>(F_DL) >> 4 : 0)) + T3F;
        if (CSW) {
#pragma unroll 1
            for (int spin = 0; spin < SPIN_MAX; ++spin) {
                if (all_lanes(rd2 == 0)) break;
                asm volatile("buffer_load_dword %0, %1, %2, %3 offen sc1\n s_waitcnt vmcnt(0)\n v_xor_b32 %0, %0, %4" : "=&v"(rd2) : "v"(rvo), "s"(r_ws), "s"(rso), "v"(tag) : "memory");
            }
        }
#pragma unroll 1
        for (int i = 0; i < 4096; ++i) { if ((int)(now32() - (t3 - TM)) >= 0) break; __builtin_amdgcn_s_sleep(1); }
        unsigned long long bad;
        asm volatile(GMLD : "=&{v[136:139]}"(gm) : [mvo] "v"(mvo), [rs] "s"(r_ws), [mso] "s"(ms2) : "memory");
        bad = 1; Wa = Wb = 0.f;
        wait_flag<1, 1>(F_ARR, 36);
        STAMP(6);
        u32 t0, t1, n = SPIN_MAX, sf = 0;
        unsigned long long m1, m2, om = ~0ull;
        const u32 fla = pin(WT ? F_W : F_L);
        v2u wl[4][4], wh[4][4];
#define WAITP(v, slp) do { v2u f_; asm volatile("s_cmp_ge_u32 %[sf], " #v "\n s_cbranch_scc1 3f\n1:\n ds_read_b64 %[f], %[fa]\n s_cmp_eq_u64 %[m], 0\n s_cbranch_scc1 2f\n" \
                     "s_memrealtime s[36:37]\n s_waitcnt lgkmcnt(0)\n s_sub_i32 s36, s36, %[t3]\n s_cmp_lt_i32 s36, 0\n s_cbranch_scc1 2f\n s_mov_b64 %[m], 0\n" OL0 OL1 \
                     "2:\n s_waitcnt lgkmcnt(0)\n v_min_u32 v140, v140, v141\n s_sub_u32 %[n], %[n], 1\n s_cbranch_scc1 3f\n" \
                     "v_readfirstlane_b32 %[sf], v140\n" slp " s_cmp_lt_u32 %[sf], " #v "\n s_cbranch_scc1 1b\n3:" \
                     : OREG("+"), [m] "+s"(om), [n] "+s"(n), [sf] "+s"(sf), [f] "=&{v[140:141]}"(f_) : [vo] "v"(ovo), [rs] "s"(r_ws), [so] "s"(oso), [t3] "s"(t3), [fa] "v"(fla) : "memory", "s36", "s37", "scc"); } while (0)
#undef WAITP
#define WAITP(v, slp) do { u32 f_; asm volatile("s_bitcmp1_b32 %[sf], 8*(" #v "-34)\n s_cbranch_scc1 3f\n1:\n ds_read_b32 %[f], %[fa]\n s_cmp_eq_u64 %[m], 0\n s_cbranch_scc1 2f\n" \
                     "s_memrealtime s[36:37]\n s_waitcnt lgkmcnt(0)\n s_sub_i32 s36, s36, %[t3]\n s_cmp_lt_i32 s36, 0\n s_cbranch_scc1 2f\n s_mov_b64 %[m], 0\n" OL0 OL1 \
                     "2:\n s_waitcnt lgkmcnt(0)\n v_readfirstlane_b32 %[sf], %[f]\n s_sub_u32 %[n], %[n], 1\n s_cbranch_scc1 3f\n" \
                     slp " s_bitcmp0_b32 %[sf], 8*(" #v "-34)\n s_cbranch_scc1 1b\n3:" \
                     : OREG("+"), [m] "+s"(om), [n] "+s"(n), [sf] "+s"(sf), [f] "=&v"(f_) : [vo] "v"(ovo), [rs] "s"(r_ws), [so] "s"(oso), [t3] "s"(t3), [fa] "v"(fla) : "memory", "s36", "s37", "scc"); } while (0)
#define TR(a) __builtin_bit_cast(v2u, __builtin_amdgcn_ds_read_tr16_b64_v4i16(lp<v4s>(a)))
#define RP(d, k, o) do { _Pragma("unroll") for (int c = 0; c < 4; ++c) { d[2 * k][c] = TR(wa[c] + k * 8192 + o); d[2 * k + 1][c] = TR(wa[c] + k * 8192 + 4096 + o); } SB(); } while (0)
        WAITP(34, "s_sleep 1\n"); WST(18); RP(wl, 0, 0);
        WAITP(35, ""); WST(19); RP(wh, 0, 2048);
        WAITP(36, ""); WST(24); RP(wl, 1, 0);
        WAITP(37, ""); WST(4);
        STAMP(22);
        RP(wh, 1, 2048);
        asm volatile("s_cmp_eq_u64 %[bad], 0\n s_cbranch_scc1 3f\n s_cmp_eq_u64 %[m], 0\n s_cbranch_scc1 2f\n1:\n s_waitcnt vmcnt(0)\n2:\n s_waitcnt vmcnt(2)\n" WGT "s_cbranch_scc0 3f\n" GMLD
                     "s_sub_u32 %[n], %[n], 1\n s_cbranch_scc0 1b\n3:"
                     : "+{v[136:139]}"(gm), [wa] "+v"(Wa), [wb] "+v"(Wb), [bad] "+s"(bad), [n] "+s"(n) : [mvo] "v"(mvo), [rs] "s"(r_ws), [mso] "s"(ms2), [tag] "v"(tag), [m] "s"(om) : "memory", "vcc", "scc");
#pragma unroll
        for (int k = 0; k < 2; ++k)
#pragma unroll
            for (int c = 0; c < 4; ++c) wf[k][c] = (v8u){wl[2 * k][c].x, wl[2 * k][c].y, wh[2 * k][c].x, wh[2 * k][c].y, wl[2 * k + 1][c].x, wl[2 * k + 1][c].y, wh[2 * k + 1][c].x, wh[2 * k + 1][c].y};
        STAMP(11);
        if (om) {
#pragma unroll 1
            for (int i = 0; i < 4096; ++i) { if ((int)(now32() - t3) >= 0) break; __builtin_amdgcn_s_sleep(1); }
        }
        STAMP(12);
#define SM(d, a, f) "v_smfmac_f32_16x16x64_bf16 v[" d "], v[" a "], %[" f "], %[idx]\n"
#define FM2(d, r0, r1) "v_mul_f32 v" #d ", %[wa], v" #r0 "\n v_fmac_f32 v" #d ", %[wa], v" #r1 "\n"
#define FA2(d, r0, r1) "v_fmac_f32 v" #d ", %[wb], v" #r0 "\n v_fmac_f32 v" #d ", %[wb], v" #r1 "\n"
#define Z2(r) "v_mov_b64 v[" r "], 0\n"
        asm volatile(
            "1:\n"
            "s_waitcnt vmcnt(0)\n"
            SM("152:155", "128:131", "a0")
            "v_max3_u32 %[t0], v128, v129, v130\n v_max3_u32 %[t1], v132, v133, v134\n"
            SM("152:155", "132:135", "b0")
            "v_max_u32 %[t0], %[t0], v131\n v_max_u32 %[t1], %[t1], v135\n"
            SM("156:159", "128:131", "a1")
            "v_cmp_eq_u32 vcc, -1, %[t0]\n v_cmp_eq_u32 %[m1], -1, %[t1]\n"
            SM("156:159", "132:135", "b1")
            "s_or_b64 %[m2], vcc, %[m1]\n"
            "s_cbranch_scc1 2f\n"
            SM("160:163", "128:131", "a2")
            FM2(137, 152, 153)
            SM("160:163", "132:135", "b2")
            FA2(137, 154, 155)
            SM("164:167", "128:131", "a3")
            FM2(141, 156, 157)
            SM("164:167", "132:135", "b3")
            FA2(141, 158, 159)
            FM2(145, 160, 161) FA2(145, 162, 163)
            "v_permlane16_swap_b32 v137, v141\n v_add_f32 v137, v137, v141\n"
            FM2(149, 164, 165) FA2(149, 166, 167)
            "s_nop 1\n"
            "v_permlane16_swap_b32 v145, v149\n v_add_f32 v145, v145, v149\n"
            "s_nop 1\n"
            "v_permlane32_swap_b32 v137, v145\n v_add_f32 v137, v137, v145\n"
            "s_nop 1\n"
            "v_mov_b32_dpp v138, v137 quad_perm:[1,0,3,2] row_mask:0xf bank_mask:0xf bound_ctrl:1\n"
            "v_cvt_pk_bf16_f32 v137, v137, v138\n"
            "v_min_u32 v140, v137, %[top]\n"
            "s_nop 0\n"
            "v_min_u32_dpp v141, v137, %[top] row_shl:2 row_mask:0xf bank_mask:0xf bound_ctrl:1\n"
            "v_min_u32_dpp v142, v137, %[top] row_shl:4 row_mask:0xf bank_mask:0xf bound_ctrl:1\n"
            "v_min_u32_dpp v143, v137, %[top] row_shl:6 row_mask:0xf bank_mask:0xf bound_ctrl:1\n"
            "buffer_store_dwordx4 v[140:143], %[avs], %[rs], %[al] offen sc1\n"
            "s_branch 3f\n"
            "2:\n"
            OLD
            "s_nop 2\n"
            Z2("152:153") Z2("154:155") Z2("156:157") Z2("158:159")
            "s_sub_u32 %[n], %[n], 1\n"
            "s_cbranch_scc0 1b\n"
            "3:\n"
            "s_nop 0\n"
            : OREG("+"), CREG("+"), [t0] "=&v"(t0), [t1] "=&v"(t1), [m1] "=&s"(m1), [m2] "=&s"(m2), [n] "+s"(n)
            : [a0] "v"(wf[0][0]), [a1] "v"(wf[0][1]), [a2] "v"(wf[0][2]), [a3] "v"(wf[0][3]), [b0] "v"(wf[1][0]), [b1] "v"(wf[1][1]), [b2] "v"(wf[1][2]), [b3] "v"(wf[1][3]),
              [wa] "v"(Wa), [wb] "v"(Wb), [idx] "v"(idx), [top] "v"(top), [vo] "v"(ovo), [so] "s"(oso), [avs] "v"(avs), [rs] "s"(r_ws), [al] "s"(aline)
            : "memory", "vcc", "scc", "v137", "v138", "v140", "v141", "v142", "v143", "v145", "v149");
        static_assert(AST16 && OCAP == 0, "");
    }
    asm volatile("" :: OIN);
    set_flag(F_A + w * 4, 3);
    STAMP(13);
}

extern "C" void kernel_launch(void* const* d_in, const int* in_sizes, int n_in, void* d_out, int out_size, void* d_ws, size_t ws_size, hipStream_t stream) {
    constexpr u32 F = 0x00020000;
    fused_decode_kernel<<<256, 640, 0, stream>>>((const u16*)d_in[0], 4096u, F, (const u16*)d_in[3], 4u << 20, F, (const u16*)d_in[1], 64u << 20, F,
                                                 (const u16*)d_in[2], 64u << 20, F, (unsigned char*)d_ws + ((size_t)WSB << 20), WS_END, F, (const u16*)d_in[6], 3584u, F,
                                                 (const u16*)d_in[7], 3584u, F, (const u16*)d_in[8], 3584u, F, (const u16*)d_in[9], 2u, F,
                                                 (const u16*)d_in[4], (u16*)d_out);
}
```
